```python
import torch

def _softmax(x, axis=-1):
    return torch.softmax(x, dim=axis)
def _gelu(x):
    return torch.nn.functional.gelu(x, approximate="tanh")
def _silu(x):
    return torch.nn.functional.silu(x)
def _sigmoid(x):
    return torch.sigmoid(x)
def _layernorm(x, axis=-1, eps=1e-5):
    m = x.mean(dim=axis, keepdim=True)
    v = x.var(dim=axis, keepdim=True, unbiased=False)
    return (x - m) / torch.sqrt(v + eps)
def _scatter_add(tgt, idx, src, axis=0):
    return tgt.clone().index_add_(axis, idx, src)
def _segment_sum(src, seg, n):
    out = torch.zeros((n,) + src.shape[1:], dtype=src.dtype)
    return out.index_add_(0, seg, src)
def _pad(x, pw):
    flat = tuple(v for pair in reversed(pw) for v in pair)
    return torch.nn.functional.pad(x, flat)

kernel_name = 'sequential_preLN__ssm_conv_s4__glu_gated_reglu__prompt'

def setup_inputs():

    _w = (lambda a: (lambda t: t.to(torch.bfloat16) if t.is_floating_point() else t)(torch.as_tensor(a)))
    def _A_setup():
        B, L, D, N = 1, 2048, 448, 256
        A_re = -(lambda _x: torch.exp(torch.as_tensor(_x)))((lambda *s: torch.randn(*s, dtype=torch.bfloat16) * 3.2)(D, N))
        A_im = torch.pi * (lambda *s: torch.randn(*s, dtype=torch.bfloat16) * 0.05)(D, N)
        B_re = (lambda *s: torch.randn(*s, dtype=torch.bfloat16) * 0.05)(D, N)
        B_im = (lambda *s: torch.randn(*s, dtype=torch.bfloat16) * 0.05)(D, N)
        C_re = (lambda *s: torch.randn(*s, dtype=torch.bfloat16) * 0.05)(D, N)
        C_im = (lambda *s: torch.randn(*s, dtype=torch.bfloat16) * 0.05)(D, N)
        log_dt = (lambda *s: torch.randn(*s, dtype=torch.bfloat16) * 0.05)(D) * 0.5 - 4.0
        Dskip = (lambda *s: torch.randn(*s, dtype=torch.bfloat16) * 0.05)(D)
        return {
            "u": _w((lambda *s: torch.randn(*s, dtype=torch.bfloat16) * 0.05)(B, L, D)),
            "A_re": _w(A_re),
            "A_im": _w(A_im),
            "B_re": _w(B_re),
            "B_im": _w(B_im),
            "C_re": _w(C_re),
            "C_im": _w(C_im),
            "log_dt": _w(log_dt),
            "Dskip": _w(Dskip),
        }
    _A_in = _A_setup()

    def _B_setup():
        B, S, D, F = 1, 2048, 448, 1792
        return {
            "x": _w((lambda *s: torch.randn(*s, dtype=torch.bfloat16) * 0.05)(B, S, D)),
            "W_gate": _w((lambda *s: torch.randn(*s, dtype=torch.bfloat16) * 0.4)(D, F) * (2.0 / D) ** 0.5),
            "W_up": _w((lambda *s: torch.randn(*s, dtype=torch.bfloat16) * 0.4)(D, F) * (2.0 / D) ** 0.5),
            "W_down": _w((lambda *s: torch.randn(*s, dtype=torch.bfloat16) * 0.4)(F, D) * (1.0 / F) ** 0.5),
        }
    _B_in = _B_setup()

    return {
        "A_u": _A_in["u"],
        "A_A_re": _A_in["A_re"],
        "A_A_im": _A_in["A_im"],
        "A_B_re": _A_in["B_re"],
        "A_B_im": _A_in["B_im"],
        "A_C_re": _A_in["C_re"],
        "A_C_im": _A_in["C_im"],
        "A_log_dt": _A_in["log_dt"],
        "A_Dskip": _A_in["Dskip"],
        "B_x": _B_in["x"],
        "B_W_gate": _B_in["W_gate"],
        "B_W_up": _B_in["W_up"],
        "B_W_down": _B_in["W_down"],
    }

def reference(A_u, A_A_re, A_A_im, A_B_re, A_B_im, A_C_re, A_C_im, A_log_dt, A_Dskip, B_x, B_W_gate, B_W_up, B_W_down):
    def _A_ref(u, A_re, A_im, B_re, B_im, C_re, C_im, log_dt, Dskip):
        Bsz, L, D = u.shape
        dt = torch.exp(log_dt)[:, None]
        A = A_re + 1j * A_im
        Bc = B_re + 1j * B_im
        Cc = C_re + 1j * C_im
        logdA = dt * A
        dB = dt * Bc
        l = (lambda x, dt: x.to(dt))(torch.arange(L), A_re.dtype)
        P = torch.exp(logdA[:, :, None] * l[None, None, :])
        K = torch.einsum("dn,dnl,dn->dl", Cc, P, dB).real
        L2 = 2 * L
        Kp = _pad(K, ((0, 0), (0, L)))
        up = (lambda x, *d: x.permute(*d))(u, 0, 2, 1)
        up = _pad(up, ((0, 0), (0, 0), (0, L)))
        Kf = (lambda x, axes=(-2,-1): torch.fft.rfft2(x.float(), dim=axes))(Kp[:, None, :], axes=(-2, -1))
        Uf = (lambda x, axes=(-2,-1): torch.fft.rfft2(x.float(), dim=axes))(up[:, :, None, :], axes=(-2, -1))
        Yf = Uf * Kf[None, :, :, :]
        y = (lambda x, s, axes=(-2,-1): torch.fft.irfft2(x, s=s, dim=axes).to(torch.bfloat16))(Yf, (1, L2), axes=(-2, -1))[:, :, 0, :L]
        y = (lambda x, *d: x.permute(*d))(y, 0, 2, 1) + u * Dskip[None, None, :]
        return y

    def _B_ref(x, W_gate, W_up, W_down):
        gate = torch.einsum("bsd,df->bsf", x, W_gate)
        up = torch.einsum("bsd,df->bsf", x, W_up)
        gate = gate * (lambda x, dt: x.to(dt))(gate > 0, gate.dtype)
        h = gate * up
        return torch.einsum("bsf,fd->bsd", h, W_down)

    x = A_u
    h = x + _A_ref(_layernorm(x, axis=-1), A_A_re, A_A_im, A_B_re, A_B_im, A_C_re, A_C_im, A_log_dt, A_Dskip)
    h2 = h + _B_ref(_layernorm(h, axis=-1), B_W_gate, B_W_up, B_W_down)
    return h2
```

```cpp
#include <hip/hip_runtime.h>
#include <hip/hip_bf16.h>
#include <stdint.h>

typedef unsigned short u16;
typedef unsigned int u32;
typedef unsigned long long u64;
typedef short s16x4 __attribute__((ext_vector_type(4)));
typedef short s16x8 __attribute__((ext_vector_type(8)));
typedef _Float16 h16x8 __attribute__((ext_vector_type(8)));
typedef _Float16 h16x2 __attribute__((ext_vector_type(2)));
typedef __bf16 bf16x2 __attribute__((ext_vector_type(2)));
typedef float f32x4 __attribute__((ext_vector_type(4)));
typedef float f32x16 __attribute__((ext_vector_type(16)));
typedef u32 u32x2 __attribute__((ext_vector_type(2)));
typedef u32 u32x4 __attribute__((ext_vector_type(4)));
#define LDS3 __attribute__((address_space(3)))
#define GAS __attribute__((address_space(1)))
#define DI __device__ __forceinline__

constexpr int L = 2048, D = 448, NM = 256, F = 1792;

constexpr size_t WS_GRAN = 0;
constexpr size_t WS_EPOCH = 16384;
constexpr size_t WS_CS = 32768;
constexpr size_t WS_HT = 65536;
constexpr size_t WS_WD = WS_HT + (size_t)D * L * 2;
constexpr size_t WS_HF = WS_WD + (size_t)D * F * 2;

DI float bf_lo(u32 w) { return __builtin_bit_cast(float, w << 16); }
DI float bf_hi(u32 w) { return __builtin_bit_cast(float, w & 0xffff0000u); }
DI float bf1(u16 w) { return __builtin_bit_cast(float, (u32)w << 16); }
DI float rbf(float a) { return (float)(__bf16)a; }
DI u32 pack_bf(float a, float b) { bf16x2 v; v.x = (__bf16)a; v.y = (__bf16)b; return __builtin_bit_cast(u32, v); }
DI u32 pack_h(float a, float b) { h16x2 v; v.x = (_Float16)a; v.y = (_Float16)b; return __builtin_bit_cast(u32, v); }
DI float wave_sum(float v) {
#pragma unroll
  for (int o = 32; o > 0; o >>= 1) v += __shfl_xor(v, o);
  return v;
}
DI float wave_max(float v) {
#pragma unroll
  for (int o = 32; o > 0; o >>= 1) v = fmaxf(v, __shfl_xor(v, o));
  return v;
}
struct cf { float r, i; };
DI cf cmul(cf a, cf b) { cf o; o.r = a.r * b.r - a.i * b.i; o.i = a.r * b.i + a.i * b.r; return o; }

DI u32 row_stats(u32x4 v, bool act) {
  float e[8];
#pragma unroll
  for (int i = 0; i < 4; ++i) { e[2 * i] = bf_lo(v[i]); e[2 * i + 1] = bf_hi(v[i]); }
  float s = 0.f;
#pragma unroll
  for (int i = 0; i < 8; ++i) s += e[i];
  s = act ? s : 0.f;
  const float mean = wave_sum(s) * (1.0f / D);
  float q = 0.f;
#pragma unroll
  for (int i = 0; i < 8; ++i) { const float t = e[i] - mean; q += t * t; }
  q = act ? q : 0.f;
  const float var = wave_sum(q) * (1.0f / D);
  const float sd = sqrtf(rbf(rbf(var) + 1e-5f));
  return pack_bf(mean, sd);
}

__global__ __launch_bounds__(512) void k_stats(const u16* __restrict__ x, u64* __restrict__ gran) {
  const int wave = threadIdx.x >> 6, lane = threadIdx.x & 63;
  const int row = blockIdx.x * 8 + wave;
  const bool act = lane < 56;
  u32x4 v = {0u, 0u, 0u, 0u};
  if (act) v = *(const u32x4*)(x + row * D + lane * 8);
  const u32 st = row_stats(v, act);
  if (lane == 0) gran[row] = (u64)st;
}

constexpr int KG_ROW = 272;
constexpr int KG_WAVE = 53 * KG_ROW;
constexpr int KC_STRIDE = 4160;
constexpr int KC_CH = 8 * KC_STRIDE;
constexpr int SL_RED = 2 * KC_CH;
constexpr int SL_U = 8 * KG_WAVE;
constexpr int U_CH = (256 + L) * 2;
constexpr int SL_X = SL_U + 2 * U_CH;
constexpr int SL_T = SL_X + 2 * L * 2;
constexpr int T_CH = 656 * 2;
constexpr int SL_TOTAL = SL_T + 2 * T_CH;
static_assert(SL_RED + 8 * 2048 <= SL_U, "lds map");

#define MFMA16(a, b, c) __builtin_amdgcn_mfma_f32_16x16x32_bf16((a), (b), (c), 0, 0, 0)

template <int TG> DI constexpr int conv_tile(int i) { return TG == 0 ? (i == 0 ? 7 : i == 1 ? 5 : i == 2 ? 2 : 0) : (i == 0 ? 6 : i == 1 ? 4 : i == 2 ? 3 : 1); }

template <int TG>
DI void conv_body(const char* ka, const char* ub, int r0, f32x4 (&acc)[4]) {
  constexpr int NP = TG == 0 ? 8 : 7;
#pragma unroll
  for (int rr = 0; rr < 4; ++rr) {
    const int r = r0 + 2 * rr;
    s16x8 A[NP], B[NP];
#pragma unroll
    for (int p = 0; p < NP; ++p) {
      A[p] = *(const s16x8*)(ka - 32 * r - 512 * p);
      B[p] = *(const s16x8*)(ub - 32 * r + 512 * p);
    }
#pragma unroll
    for (int i = 0; i < 4; ++i) {
      const int t = conv_tile<TG>(i);
#pragma unroll
      for (int p = 0; p <= t; ++p) acc[i] = MFMA16(A[p], B[t - p], acc[i]);
    }
  }
}

__global__ __launch_bounds__(512, 2) void k_ssm(
    const u16* __restrict__ x, const u16* __restrict__ A_re, const u16* __restrict__ A_im,
    const u16* __restrict__ B_re, const u16* __restrict__ B_im, const u16* __restrict__ C_re,
    const u16* __restrict__ C_im, const u16* __restrict__ log_dt, const u16* __restrict__ Dskip,
    const u16* __restrict__ Wg, const u16* __restrict__ Wu, const u16* __restrict__ Wd,
    char* __restrict__ ws) {
  __shared__ __attribute__((aligned(16))) char lds[SL_TOTAL];
  const int tid = threadIdx.x, lane = tid & 63;
  const int wave = __builtin_amdgcn_readfirstlane(tid >> 6);
  const int xcd = blockIdx.x & 7, idx = blockIdx.x >> 3;

  if (xcd == 7) {
    {
      const int slice = idx >> 1, part = idx & 1;
      const u16* W = part ? Wu : Wg;
      float* red = (float*)lds;
      const int rg = tid / 14, cc = tid - rg * 14;
      u16 wv[7][8];
#pragma unroll
      for (int i = 0; i < 7; ++i) {
        const int fr = idx * 49 + wave + 8 * i;
        const int frc = (wave + 8 * i < 49) ? fr : idx * 49;
        const int dt = frc / 112, ks = frc - dt * 112;
#pragma unroll
        for (int j = 0; j < 8; ++j) {
          const int f = 16 * ks + 8 * (j >> 2) + 4 * (lane >> 5) + (j & 3);
          wv[i][j] = Wd[f * D + 32 * dt + (lane & 31)];
        }
      }
      if (tid < 448) {
        float s[8];
#pragma unroll
        for (int j = 0; j < 8; ++j) s[j] = 0.f;
#pragma unroll
        for (int i = 0; i < 14; ++i) {
          const u32x4 v = *(const u32x4*)(W + (rg + 32 * i) * F + slice * 112 + cc * 8);
#pragma unroll
          for (int j = 0; j < 4; ++j) { s[2 * j] += bf_lo(v[j]); s[2 * j + 1] += bf_hi(v[j]); }
        }
#pragma unroll
        for (int j = 0; j < 8; ++j) red[rg * 112 + cc * 8 + j] = s[j];
      }
#pragma unroll
      for (int i = 0; i < 7; ++i) {
        if (wave + 8 * i < 49) {
          u32x4 o;
#pragma unroll
          for (int j = 0; j < 4; ++j) o[j] = (u32)wv[i][2 * j] | ((u32)wv[i][2 * j + 1] << 16);
          *(u32x4*)(ws + WS_WD + (size_t)(idx * 49 + wave + 8 * i) * 1024 + lane * 16) = o;
        }
      }
      __syncthreads();
      if (tid < 112) {
        float s = 0.f;
#pragma unroll
        for (int i = 0; i < 32; ++i) s += red[i * 112 + tid];
        ((float*)(ws + WS_CS))[slice * 224 + (tid >> 4) * 32 + part * 16 + (tid & 15)] = s;
      }
    }
    return;
  }

  const int cp = xcd * 32 + idx;
  const int ch = wave >> 2, wq = wave & 3;
  const int d = 2 * cp + ch;

  const int pi = d * NM + wq * 64 + lane;
  const u16 v_are = A_re[pi], v_aim = A_im[pi], v_bre = B_re[pi], v_bim = B_im[pi], v_cre = C_re[pi], v_cim = C_im[pi];
  const u16 v_ldt = log_dt[d], v_dsk = Dskip[d];
  u32 xs[4];
#pragma unroll
  for (int i = 0; i < 4; ++i) xs[i] = *(const u32*)(x + (4 * tid + i) * D + 2 * cp);

  char* kg = lds + wave * KG_WAVE;
  float inv_sc;
  {
    const float dt = rbf(expf(bf1(v_ldt)));
    const float p2 = (dt * bf1(v_are)) * 1.4426950408889634f;
    const float rv = (dt * bf1(v_aim)) * 0.15915494309189535f;
    const float dbr = dt * bf1(v_bre), dbi = dt * bf1(v_bim);
    const float cr = bf1(v_cre), ci = bf1(v_cim);
    cf cb; cb.r = cr * dbr - ci * dbi; cb.i = cr * dbi + ci * dbr;
    const float mx = wave_max(fmaxf(fabsf(cb.r), fabsf(cb.i)));
    int e = __builtin_amdgcn_frexp_expf(mx);
    e = e < -100 ? -100 : (e > 100 ? 100 : e);
    const float sc = __builtin_amdgcn_ldexpf(1.0f, 10 - e);
    inv_sc = __builtin_amdgcn_ldexpf(1.0f, e - 10);
    cb.r *= sc; cb.i *= sc;
    auto cpow = [&](float m) {
      const float mag = __builtin_amdgcn_exp2f(p2 * m);
      const float t = __builtin_amdgcn_fractf(rv * m);
      cf o; o.r = mag * __builtin_amdgcn_cosf(t); o.i = mag * __builtin_amdgcn_sinf(t); return o;
    };
    const cf z1 = cpow(1.f), w8 = cpow(8.f), w256 = cpow(256.f);
    const cf z2 = cmul(z1, z1), z4 = cmul(z2, z2);
    u32* row = (u32*)(kg + lane * 4);
    auto put = [&](int r, cf v) { row[r * (KG_ROW / 4)] = pack_h(v.r, v.i); };
    cf h = cb, t = cb;
    put(0, h);
#pragma unroll
    for (int k = 1; k < 8; ++k) { t = cmul(t, z1); put(k, t); }
    h = cmul(h, w256); put(8, h); t = h;
#pragma unroll
    for (int k = 1; k < 4; ++k) { t = cmul(t, z2); put(8 + k, t); }
    h = cmul(h, w256); put(12, h); put(13, cmul(h, z4));
    h = cmul(h, w256); put(14, h); put(15, cmul(h, z4));
#pragma unroll
    for (int k = 0; k < 5; ++k) { h = cmul(h, w256); put(16 + k, h); }
    cf c8; c8.r = w8.r; c8.i = -w8.i;
    const cf c16 = cmul(c8, c8);
    cf ea; ea.r = 1.f; ea.i = 0.f;
    cf eb = c8;
    put(21, ea); put(22, eb);
#pragma unroll
    for (int k = 1; k < 16; ++k) { ea = cmul(ea, c16); eb = cmul(eb, c16); put(21 + 2 * k, ea); put(22 + 2 * k, eb); }
  }
  {
    f32x16 acc;
#pragma unroll
    for (int i = 0; i < 16; ++i) acc[i] = 0.f;
    const char* fa = kg + (lane & 31) * KG_ROW + (lane >> 5) * 16;
#pragma unroll
    for (int s = 0; s < 8; ++s) {
      const h16x8 a = *(const h16x8*)(fa + 32 * s);
      const h16x8 b = *(const h16x8*)(fa + 21 * KG_ROW + 32 * s);
      acc = __builtin_amdgcn_mfma_f32_32x32x16_f16(a, b, acc, 0, 0, 0);
    }
    f32x4* pt = (f32x4*)kg;
#pragma unroll
    for (int q = 0; q < 3; ++q) {
      f32x4 o;
#pragma unroll
      for (int j = 0; j < 4; ++j) o[j] = acc[4 * q + j] * inv_sc;
      pt[q * 64 + lane] = o;
    }
  }
  __syncthreads();
  if (wq < 3) {
    f32x4 s = ((const f32x4*)(lds + (ch * 4) * KG_WAVE))[wq * 64 + lane];
#pragma unroll
    for (int w = 1; w < 4; ++w) s += ((const f32x4*)(lds + (ch * 4 + w) * KG_WAVE))[wq * 64 + lane];
    u16* T = (u16*)(lds + SL_T + ch * T_CH);
    const int lo = lane & 31, hh = lane >> 5;
#pragma unroll
    for (int j = 0; j < 4; ++j) {
      int ci;
      if (wq == 0) ci = 8 * lo + 4 * hh + j;
      else if (wq == 1) ci = hh ? 384 + 64 * (j >> 1) + (j & 1) + 2 * lo : 256 + 4 * lo + j;
      else ci = hh ? ((j == 0 && lo == 0) ? 640 : 648) : 512 + 32 * j + lo;
      T[ci] = (u16)pack_bf(s[j], 0.f);
    }
  } else {
    *(u32x2*)(lds + SL_U + ch * U_CH + lane * 8) = u32x2{0u, 0u};
  }
  __syncthreads();
  {
    const u16* T = (const u16*)(lds + SL_T + ch * T_CH);
    const int tc = tid & 255;
    const int i0 = 2047 - 8 * tc;
    u32 e[15];
#pragma unroll
    for (int g = 0; g < 2; ++g) {
      const int top = i0 - 8 * g;
      int s = 24 - __builtin_clz((u32)(top | 128));
      const int bias = (1 << s >> 1) - (s > 0 ? 1 : 0);
      const int msk = s > 0 ? 1 : 0;
#pragma unroll
      for (int j = 0; j < 8; ++j) {
        if (8 * g + j < 15) {
          const int i = top - j;
          const int ci = 128 * s + ((i + bias + ((i >> s) & msk)) >> s);
          const u32 v = T[top < 0 ? 0 : ci];
          e[8 * g + j] = top < 0 ? 0u : v;
        }
      }
    }
    u32 ev[7], od[7];
#pragma unroll
    for (int k = 0; k < 7; ++k) { ev[k] = e[2 * k] | (e[2 * k + 1] << 16); od[k] = e[2 * k + 1] | (e[2 * k + 2] << 16); }
    char* kc = lds + ch * KC_CH + tc * 16;
#pragma unroll
    for (int s = 0; s < 4; ++s) {
      *(u32x4*)(kc + (2 * s) * KC_STRIDE) = u32x4{ev[s], ev[s + 1], ev[s + 2], ev[s + 3]};
      *(u32x4*)(kc + (2 * s + 1) * KC_STRIDE) = u32x4{od[s], od[s + 1], od[s + 2], od[s + 3]};
    }
    if (tc < 8) *(u32x4*)(lds + ch * KC_CH + tc * KC_STRIDE + 256 * 16) = u32x4{0u, 0u, 0u, 0u};
  }
  {
    const u64* gran = (const u64*)(ws + WS_GRAN) + 4 * tid;
    float u0[4], u1[4];
#pragma unroll
    for (int i = 0; i < 4; ++i) {
      const u32 st = (u32)gran[i];
      const float m = bf_lo(st), sd = bf_hi(st);
      u0[i] = rbf(bf_lo(xs[i]) - m) / sd;
      u1[i] = rbf(bf_hi(xs[i]) - m) / sd;
    }
    *(u32x2*)(lds + SL_U + 512 + tid * 8) = u32x2{pack_bf(u0[0], u0[1]), pack_bf(u0[2], u0[3])};
    *(u32x2*)(lds + SL_U + U_CH + 512 + tid * 8) = u32x2{pack_bf(u1[0], u1[1]), pack_bf(u1[2], u1[3])};
    *(u32x2*)(lds + SL_X + tid * 8) = u32x2{(xs[0] & 0xffffu) | (xs[1] << 16), (xs[2] & 0xffffu) | (xs[3] << 16)};
    *(u32x2*)(lds + SL_X + L * 2 + tid * 8) = u32x2{(xs[0] >> 16) | (xs[1] & 0xffff0000u), (xs[2] >> 16) | (xs[3] & 0xffff0000u)};
  }
  __syncthreads();
  const int tg = wq & 1, rh = wq >> 1;
  f32x4 acc[4];
#pragma unroll
  for (int i = 0; i < 4; ++i) acc[i] = f32x4{0.f, 0.f, 0.f, 0.f};
  {
    const int b = lane & 15, g = lane >> 4;
    const char* ka = lds + ch * KC_CH + (7 - (b & 7)) * KC_STRIDE + (2040 - 8 * (b >> 3) - 16 * (g >> 1) + 8 * (g & 1)) * 2;
    const char* ub = lds + SL_U + ch * U_CH + 512 + (16 * (b - (g >> 1)) + 8 * (g & 1)) * 2;
    if (tg == 0) conv_body<0>(ka, ub, 8 * rh, acc);
    else conv_body<1>(ka, ub, 8 * rh, acc);
  }
  {
    f32x4* red = (f32x4*)(lds + SL_RED);
    red[(wave * 2) * 64 + lane] = rh ? acc[0] : acc[1];
    red[(wave * 2 + 1) * 64 + lane] = rh ? acc[3] : acc[2];
    __syncthreads();
    const f32x4 o0 = red[((wave ^ 2) * 2) * 64 + lane], o1 = red[((wave ^ 2) * 2 + 1) * 64 + lane];
    f32x4 y[2];
    y[0] = (rh ? acc[1] : acc[0]) + o0;
    y[1] = (rh ? acc[2] : acc[3]) + o1;
    const float dsk = bf1(v_dsk);
#pragma unroll
    for (int k = 0; k < 2; ++k) {
      const int ai = rh ? (k ? 2 : 1) : (k ? 3 : 0);
      const int t = tg ? (ai == 0 ? 6 : ai == 1 ? 4 : ai == 2 ? 3 : 1) : (ai == 0 ? 7 : ai == 1 ? 5 : ai == 2 ? 2 : 0);
      const int l0 = 256 * t + 16 * (lane & 15) + 4 * (lane >> 4);
      const u32x2 uu = *(const u32x2*)(lds + SL_U + ch * U_CH + 512 + l0 * 2);
      const u32x2 xx = *(const u32x2*)(lds + SL_X + ch * L * 2 + l0 * 2);
      float hv[4];
#pragma unroll
      for (int j = 0; j < 4; ++j) {
        const float uf = (j & 1) ? bf_hi(uu[j >> 1]) : bf_lo(uu[j >> 1]);
        const float xf = (j & 1) ? bf_hi(xx[j >> 1]) : bf_lo(xx[j >> 1]);
        hv[j] = xf + rbf(rbf(y[k][j]) + rbf(uf * dsk));
      }
      *(u32x2*)(ws + WS_HT + (size_t)d * L * 2 + l0 * 2) = u32x2{pack_bf(hv[0], hv[1]), pack_bf(hv[2], hv[3])};
    }
  }
}

constexpr int F1_BLK = 2048 + 1792 + 128 + 1792;
constexpr int F1_STAGE = 4 * F1_BLK;
constexpr int F1_NST = 7;
constexpr int F1_CS = F1_NST * F1_STAGE;
constexpr int F1_TOTAL = F1_CS + 896;
#define MFMA32(a, b, c) __builtin_amdgcn_mfma_f32_32x32x16_bf16((a), (b), (c), 0, 0, 0)

DI s16x8 tr2(const char* p, int step) {
  const s16x4 a = __builtin_amdgcn_ds_read_tr16_b64_v4i16((LDS3 s16x4*)p);
  const s16x4 b = __builtin_amdgcn_ds_read_tr16_b64_v4i16((LDS3 s16x4*)(p + step));
  return __builtin_shufflevector(a, b, 0, 1, 2, 3, 4, 5, 6, 7);
}
DI u32 lds_addr(const void* p) { return (u32)(uintptr_t)(const LDS3 void*)p; }

__global__ __launch_bounds__(256, 1) void k_ffn1(const u16* __restrict__ Wg, const u16* __restrict__ Wu, char* __restrict__ ws) {
  __shared__ __attribute__((aligned(16))) char lds[F1_TOTAL];
  const int tid = threadIdx.x, lane = tid & 63;
  const int wave = __builtin_amdgcn_readfirstlane(tid >> 6);
  const int xcd = blockIdx.x & 7, bi = blockIdx.x >> 3;
  const int panel = (xcd & 1) * 8 + (bi & 7), slice = (xcd >> 1) * 4 + (bi >> 3);

  const char* hb = ws + WS_HT + panel * 256 - 4096;
  const char* gb = (const char*)Wg + slice * 224 - 4096;
  const char* ub = (const char*)Wu + slice * 224 - 4096;
  u32 ho0, ho1, wo0, wo1, wo2, wo3;
  {
    const int r = lane >> 4;
    const u32 h = (u32)((wave * 8 + r) * (L * 2) + (((lane & 15) ^ (r << 2)) * 16) + 4096);
    ho0 = h;
    ho1 = h + 4 * (L * 2) - 1024;
    const int lw = lane < 56 ? lane : 55;
    const int k = lw / 14, jj = lw - k * 14;
    const u32 w = (u32)((wave * 8 + k) * (F * 2) + jj * 16 + 4096);
    wo0 = w;
    wo1 = w + 4 * (F * 2) - 896;
    wo2 = w - 1920;
    wo3 = w + 4 * (F * 2) - 2816;
  }
  const u32 lbase = lds_addr(lds) + wave * F1_BLK;
  auto stage = [&](int c) {
    u32 keep;
    asm volatile(
        "s_mov_b32 %0, m0\n\t"
        "s_mov_b32 m0, %1\n\t"
        "s_nop 0\n\t"
        "global_load_lds_dwordx4 %2, %8\n\t"
        "global_load_lds_dwordx4 %3, %8 offset:1024\n\t"
        "s_mov_b32 m0, %12\n\t"
        "s_mov_b64 exec, %11\n\t"
        "global_load_lds_dwordx4 %4, %9\n\t"
        "global_load_lds_dwordx4 %5, %9 offset:896\n\t"
        "global_load_lds_dwordx4 %6, %10 offset:1920\n\t"
        "global_load_lds_dwordx4 %7, %10 offset:2816\n\t"
        "s_mov_b64 exec, -1\n\t"
        "s_mov_b32 m0, %0"
        : "=&s"(keep)
        : "s"(lbase + (c % F1_NST) * F1_STAGE), "v"(ho0), "v"(ho1), "v"(wo0), "v"(wo1), "v"(wo2), "v"(wo3),
          "s"(hb + c * 32 * (L * 2)), "s"(gb + c * 32 * (F * 2)), "s"(ub + c * 32 * (F * 2)), "s"(0x00ffffffffffffffull),
          "s"(lbase + (c % F1_NST) * F1_STAGE + 2048)
        : "memory");
  };
  {
    u32 keep;
    asm volatile(
        "s_mov_b32 %0, m0\n\t"
        "s_mov_b32 m0, %1\n\t"
        "s_mov_b64 exec, %4\n\t"
        "global_load_lds_dwordx4 %2, %3\n\t"
        "s_mov_b64 exec, -1\n\t"
        "s_mov_b32 m0, %0"
        : "=&s"(keep)
        : "s"(lds_addr(lds) + F1_CS + wave * 224), "v"(lane * 16), "s"(ws + WS_CS + slice * 896 + wave * 224), "s"(0x3fffull)
        : "memory");
  }
#pragma unroll
  for (int c = 0; c < F1_NST; ++c) stage(c);

  const int hh = lane >> 5, blk = (lane >> 4) & 1, q = (lane & 15) >> 2, p = lane & 3;
  const char* bB = lds + hh * F1_BLK + q * 256 + ((wave ^ q) * 64) + blk * 32 + 8 * p;
  const char* bA = lds + hh * F1_BLK + 2048 + q * 224 + blk * 1920 + 8 * p;

  f32x16 acc[7];
#pragma unroll
  for (int t = 0; t < 7; ++t)
#pragma unroll
    for (int i = 0; i < 16; ++i) acc[t][i] = 0.f;
  float ssum = 0.f, ssq = 0.f;
  bf16x2 ones; ones.x = (__bf16)1.0f; ones.y = (__bf16)1.0f;

  s16x8 A0[7], A1[7], B0, B1;
  auto rd = [&](int c, int s, s16x8 (&A)[7], s16x8& B) {
    const int o = (c % F1_NST) * F1_STAGE;
    B = tr2(bB + o + s * 2 * F1_BLK, 4 * 256);
#pragma unroll
    for (int t = 0; t < 7; ++t) A[t] = tr2(bA + o + s * 2 * F1_BLK + t * 32, 4 * 224);
  };
  auto mm = [&](s16x8 (&A)[7], s16x8& B) {
#pragma unroll
    for (int t = 0; t < 7; ++t) acc[t] = MFMA32(A[t], B, acc[t]);
    const u32x4 bw = __builtin_bit_cast(u32x4, B);
#pragma unroll
    for (int i = 0; i < 4; ++i) {
      const u32 w = bw[i];
      const bf16x2 v = __builtin_bit_cast(bf16x2, w);
      ssum = __builtin_amdgcn_fdot2_f32_bf16(v, ones, ssum, false);
      ssq = __builtin_amdgcn_fdot2_f32_bf16(v, v, ssq, false);
    }
  };
  asm volatile("s_waitcnt vmcnt(36)" ::: "memory");
  __builtin_amdgcn_s_barrier();
  rd(0, 0, A0, B0);
#pragma unroll
  for (int c = 0; c < 14; ++c) {
    rd(c, 1, A1, B1);
    mm(A0, B0);
    if (c < 13) {
      const int pend = (c + F1_NST < 14 ? F1_NST - 2 : 13 - (c + 1)) * 6;
      switch (pend) {
        case 30: asm volatile("s_waitcnt vmcnt(30) lgkmcnt(0)" ::: "memory"); break;
        case 36: asm volatile("s_waitcnt vmcnt(36) lgkmcnt(0)" ::: "memory"); break;
        case 24: asm volatile("s_waitcnt vmcnt(24) lgkmcnt(0)" ::: "memory"); break;
        case 18: asm volatile("s_waitcnt vmcnt(18) lgkmcnt(0)" ::: "memory"); break;
        case 12: asm volatile("s_waitcnt vmcnt(12) lgkmcnt(0)" ::: "memory"); break;
        case 6: asm volatile("s_waitcnt vmcnt(6) lgkmcnt(0)" ::: "memory"); break;
        default: asm volatile("s_waitcnt vmcnt(0) lgkmcnt(0)" ::: "memory"); break;
      }
      __builtin_amdgcn_s_barrier();
      if (c + F1_NST < 14) stage(c + F1_NST);
      rd(c + 1, 0, A0, B0);
    }
    mm(A1, B1);
  }
  ssum += __shfl_xor(ssum, 32);
  ssq += __shfl_xor(ssq, 32);
  const float mean = ssum * (1.0f / D);
  const float var = fmaxf(ssq * (1.0f / D) - mean * mean, 0.f);
  const float mb = rbf(mean);
  const float rs = 1.0f / rbf(sqrtf(rbf(rbf(var) + 1e-5f)));
  const float* cs = (const float*)(lds + F1_CS) + 4 * hh;
  char* dst = ws + WS_HF + ((size_t)(panel * 4 + wave) * 112 + slice * 7) * 1024 + lane * 16;
#pragma unroll
  for (int t = 0; t < 7; ++t) {
    const f32x4 g0 = *(const f32x4*)(cs + t * 32), g1 = *(const f32x4*)(cs + t * 32 + 8);
    const f32x4 u0 = *(const f32x4*)(cs + t * 32 + 16), u1 = *(const f32x4*)(cs + t * 32 + 24);
    float hv[8];
#pragma unroll
    for (int j = 0; j < 8; ++j) {
      const float g = rbf((acc[t][j] - mb * (j < 4 ? g0[j & 3] : g1[j & 3])) * rs);
      const float u = rbf((acc[t][8 + j] - mb * (j < 4 ? u0[j & 3] : u1[j & 3])) * rs);
      hv[j] = (g > 0.f ? g : 0.f) * u;
    }
    *(u32x4*)(dst + t * 1024) = u32x4{pack_bf(hv[0], hv[1]), pack_bf(hv[2], hv[3]), pack_bf(hv[4], hv[5]), pack_bf(hv[6], hv[7])};
  }
}

constexpr int F2_WIN = 12;
constexpr int F2_ROW = 64 * 4 + 16;
constexpr int F2_PART = 64 * F2_ROW;
constexpr int F2_H = 4 * F2_PART;
constexpr int F2_TOTAL = F2_H + 64 * 128;

__global__ __launch_bounds__(256, 1) void k_ffn2(const char* __restrict__ ws, u16* __restrict__ out) {
  __shared__ __attribute__((aligned(16))) char lds[F2_TOTAL];
  const int tid = threadIdx.x, lane = tid & 63;
  const int wave = __builtin_amdgcn_readfirstlane(tid >> 6);
  const int xcd = blockIdx.x & 7, bi = blockIdx.x >> 3;
  const int tgp = xcd * 4 + (bi & 3), dp = bi >> 2;
  const s16x8* pa = (const s16x8*)(ws + WS_WD) + ((size_t)(2 * dp) * 112 + wave * 28) * 64 + lane;
  const s16x8* pb = (const s16x8*)(ws + WS_HF) + ((size_t)(2 * tgp) * 112 + wave * 28) * 64 + lane;
  u32x4 hres[2];
#pragma unroll
  for (int j = 0; j < 2; ++j) {
    const int c = tid + 256 * j;
    hres[j] = *(const u32x4*)(ws + WS_HT + (size_t)(dp * 64 + (c >> 3)) * L * 2 + tgp * 128 + (c & 7) * 16);
  }
  s16x8 ra[F2_WIN][2], rb[F2_WIN][2];
#pragma unroll
  for (int k = 0; k < F2_WIN; ++k) {
    ra[k][0] = pa[k * 64]; rb[k][0] = pb[k * 64];
    ra[k][1] = pa[(112 + k) * 64]; rb[k][1] = pb[(112 + k) * 64];
  }
  __builtin_amdgcn_sched_barrier(0);
  f32x16 acc[2][2];
#pragma unroll
  for (int a = 0; a < 2; ++a)
#pragma unroll
    for (int b = 0; b < 2; ++b)
#pragma unroll
      for (int i = 0; i < 16; ++i) acc[a][b][i] = 0.f;
#pragma unroll
  for (int k = 0; k < 28; ++k) {
    const int s = k % F2_WIN;
#pragma unroll
    for (int a = 0; a < 2; ++a)
#pragma unroll
      for (int b = 0; b < 2; ++b) acc[a][b] = MFMA32(ra[s][a], rb[s][b], acc[a][b]);
    if (k + F2_WIN < 28) {
      ra[s][0] = pa[(k + F2_WIN) * 64]; rb[s][0] = pb[(k + F2_WIN) * 64];
      ra[s][1] = pa[(112 + k + F2_WIN) * 64]; rb[s][1] = pb[(112 + k + F2_WIN) * 64];
    }
    __builtin_amdgcn_sched_barrier(0);
  }
  {
    char* pp = lds + wave * F2_PART + (lane & 31) * F2_ROW + (lane >> 5) * 16;
#pragma unroll
    for (int a = 0; a < 2; ++a)
#pragma unroll
      for (int b = 0; b < 2; ++b)
#pragma unroll
        for (int g = 0; g < 4; ++g)
          *(f32x4*)(pp + b * 32 * F2_ROW + a * 128 + g * 32) = f32x4{acc[a][b][4 * g], acc[a][b][4 * g + 1], acc[a][b][4 * g + 2], acc[a][b][4 * g + 3]};
#pragma unroll
    for (int j = 0; j < 2; ++j) *(u32x4*)(lds + F2_H + (tid + 256 * j) * 16) = hres[j];
  }
  __syncthreads();
#pragma unroll
  for (int j = 0; j < 2; ++j) {
    const int c = tid + 256 * j, tok = c >> 3, dc = c & 7;
    const char* pp = lds + tok * F2_ROW + dc * 32;
    f32x4 s0 = *(const f32x4*)pp, s1 = *(const f32x4*)(pp + 16);
#pragma unroll
    for (int w = 1; w < 4; ++w) { s0 += *(const f32x4*)(pp + w * F2_PART); s1 += *(const f32x4*)(pp + w * F2_PART + 16); }
    const u16* hr = (const u16*)(lds + F2_H) + dc * 8 * 64 + tok;
    float o[8];
#pragma unroll
    for (int i = 0; i < 8; ++i) o[i] = bf1(hr[i * 64]) + rbf(i < 4 ? s0[i & 3] : s1[i & 3]);
    *(u32x4*)(out + (size_t)(tgp * 64 + tok) * D + dp * 64 + dc * 8) =
        u32x4{pack_bf(o[0], o[1]), pack_bf(o[2], o[3]), pack_bf(o[4], o[5]), pack_bf(o[6], o[7])};
  }
}

extern "C" void kernel_launch(void* const* d_in, const int* in_sizes, int n_in,
                              void* d_out, int out_size, void* d_ws, size_t ws_size,
                              hipStream_t stream) {
  const u16* x = (const u16*)d_in[0];
  char* ws = (char*)d_ws;
  k_stats<<<256, 512, 0, stream>>>(x, (u64*)(ws + WS_GRAN));
  k_ssm<<<256, 512, 0, stream>>>(x, (const u16*)d_in[1], (const u16*)d_in[2], (const u16*)d_in[3], (const u16*)d_in[4],
                                 (const u16*)d_in[5], (const u16*)d_in[6], (const u16*)d_in[7], (const u16*)d_in[8],
                                 (const u16*)d_in[10], (const u16*)d_in[11], (const u16*)d_in[12], ws);
  k_ffn1<<<256, 256, 0, stream>>>((const u16*)d_in[10], (const u16*)d_in[11], ws);
  k_ffn2<<<224, 256, 0, stream>>>(ws, (u16*)d_out);
}
```

```cpp
#include <hip/hip_runtime.h>
#include <hip/hip_bf16.h>
#include <stdint.h>

typedef unsigned short u16;
typedef unsigned int u32;
typedef unsigned long long u64;
typedef short s16x4 __attribute__((ext_vector_type(4)));
typedef short s16x8 __attribute__((ext_vector_type(8)));
typedef _Float16 h16x8 __attribute__((ext_vector_type(8)));
typedef _Float16 h16x2 __attribute__((ext_vector_type(2)));
typedef __bf16 bf16x2 __attribute__((ext_vector_type(2)));
typedef float f32x2 __attribute__((ext_vector_type(2)));
typedef float f32x4 __attribute__((ext_vector_type(4)));
typedef float f32x16 __attribute__((ext_vector_type(16)));
typedef u32 u32x2 __attribute__((ext_vector_type(2)));
typedef u32 u32x4 __attribute__((ext_vector_type(4)));
#define LDS3 __attribute__((address_space(3)))
#define GAS __attribute__((address_space(1)))
#define DI __device__ __forceinline__
#define SCHED0() __builtin_amdgcn_sched_barrier(0)

#define WT_AUX 16
constexpr int L = 2048, D = 448, NM = 256, F = 1792;

constexpr u32 WS_SG = 0;
constexpr u32 WS_EPOCH = 16384;
constexpr u32 WS_HT = 65536;
constexpr u32 WS_WD = WS_HT + D * L * 2;
constexpr u32 WS_HF = WS_WD + D * F * 2;
constexpr u32 WS_XG = WS_HF + L * F * 2;
constexpr u32 WS_WT = WS_XG + (D / 2) * L * 8;
constexpr u32 WS_CS = WS_WT + 2 * D * F * 2;
constexpr u32 WS_END = WS_CS + 16 * 14 * 896;

#define STAMP(kid, i) do {} while (0)
#define STAMP_DECL do {} while (0)

DI float bf_lo(u32 w) { return __builtin_bit_cast(float, w << 16); }
DI float bf_hi(u32 w) { return __builtin_bit_cast(float, w & 0xffff0000u); }
DI float bf1(u16 w) { return __builtin_bit_cast(float, (u32)w << 16); }
DI float rbf(float a) { return (float)(__bf16)a; }
DI u32 pack_bf(float a, float b) { bf16x2 v; v.x = (__bf16)a; v.y = (__bf16)b; return __builtin_bit_cast(u32, v); }
DI u32 pack_h(f32x2 a) { h16x2 v; v.x = (_Float16)a.x; v.y = (_Float16)a.y; return __builtin_bit_cast(u32, v); }
DI u32 lds_addr(const void* p) { return (u32)(uintptr_t)(const LDS3 void*)p; }
DI u32 next_tag(u32 e) { return e * 2654435761u + 0x9E3779B9u; }

DI void wave_reduce3(float& a, float& b, int& c) {
#define RED3(ctl) "v_add_f32_dpp %0, %0, %0 " ctl "\n\tv_add_f32_dpp %1, %1, %1 " ctl "\n\tv_max_i32_dpp %2, %2, %2 " ctl "\n\t"
  asm("s_nop 1\n\t"
      RED3("row_shr:1 row_mask:0xf bank_mask:0xf bound_ctrl:1") RED3("row_shr:2 row_mask:0xf bank_mask:0xf bound_ctrl:1")
      RED3("row_shr:4 row_mask:0xf bank_mask:0xf bound_ctrl:1") RED3("row_shr:8 row_mask:0xf bank_mask:0xf bound_ctrl:1")
      RED3("row_bcast:15 row_mask:0xa bank_mask:0xf") RED3("row_bcast:31 row_mask:0xc bank_mask:0xf")
      "s_nop 0"
      : "+v"(a), "+v"(b), "+v"(c));
#undef RED3
}
DI void st128_dev(const char* base, u32 off, u32x4 v) {
  asm volatile("global_store_dwordx4 %0, %1, %2 sc1\n\ts_nop 1" :: "v"(off), "v"(v), "s"(base) : "memory");
}
DI float lane63(float v) { return __builtin_bit_cast(float, __builtin_amdgcn_readlane(__builtin_bit_cast(int, v), 63)); }
DI f32x2 cmul(f32x2 a, f32x2 b) {
  f32x2 t, o;
  asm("v_pk_mul_f32 %0, %1, %2 op_sel_hi:[0,1]" : "=v"(t) : "v"(a), "v"(b));
  asm("v_pk_fma_f32 %0, %1, %2, %3 op_sel:[1,1,0] op_sel_hi:[1,0,1] neg_lo:[0,1,0]" : "=v"(o) : "v"(a), "v"(b), "v"(t));
  return o;
}
DI u32 finish_stats(float mean, float var) { return pack_bf(mean, __builtin_amdgcn_sqrtf(rbf(rbf(fmaxf(var, 0.f)) + 1e-5f))); }
DI float dot_bf(u32 a, u32 b, float c) {
  return __builtin_amdgcn_fdot2_f32_bf16(__builtin_bit_cast(bf16x2, a), __builtin_bit_cast(bf16x2, b), c, false);
}

constexpr int KG_ROW = 1040;
constexpr int KG_CH = 53 * KG_ROW;
constexpr int KC_STRIDE = 4160;
constexpr int KC_CH = 4 * KC_STRIDE;
constexpr int SL_RED = 2 * KC_CH;
constexpr int SL_U = 2 * KG_CH;
constexpr int U_CH = (256 + L) * 2;
constexpr int SL_X = SL_U + 2 * U_CH;
constexpr int XR_ROW = 912;
constexpr int SL_T = SL_X + 2 * L * 2;
constexpr int SL_FLAG = SL_T + 770 * 4;
constexpr int SL_W = SL_T + 772 * 4;
constexpr int SL_TOTAL = SL_W + 48 * 448;
static_assert(SL_RED + 8 * 2048 <= SL_U && 8 * XR_ROW <= 2 * L * 2 && SL_W % 16 == 0 && SL_TOTAL <= 163840 && KG_CH + 768 < 65536, "lds map");

struct GatherTab { u32 a[256 * 12]; };
constexpr GatherTab make_gather_tab() {
  GatherTab t = {};
  for (int tc = 0; tc < 256; ++tc)
    for (int j = 0; j < 12; ++j) {
      const int i = 2047 - 8 * tc - j;
      int slot = 768;
      if (i >= 0) {
        const int s = i < 256 ? 0 : i < 512 ? 1 : i < 1024 ? 2 : 3;
        const int lag = s ? ((i + (1 << (s - 1)) - 1 + ((i >> s) & 1)) >> s) << s : i;
        const int lo = (lag & 255) >> 3, b = lag >> 8, r = lag & 7;
        const int row = b == 0 ? r : b == 1 ? 8 + (r >> 1) : b < 4 ? 12 + 2 * (b - 2) + (r >> 2) : 12 + b;
        slot = row * 32 + lo;
      }
      t.a[tc * 12 + j] = SL_T + 4 * slot;
    }
  return t;
}
static __device__ const GatherTab gather_tab = make_gather_tab();

DI s16x8 tr2(u32 a, int o0, int o1) {
  const s16x4 lo = __builtin_amdgcn_ds_read_tr16_b64_v4i16((LDS3 s16x4*)(uintptr_t)(a + o0));
  const s16x4 hi = __builtin_amdgcn_ds_read_tr16_b64_v4i16((LDS3 s16x4*)(uintptr_t)(a + o1));
  return __builtin_shufflevector(lo, hi, 0, 1, 2, 3, 4, 5, 6, 7);
}
template <int N> DI void wait_vm() { asm volatile("s_waitcnt vmcnt(%c0)" :: "i"(N) : "memory"); }
DI float dot_ones(s16x8 f, float s) {
  const u32x4 w4 = __builtin_bit_cast(u32x4, f);
#pragma unroll
  for (int i = 0; i < 4; ++i) s = dot_bf(w4[i], 0x3f803f80u, s);
  return s;
}
DI float half_sum(float v) {
  const u32 w = __builtin_bit_cast(u32, v);
  const auto r = __builtin_amdgcn_permlane32_swap(w, w, false, false);
  const u32 r0 = r[0], r1 = r[1];
  return __builtin_bit_cast(float, r0) + __builtin_bit_cast(float, r1);
}
DI void set_m0(u32 v) { asm volatile("s_mov_b32 m0, %0\n\ts_nop 0" :: "s"(v) : "memory"); }
template <int R0, int R1, int R2, int R3, int R4, int R5>
DI void put6(u32 v0, u32 v1, u32 v2, u32 v3, u32 v4, u32 v5) {
  asm volatile("ds_write_addtid_b32 %0 offset:%c6\n\tds_write_addtid_b32 %1 offset:%c7\n\tds_write_addtid_b32 %2 offset:%c8\n\t"
               "ds_write_addtid_b32 %3 offset:%c9\n\tds_write_addtid_b32 %4 offset:%c10\n\tds_write_addtid_b32 %5 offset:%c11"
               :: "v"(v0), "v"(v1), "v"(v2), "v"(v3), "v"(v4), "v"(v5),
                  "i"(R0 * KG_ROW), "i"(R1 * KG_ROW), "i"(R2 * KG_ROW), "i"(R3 * KG_ROW), "i"(R4 * KG_ROW), "i"(R5 * KG_ROW)
               : "memory");
}

#define MFMA16(a, b, c) __builtin_amdgcn_mfma_f32_16x16x32_bf16((a), (b), (c), 0, 0, 0)
#define MFMA32(a, b, c) __builtin_amdgcn_mfma_f32_32x32x16_bf16((a), (b), (c), 0, 0, 0)

template <int TG> DI constexpr int conv_tile(int i) { return TG == 0 ? (i == 0 ? 7 : i == 1 ? 5 : i == 2 ? 2 : 0) : (i == 0 ? 6 : i == 1 ? 4 : i == 2 ? 3 : 1); }

struct ConvSet { u32x2 al[8], ah[8]; s16x8 b[8]; };
template <int RR, int P> DI void conv_rd(ConvSet& s, u32 ka, u32 ub) {
  asm volatile("ds_read_b64 %0, %3 offset:%c5\n\tds_read_b64 %1, %3 offset:%c6\n\tds_read_b128 %2, %4 offset:%c7"
               : "=&v"(s.al[P]), "=&v"(s.ah[P]), "=&v"(s.b[P])
               : "v"(ka), "v"(ub), "i"(512 * (7 - P) + 64 * (3 - RR)), "i"(512 * (7 - P) + 64 * (3 - RR) + 8), "i"(512 * P + 64 * (3 - RR)));
}
DI s16x8 conv_a(const ConvSet& s, int p) {
  return __builtin_bit_cast(s16x8, __builtin_shufflevector(s.al[p], s.ah[p], 0, 1, 2, 3));
}
template <int TG, int RR, bool PRE> DI void conv_step(const ConvSet& c, ConvSet& n, u32 ka, u32 ub, f32x4 (&acc)[4]) {
  constexpr int NP = TG == 0 ? 8 : 7;
  asm volatile("s_waitcnt lgkmcnt(0)" ::: "memory");
  SCHED0();
#define CV_MM(i, p) acc[i] = MFMA16(conv_a(c, p), c.b[conv_tile<TG>(i) - (p)], acc[i])
#define CV_RD(p) do { if (PRE && (p) < NP) conv_rd<PRE ? RR + 1 : RR, (p)>(n, ka, ub); SCHED0(); } while (0)
  CV_MM(0, 0); CV_RD(0); CV_MM(1, 0); CV_RD(1);
  CV_MM(0, 1); CV_RD(2); CV_MM(1, 1); CV_RD(3);
  CV_MM(0, 2); CV_RD(4); CV_MM(1, 2); CV_RD(5);
  CV_MM(0, 3); CV_RD(6); CV_MM(1, 3); CV_RD(7);
  CV_MM(0, 4); CV_MM(1, 4); CV_MM(0, 5); CV_MM(2, 0); CV_MM(0, 6); CV_MM(2, 1); CV_MM(3, 0); CV_MM(2, 2);
  if (TG == 0) { CV_MM(0, 7); CV_MM(1, 5); } else { CV_MM(2, 3); CV_MM(3, 1); }
  SCHED0();
#undef CV_MM
#undef CV_RD
}
template <int TG> DI void conv_body(u32 ka, u32 ub, f32x4 (&acc)[4]) {
  ConvSet s0, s1;
  conv_rd<0, 0>(s0, ka, ub); conv_rd<0, 1>(s0, ka, ub); conv_rd<0, 2>(s0, ka, ub); conv_rd<0, 3>(s0, ka, ub);
  conv_rd<0, 4>(s0, ka, ub); conv_rd<0, 5>(s0, ka, ub); conv_rd<0, 6>(s0, ka, ub);
  if (TG == 0) conv_rd<0, 7>(s0, ka, ub);
  conv_step<TG, 0, true>(s0, s1, ka, ub, acc);
  conv_step<TG, 1, true>(s1, s0, ka, ub, acc);
  conv_step<TG, 2, true>(s0, s1, ka, ub, acc);
  conv_step<TG, 3, false>(s1, s0, ka, ub, acc);
}

__global__ __launch_bounds__(512, 2) void k_ssm(
    const u16* __restrict__ x, const u16* __restrict__ A_re, const u16* __restrict__ A_im,
    const u16* __restrict__ B_re, const u16* __restrict__ B_im, const u16* __restrict__ C_re,
    const u16* __restrict__ C_im, const u16* __restrict__ log_dt, const u16* __restrict__ Dskip,
    const u16* __restrict__ Wg, const u16* __restrict__ Wu, const u16* __restrict__ Wd,
    char* __restrict__ ws) {
  __shared__ __attribute__((aligned(16))) char lds[SL_TOTAL];
  STAMP_DECL;
  STAMP(0, 0);
  const int tid = threadIdx.x, lane = tid & 63;
  const int wave = __builtin_amdgcn_readfirstlane(tid >> 6);
  const int xcd = blockIdx.x & 7, idx = blockIdx.x >> 3;
  const bool chore = xcd == 7;
  const __amdgpu_buffer_rsrc_t rs = __builtin_amdgcn_make_buffer_rsrc((void*)ws, 0, (int)WS_END, 0x00020000);

  const int cp = chore ? 0 : xcd * 32 + idx;
  const int ch = wave >> 2, wq = wave & 3;
  const int d = 2 * cp + ch;

  const int row = blockIdx.x * 8 + wave;
  u32x4 xr = {0u, 0u, 0u, 0u};
  if (lane < 56) xr = *(const u32x4*)((const char*)(x + row * D) + lane * 16);
  const u32 epoch = __builtin_amdgcn_raw_buffer_load_b32(rs, 0, WS_EPOCH, 16);
  const int pb = d * NM + wq * 64;
  const u16 v_are = (A_re + pb)[lane], v_aim = (A_im + pb)[lane], v_bre = (B_re + pb)[lane], v_bim = (B_im + pb)[lane];
  const u16 v_cre = (C_re + pb)[lane], v_cim = (C_im + pb)[lane];
  const u16 v_ldt = log_dt[d], v_dsk = Dskip[d];
  const int tc = tid & 255;
  u32x4 ga0 = *(const u32x4*)(gather_tab.a + tc * 12), ga1 = *(const u32x4*)(gather_tab.a + tc * 12 + 4),
        ga2 = *(const u32x4*)(gather_tab.a + tc * 12 + 8);
  const bool tiler = idx < 28;
  const int wu = (xcd & 1) * 28 + idx, wslice = 4 * (xcd >> 1) + wu / 14, wchunk = wu % 14, wv = xcd * 28 + idx;
  const u32 wo_gu = lane * 16 + ((lane * 37) >> 9) * (F * 2 - 224), wo_dn = lane * 16 + (lane >= 28 ? 448 : 0);
  SCHED0();

  *(u32x4*)(lds + SL_X + wave * XR_ROW + min(lane, 56) * 16) = xr;
  const u32 tag = next_tag(epoch);
  u32 pa = v_are, pi_ = v_aim, pbr = v_bre, pbi = v_bim, pcr = v_cre, pci = v_cim, pl = v_ldt, pd = v_dsk;
  asm volatile("" : "+v"(pa), "+v"(pi_), "+v"(pbr), "+v"(pbi), "+v"(pcr), "+v"(pci), "+v"(pl), "+v"(pd), "+v"(ga0), "+v"(ga1), "+v"(ga2));
  __syncthreads();
  if (tid < 448) {
    const u32* xr_l = (const u32*)(lds + SL_X + (tid & 3) * 2 * XR_ROW) + (tid >> 2);
    const u32 a0 = xr_l[0], a1 = xr_l[XR_ROW / 4], a2 = xr_l[112], a3 = xr_l[XR_ROW / 4 + 112];
    const u32 o = (tid >> 2) * (L * 8) + (tid & 3) * 16;
    st128_dev(ws + WS_XG + blockIdx.x * 64, o, u32x4{a0, tag, a1, tag});
    st128_dev(ws + WS_XG + 112 * (L * 8) + blockIdx.x * 64, o, u32x4{a2, tag, a3, tag});
  }
  STAMP(0, 1);
  f32x2 cb;
  float inv_sc, p2, rv;
  {
    float s = 0.f, q = 0.f;
#pragma unroll
    for (int i = 0; i < 4; ++i) { s = dot_bf(xr[i], 0x3f803f80u, s); q = dot_bf(xr[i], xr[i], q); }
    const float dt = rbf(__builtin_amdgcn_exp2f(bf_lo(pl) * 1.4426950408889634f));
    p2 = (dt * bf_lo(pa)) * 1.4426950408889634f;
    rv = (dt * bf_lo(pi_)) * 0.15915494309189535f;
    cb = cmul(f32x2{bf_lo(pcr), bf_lo(pci)}, f32x2{bf_lo(pbr), bf_lo(pbi)} * dt);
    const float cbx = cb.x, cby = cb.y;
    int mx = max(__builtin_bit_cast(int, cbx) & 0x7fffffff, __builtin_bit_cast(int, cby) & 0x7fffffff);
    wave_reduce3(s, q, mx);
    if (lane == 63) {
      const float mean = s * (1.0f / D);
      __builtin_amdgcn_raw_buffer_store_b64(u32x2{finish_stats(mean, q * (1.0f / D) - mean * mean), tag}, rs, 0, WS_SG + row * 8, 16);
    }
    int eb = __builtin_amdgcn_readlane(mx, 63) >> 23;
    eb = __builtin_amdgcn_readfirstlane(eb < 27 ? 27 : (eb > 227 ? 227 : eb));
    inv_sc = __builtin_bit_cast(float, (eb - 9) << 23);
    cb *= __builtin_bit_cast(float, (263 - eb) << 23);
  }
  if (tiler) {
    asm volatile("s_mov_b32 m0, %0\n\ts_mov_b64 exec, %7\n\t"
                 "global_load_lds_dwordx4 %2, %4\n\tglobal_load_lds_dwordx4 %2, %5 offset:896\n\t"
                 "s_mov_b32 m0, %1\n\ts_nop 0\n\tglobal_load_lds_dwordx4 %3, %6\n\t"
                 "s_mov_b64 exec, -1"
                 :: "s"(lds_addr(lds) + SL_W + wave * 1792), "s"(lds_addr(lds) + SL_W + 32 * 448 + wave * 896), "v"(wo_gu), "v"(wo_dn),
                   "s"((const char*)Wg + (wchunk * 32 + wave * 4) * (F * 2) + wslice * 224),
                   "s"((const char*)Wu + (wchunk * 32 + wave * 4) * (F * 2) + wslice * 224 - 896),
                   "s"((const char*)Wd + ((wv >> 1) * 16 + wave * 2) * (D * 2) + (wv & 1) * 448), "s"(0x00ffffffffffffffull)
                 : "memory");
  }
  auto tile_weights = [&]() {
    if (tiler && wave < 7) {
      const int hh = lane >> 5, blk = (lane >> 4) & 1, q = (lane & 15) >> 2, p = lane & 3;
      const u32 a = lds_addr(lds) + SL_W + wave * 32 + hh * 3584 + q * 224 + blk * 896 + 8 * p;
      const s16x8 f0 = tr2(a, 0, 1792), f1 = tr2(a, 4 * 1792, 5 * 1792);
      const u32 b = lds_addr(lds) + SL_W + 32 * 448 + wave * 64 + (4 * hh + q) * 448 + blk * 32 + 8 * p;
      const s16x8 fd = tr2(b, 0, 8 * 448);
      const char* wt = ws + WS_WT + ((wslice * 14 + wchunk) * 14 + wave) * 1024;
      st128_dev(wt, lane * 16, __builtin_bit_cast(u32x4, f0));
      st128_dev(wt + 7 * 1024, lane * 16, __builtin_bit_cast(u32x4, f1));
      st128_dev(ws + WS_WD + (((wv & 1) * 7 + wave) * 112 + (wv >> 1)) * 1024, lane * 16, __builtin_bit_cast(u32x4, fd));
      const float cs = half_sum(dot_ones(f1, dot_ones(f0, 0.f)));
      if (lane < 32) __builtin_amdgcn_raw_buffer_store_b32(__builtin_bit_cast(u32, cs), rs, lane * 4, WS_CS + (wslice * 14 + wchunk) * 896 + wave * 128, WT_AUX);
    }
  };
  STAMP(0, 2);
  if (chore) {
    wait_vm<0>();
    __syncthreads();
    tile_weights();
    STAMP(0, 9);
    return;
  }

  {
    set_m0(lds_addr(lds) + ch * KG_CH + wq * 256);
    auto cpow = [&](float mp, float mr) {
      const float mag = __builtin_amdgcn_exp2f(p2 * mp);
      const float t = __builtin_amdgcn_fractf(rv * mr);
      return f32x2{__builtin_amdgcn_cosf(t), __builtin_amdgcn_sinf(t)} * mag;
    };
    const f32x2 z1 = cpow(1.f, 1.f), c8 = cpow(8.f, -8.f), w256 = cpow(256.f, 256.f);
    const f32x2 z2 = cmul(z1, z1), c16 = cmul(c8, c8);
    const f32x2 z4 = cmul(z2, z2), c32 = cmul(c16, c16);
    f32x2 e0 = c32, e1 = c8, e2 = c16, e3 = cmul(c16, c8);
    f32x2 fa = cb, fb = cmul(cb, w256), fc = fb, f512, f768;
#define STEP() do { fa = cmul(fa, z1); fb = cmul(fb, w256); e1 = cmul(e1, c32); e2 = cmul(e2, c32); e3 = cmul(e3, c32); } while (0)
#define PUT(k, rb, E0) put6<k, rb, 21 + 4 * k, 22 + 4 * k, 23 + 4 * k, 24 + 4 * k>(pack_h(fa), pack_h(fb), E0, pack_h(e1), pack_h(e2), pack_h(e3))
    PUT(0, 8, 0x3c00u);
    STEP(); f512 = fb; PUT(1, 12, pack_h(e0));
    STEP(); e0 = cmul(e0, c32); f768 = fb; PUT(2, 14, pack_h(e0));
    STEP(); e0 = cmul(e0, c32); PUT(3, 16, pack_h(e0));
    STEP(); e0 = cmul(e0, c32); PUT(4, 17, pack_h(e0));
    STEP(); e0 = cmul(e0, c32); PUT(5, 18, pack_h(e0));
    STEP(); e0 = cmul(e0, c32); PUT(6, 19, pack_h(e0));
    STEP(); e0 = cmul(e0, c32); PUT(7, 20, pack_h(e0));
    const f32x2 g1 = cmul(fc, z2), g2 = cmul(fc, z4), g3 = cmul(g1, z4);
    const u32 h772 = pack_h(cmul(f768, z4));
    put6<9, 10, 11, 13, 15, 15>(pack_h(g1), pack_h(g2), pack_h(g3), pack_h(cmul(f512, z4)), h772, h772);
#undef STEP
#undef PUT
    if (wq == 3) *(u32x2*)(lds + SL_U + ch * U_CH + lane * 8) = u32x2{0u, 0u};
    if (wave == 2 && lane == 0) *(u32x4*)(lds + SL_T + 768 * 4) = u32x4{0u, 0u, 0u, 0u};
  }
  STAMP(0, 3);
  {
    char* kg = lds + ch * KG_CH + wq * 256;
    const char* fa = kg + (lane & 31) * KG_ROW + (lane >> 5) * 16;
    h16x8 a[8], b[8];
    asm volatile("s_waitcnt lgkmcnt(0)" ::: "memory");
#pragma unroll
    for (int s = 0; s < 8; ++s) { a[s] = *(const h16x8*)(fa + 32 * s); b[s] = *(const h16x8*)(fa + 21 * KG_ROW + 32 * s); }
    SCHED0();
    f32x16 acc;
#pragma unroll
    for (int i = 0; i < 16; ++i) acc[i] = 0.f;
#pragma unroll
    for (int s = 0; s < 8; ++s) acc = __builtin_amdgcn_mfma_f32_32x32x16_f16(a[s], b[s], acc, 0, 0, 0);
    char* pt = kg + (lane >> 4) * KG_ROW + (lane & 15) * 16;
#pragma unroll
    for (int q = 0; q < 3; ++q)
      *(f32x4*)(pt + 4 * q * KG_ROW) = f32x4{acc[4 * q], acc[4 * q + 1], acc[4 * q + 2], acc[4 * q + 3]} * inv_sc;
  }
  STAMP(0, 4);
  __syncthreads();
  if (wq < 3) {
    const f32x4* pt = (const f32x4*)(lds + ch * KG_CH + 4 * wq * KG_ROW + (lane >> 4) * KG_ROW + (lane & 15) * 16);
    const f32x4 s = (pt[0] + pt[16]) + (pt[32] + pt[48]);
    const u32 p01 = pack_bf(s[0], s[1]), p23 = pack_bf(s[2], s[3]);
    u16* T = (u16*)(lds + SL_T + wq * 1024 + ch * 2 + (lane >> 5) * 512 + (lane & 31) * 4);
    T[0] = (u16)p01; T[64] = (u16)(p01 >> 16); T[128] = (u16)p23; T[192] = (u16)(p23 >> 16);
  }
  const u32 xo = WS_XG + cp * (L * 8);
  u32x4 gx0 = __builtin_amdgcn_raw_buffer_load_b128(rs, tid * 32, xo, 16), gx1 = __builtin_amdgcn_raw_buffer_load_b128(rs, tid * 32 + 16, xo, 16);
  u32x4 gs0 = __builtin_amdgcn_raw_buffer_load_b128(rs, tid * 32, WS_SG, 16), gs1 = __builtin_amdgcn_raw_buffer_load_b128(rs, tid * 32 + 16, WS_SG, 16);
  __syncthreads();
  {
    u32 e[11];
#pragma unroll
    for (int j = 0; j < 11; ++j) {
      const u32 a = j < 4 ? ga0[j & 3] : j < 8 ? ga1[j & 3] : ga2[j & 3];
      e[j] = *(const LDS3 u32*)(uintptr_t)(a + lds_addr(lds));
    }
    const u32 sel = ch ? 0x07060302u : 0x05040100u;
    char* kc = lds + ch * KC_CH + tc * 16;
#pragma unroll
    for (int s = 0; s < 4; ++s)
      *(u32x4*)(kc + s * KC_STRIDE) = u32x4{__builtin_amdgcn_perm(e[s + 1], e[s], sel), __builtin_amdgcn_perm(e[s + 3], e[s + 2], sel),
                                            __builtin_amdgcn_perm(e[s + 5], e[s + 4], sel), __builtin_amdgcn_perm(e[s + 7], e[s + 6], sel)};
    if (wq == 0 && lane < 8) *(u32x4*)(lds + ch * KC_CH + (lane >> 1) * KC_STRIDE + (256 + (lane & 1)) * 16) = u32x4{0u, 0u, 0u, 0u};
  }
  STAMP(0, 5);
  {
    bool clean = true;
    for (u32 spins = 0;; ++spins) {
      const bool ok = gx0[1] == tag && gx0[3] == tag && gx1[1] == tag && gx1[3] == tag &&
                      gs0[1] == tag && gs0[3] == tag && gs1[1] == tag && gs1[3] == tag;
      if (__all(ok)) break;
      if (spins >= 4096u) { clean = false; break; }
      __builtin_amdgcn_s_sleep(2);
      asm volatile("" ::: "memory");
      gx0 = __builtin_amdgcn_raw_buffer_load_b128(rs, tid * 32, xo, 16); gx1 = __builtin_amdgcn_raw_buffer_load_b128(rs, tid * 32 + 16, xo, 16);
      gs0 = __builtin_amdgcn_raw_buffer_load_b128(rs, tid * 32, WS_SG, 16); gs1 = __builtin_amdgcn_raw_buffer_load_b128(rs, tid * 32 + 16, WS_SG, 16);
    }
    u32 xs[4] = {gx0[0], gx0[2], gx1[0], gx1[2]}, st[4] = {gs0[0], gs0[2], gs1[0], gs1[2]};
    if (!clean) {
      *(u32*)(lds + SL_FLAG) = 1u;
#pragma unroll 1
      for (int i = 0; i < 4; ++i) {
        const u32* xrow = (const u32*)(x + (4 * tid + i) * D);
        float s = 0.f, q = 0.f;
#pragma unroll 1
        for (int j = 0; j < D / 2; ++j) { const u32 w = xrow[j]; s = dot_bf(w, 0x3f803f80u, s); q = dot_bf(w, w, q); }
        const float mean = s * (1.0f / D);
        const u32 sv = finish_stats(mean, q * (1.0f / D) - mean * mean), xv = xrow[cp];
        if (i == 0) { xs[0] = xv; st[0] = sv; } else if (i == 1) { xs[1] = xv; st[1] = sv; }
        else if (i == 2) { xs[2] = xv; st[2] = sv; } else { xs[3] = xv; st[3] = sv; }
      }
    }
    f32x2 uv[4];
#pragma unroll
    for (int i = 0; i < 4; ++i)
      uv[i] = (f32x2{bf_lo(xs[i]), bf_hi(xs[i])} - bf_lo(st[i])) * __builtin_amdgcn_rcpf(bf_hi(st[i]));
    char* up = lds + SL_U + tid * 8;
    *(u32x2*)(up + 512) = u32x2{pack_bf(uv[0].x, uv[1].x), pack_bf(uv[2].x, uv[3].x)};
    *(u32x2*)(up + 512 + U_CH) = u32x2{pack_bf(uv[0].y, uv[1].y), pack_bf(uv[2].y, uv[3].y)};
    *(u32x2*)(up + 2 * U_CH) = u32x2{__builtin_amdgcn_perm(xs[1], xs[0], 0x05040100u), __builtin_amdgcn_perm(xs[3], xs[2], 0x05040100u)};
    *(u32x2*)(up + 2 * U_CH + L * 2) = u32x2{__builtin_amdgcn_perm(xs[1], xs[0], 0x07060302u), __builtin_amdgcn_perm(xs[3], xs[2], 0x07060302u)};
  }
  STAMP(0, 6);
  wait_vm<0>();
  __syncthreads();
  if (blockIdx.x == 0 && tid == 0 && *(const u32*)(lds + SL_FLAG) == 0u) *(u32*)(ws + WS_EPOCH) = tag;
  tile_weights();
  STAMP(0, 7);
  const int tg = wq & 1, rh = wq >> 1;
  f32x4 acc[4];
#pragma unroll
  for (int i = 0; i < 4; ++i) acc[i] = f32x4{0.f, 0.f, 0.f, 0.f};
  {
    const int b = lane & 15, g = lane >> 4, r3 = 8 * rh + 6;
    const u32 gg = 16 * (g & 1) - 32 * (g >> 1);
    const u32 ka = lds_addr(lds) + ch * KC_CH + 3 * KC_STRIDE + (2044 - 16 * r3 - 256 * 7) * 2 - (b & 3) * KC_STRIDE - 8 * (b >> 2) + gg;
    const u32 ub = lds_addr(lds) + SL_U + ch * U_CH + 512 - 32 * r3 + 32 * b + gg;
    if (tg == 0) conv_body<0>(ka, ub, acc);
    else conv_body<1>(ka, ub, acc);
  }
  STAMP(0, 8);
  {
    f32x4* red = (f32x4*)(lds + SL_RED) + lane;
    f32x4 y[2];
    if (rh) { red[wave * 128] = acc[0]; red[wave * 128 + 64] = acc[3]; y[0] = acc[1]; y[1] = acc[2]; }
    else { red[wave * 128] = acc[1]; red[wave * 128 + 64] = acc[2]; y[0] = acc[0]; y[1] = acc[3]; }
    const int t0 = rh ? 5 - tg : 7 - tg, t1 = rh ? 2 + tg : tg;
    const int lo = 32 * (lane & 15) + 8 * (lane >> 4);
    const char* up = lds + SL_U + ch * U_CH + 512 + lo;
    const char* xp = lds + SL_X + ch * L * 2 + lo;
    const u32x2 uu0 = *(const u32x2*)(up + 512 * t0), uu1 = *(const u32x2*)(up + 512 * t1);
    const u32x2 xx0 = *(const u32x2*)(xp + 512 * t0), xx1 = *(const u32x2*)(xp + 512 * t1);
    const float dsk = bf_lo(pd);
    f32x2 b0[2], b1[2];
#pragma unroll
    for (int j = 0; j < 2; ++j) {
      b0[j] = __builtin_elementwise_fma(f32x2{bf_lo(uu0[j]), bf_hi(uu0[j])}, f32x2{dsk, dsk}, f32x2{bf_lo(xx0[j]), bf_hi(xx0[j])}) + f32x2{y[0][2 * j], y[0][2 * j + 1]};
      b1[j] = __builtin_elementwise_fma(f32x2{bf_lo(uu1[j]), bf_hi(uu1[j])}, f32x2{dsk, dsk}, f32x2{bf_lo(xx1[j]), bf_hi(xx1[j])}) + f32x2{y[1][2 * j], y[1][2 * j + 1]};
    }
    __syncthreads();
    const f32x4 o0 = red[(wave ^ 2) * 128], o1 = red[(wave ^ 2) * 128 + 64];
    const u32 hb = WS_HT + d * 256, ho = (lo & 255) + (lo >> 8) * (D * 256);
    __builtin_amdgcn_raw_buffer_store_b64(u32x2{pack_bf(b0[0].x + o0[0], b0[0].y + o0[1]), pack_bf(b0[1].x + o0[2], b0[1].y + o0[3])}, rs, ho, hb + 2 * D * 256 * t0, 0);
    __builtin_amdgcn_raw_buffer_store_b64(u32x2{pack_bf(b1[0].x + o1[0], b1[0].y + o1[1]), pack_bf(b1[1].x + o1[2], b1[1].y + o1[3])}, rs, ho, hb + 2 * D * 256 * t1, 0);
  }
  STAMP(0, 9);
}

constexpr int F1_H = 32 * 256;
constexpr int F1_STAGE = F1_H + 14 * 1024;
constexpr int F1_NST = 7;
constexpr int F1_CS = F1_NST * F1_STAGE;
constexpr int F1_ST = F1_CS + 896;
constexpr int F1_TOTAL = F1_ST + 1024;

DI constexpr int f1_pre(int c) { return c < 0 ? 4 : (2 * c + 6 < c + 7 ? 2 * c + 6 : (c + 7 < 14 ? c + 7 : 14)); }

constexpr int F1_PF = 13;
constexpr int F1_LASTB = 9;

template <int C, class S, class X> DI void f1_loader_steps(S& stage, X& stats) {
  if constexpr (C < 14) {
#pragma unroll
    for (int s = f1_pre(C - 1); s < f1_pre(C); ++s) stage(s);
    stats(C);
    if constexpr (C <= F1_LASTB) {
      wait_vm<(C == F1_LASTB ? 0 : (f1_pre(C) - (C + 2)) * 6) + (C >= 8 ? F1_PF : 0)>();
      __builtin_amdgcn_s_barrier();
      asm volatile("" ::: "memory");
    }
    f1_loader_steps<C + 1>(stage, stats);
  }
}

struct F1Frag { s16x8 a[4], b[2]; };

__global__ __launch_bounds__(512, 2) void k_ffn1(char* __restrict__ ws) {
  __shared__ __attribute__((aligned(16))) char lds[F1_TOTAL];
  STAMP_DECL;
  STAMP(1, 0);
  const int tid = threadIdx.x, lane = tid & 63;
  const int wave = __builtin_amdgcn_readfirstlane(tid >> 6);
  const int xcd = blockIdx.x & 7, bi = blockIdx.x >> 3;
  const int panel = (xcd & 1) * 8 + (bi & 7), slice = (xcd >> 1) * 4 + (bi >> 3);

  if (wave >= 4) {
    const int lw = wave - 4;
    const char* hb = ws + WS_HT + panel * (D * 256);
    const char* wb = ws + WS_WT + slice * (14 * 14 * 1024);
    const int r = lane >> 4;
    const u32 ho = (u32)((lw * 8 + r) * 256 + (((lane & 15) ^ (r << 2)) * 16));
    const u32 wo = lw * 3584 + lane * 16;
    const u32 lbase = lds_addr(lds) + lw * 2048;
    auto stage = [&](int c) {
      u32 keep;
      asm volatile(
          "s_mov_b32 %0, m0\n\t"
          "s_mov_b32 m0, %1\n\t"
          "s_nop 0\n\t"
          "global_load_lds_dwordx4 %3, %5\n\t"
          "global_load_lds_dwordx4 %3, %5 offset:1024\n\t"
          "s_mov_b32 m0, %2\n\t"
          "s_nop 0\n\t"
          "global_load_lds_dwordx4 %4, %6\n\t"
          "global_load_lds_dwordx4 %4, %6 offset:1024\n\t"
          "global_load_lds_dwordx4 %4, %6 offset:2048\n\t"
          "s_mov_b64 exec, %7\n\t"
          "global_load_lds_dwordx4 %4, %6 offset:3072\n\t"
          "s_mov_b64 exec, -1\n\t"
          "s_mov_b32 m0, %0"
          : "=&s"(keep)
          : "s"(lbase + (c % F1_NST) * F1_STAGE), "s"(lbase + (c % F1_NST) * F1_STAGE + F1_H + lw * (3584 - 2048)),
            "v"(ho), "v"(wo), "s"(hb + c * 32 * 256), "s"(wb + c * 14 * 1024), "s"(0xffffffffull)
          : "memory");
    };
    float v[14];
    auto stats = [&](int c) {
      if (c == 8) {
      const char* wd = ws + WS_WD + bi * 50176 + lw * 12544;
      u32 keep;
      asm volatile("s_mov_b32 %0, m0\n\ts_mov_b32 m0, %1\n\ts_nop 0\n\t"
                   "global_load_lds_dwordx4 %2, %3\n\tglobal_load_lds_dwordx4 %2, %3 offset:1024\n\t"
                   "global_load_lds_dwordx4 %2, %3 offset:2048\n\tglobal_load_lds_dwordx4 %2, %3 offset:3072\n\t"
                   "global_load_lds_dwordx4 %2, %4\n\tglobal_load_lds_dwordx4 %2, %4 offset:1024\n\t"
                   "global_load_lds_dwordx4 %2, %4 offset:2048\n\tglobal_load_lds_dwordx4 %2, %4 offset:3072\n\t"
                   "global_load_lds_dwordx4 %2, %5\n\tglobal_load_lds_dwordx4 %2, %5 offset:1024\n\t"
                   "global_load_lds_dwordx4 %2, %5 offset:2048\n\tglobal_load_lds_dwordx4 %2, %5 offset:3072\n\t"
                   "s_mov_b64 exec, 0xffff\n\t"
                   "global_load_lds_dwordx4 %2, %6\n\t"
                   "s_mov_b64 exec, -1\n\ts_mov_b32 m0, %0"
                   : "=&s"(keep)
                   : "s"(lds_addr(lds) + lw * 4096), "v"(lane * 16), "s"(wd), "s"(wd + 4096), "s"(wd + 8192), "s"(wd + 12288)
                   : "memory");
    }
      if (c == F1_LASTB + 1) {
        const int ci = tid - 256 < 224 ? tid - 256 : 223;
        const float* cp = (const float*)(ws + WS_CS + slice * (14 * 896)) + ci;
#pragma unroll
        for (int i = 0; i < 14; ++i) v[i] = cp[i * 224];
      }
    };
    stage(0); stage(1); stage(2); stage(3);
    wait_vm<18>();
    __builtin_amdgcn_s_barrier();
    asm volatile("" ::: "memory");
    f1_loader_steps<0>(stage, stats);
    float csum = 0.f;
#pragma unroll
    for (int i = 0; i < 14; ++i) csum += v[i];
    if (tid - 256 < 224) *(float*)(lds + F1_CS + (tid - 256) * 4) = csum;
    __syncthreads();
    wait_vm<0>();
    STAMP(1, 9);
    return;
  }

  const int th = wave >> 1, g = wave & 1;
  const int hh = lane >> 5, blk = (lane >> 4) & 1, q = (lane & 15) >> 2, p = lane & 3;
  const int town = 2 * th + g, toth = 2 * th + 1 - g;
  u32 bO[4], bX[4], bA[4];
#pragma unroll
  for (int i = 0; i < 4; ++i) {
    const u32 o = lds_addr(lds) + i * 2 * F1_STAGE;
    bO[i] = o + (8 * hh + q) * 256 + ((town ^ q) * 64) + blk * 32 + 8 * p;
    bX[i] = o + (8 * hh + q) * 256 + ((toth ^ q) * 64) + blk * 32 + 8 * p;
    bA[i] = o + F1_H + lane * 16;
    asm volatile("" : "+v"(bO[i]), "+v"(bX[i]), "+v"(bA[i]));
  }
  const u32 g3 = g * 3072;
  f32x16 acc[7];
#pragma unroll
  for (int t = 0; t < 7; ++t)
#pragma unroll
    for (int i = 0; i < 16; ++i) acc[t][i] = 0.f;
  float ss0 = 0.f, ss1 = 0.f, sq0 = 0.f, sq1 = 0.f;
  u32 bG[4];
#pragma unroll
  for (int i = 0; i < 4; ++i) { bG[i] = bA[i] + g3; asm volatile("" : "+v"(bG[i])); }
  auto rd = [&](int c, int s, F1Frag& f) {
    const int st = c % F1_NST, i = st >> 1, o = (st & 1) * F1_STAGE + s * 4096, oa = (st & 1) * F1_STAGE + s * 7168;
    f.b[0] = tr2(bO[i], o, o + 1024);
    f.a[0] = *(const LDS3 s16x8*)(uintptr_t)(bG[i] + oa);
    f.b[1] = tr2(bX[i], o, o + 1024);
    f.a[1] = *(const LDS3 s16x8*)(uintptr_t)(bG[i] + oa + 1024);
    f.a[2] = *(const LDS3 s16x8*)(uintptr_t)(bG[i] + oa + 2048);
    f.a[3] = *(const LDS3 s16x8*)(uintptr_t)(bA[i] + oa + 6144);
  };
  auto mm = [&](const F1Frag& f) {
    acc[0] = MFMA32(f.a[0], f.b[0], acc[0]);
    acc[3] = MFMA32(f.a[0], f.b[1], acc[3]);
    acc[1] = MFMA32(f.a[1], f.b[0], acc[1]);
    acc[4] = MFMA32(f.a[1], f.b[1], acc[4]);
    acc[2] = MFMA32(f.a[2], f.b[0], acc[2]);
    acc[5] = MFMA32(f.a[2], f.b[1], acc[5]);
    acc[6] = MFMA32(f.a[3], f.b[0], acc[6]);
    const u32x4 bw = __builtin_bit_cast(u32x4, f.b[0]);
#pragma unroll
    for (int i = 0; i < 4; ++i) {
      const float lo = bf_lo(bw[i]), hi = bf_hi(bw[i]);
      ss0 += lo; sq0 = __builtin_fmaf(lo, lo, sq0);
      ss1 += hi; sq1 = __builtin_fmaf(hi, hi, sq1);
      asm("" : "+v"(ss0)); asm("" : "+v"(sq0)); asm("" : "+v"(ss1)); asm("" : "+v"(sq1));
    }
  };
  auto interleave = [&]() {
#pragma unroll
    for (int i = 0; i < 4; ++i) {
      __builtin_amdgcn_sched_group_barrier(0x8, 1, 0);
      __builtin_amdgcn_sched_group_barrier(0x100, 2, 0);
      __builtin_amdgcn_sched_group_barrier(0x2, 3, 0);
    }
#pragma unroll
    for (int i = 0; i < 3; ++i) {
      __builtin_amdgcn_sched_group_barrier(0x8, 1, 0);
      __builtin_amdgcn_sched_group_barrier(0x2, 4, 0);
    }
    SCHED0();
  };
  F1Frag f0, f1;
  __builtin_amdgcn_s_barrier();
  asm volatile("" ::: "memory");
  STAMP(1, 1);
  rd(0, 0, f0);
  SCHED0();
#pragma unroll
  for (int c = 0; c < 14; ++c) {
    rd(c, 1, f1);
    mm(f0);
    interleave();
    if (c <= F1_LASTB) {
      asm volatile("s_waitcnt lgkmcnt(0)" ::: "memory");
      __builtin_amdgcn_s_barrier();
      asm volatile("" ::: "memory");
      SCHED0();
    }
    if (c < 13) rd(c + 1, 0, f0);
    mm(f1);
    interleave();
  }
  STAMP(1, 3);
  f32x2 so;
  {
    const float mean = half_sum(ss0 + ss1) * (1.0f / D);
    const float var = fmaxf(half_sum(sq0 + sq1) * (1.0f / D) - mean * mean, 0.f);
    const float r = __builtin_amdgcn_rcpf(rbf(__builtin_amdgcn_sqrtf(rbf(rbf(var) + 1e-5f))));
    so = f32x2{-rbf(mean), r * r};
  }
  if (lane < 32) *(f32x2*)(lds + F1_ST + (town * 32 + lane) * 8) = so;
  __syncthreads();
  const f32x2 sx = *(const f32x2*)(lds + F1_ST + (toth * 32 + (lane & 31)) * 8);
  const float* cs = (const float*)(lds + F1_CS) + 4 * hh;
#pragma unroll
  for (int t = 0; t < 7; ++t) {
    const int wt = t == 6 ? 6 : 3 * g + t % 3;
    const f32x2 s = (t >= 3 && t < 6) ? sx : so;
    const int tt = (t >= 3 && t < 6) ? toth : town;
    const float* c4 = cs + wt * 32;
    const f32x4 g0 = *(const f32x4*)(c4), g1 = *(const f32x4*)(c4 + 8), u0 = *(const f32x4*)(c4 + 16), u1 = *(const f32x4*)(c4 + 24);
    u32x4 o;
#pragma unroll
    for (int j = 0; j < 4; ++j) {
      const f32x2 cg = j < 2 ? f32x2{g0[2 * j], g0[2 * j + 1]} : f32x2{g1[2 * j - 4], g1[2 * j - 3]};
      const f32x2 cu = j < 2 ? f32x2{u0[2 * j], u0[2 * j + 1]} : f32x2{u1[2 * j - 4], u1[2 * j - 3]};
      f32x2 gv = __builtin_elementwise_fma(cg, s.xx, f32x2{acc[t][2 * j], acc[t][2 * j + 1]});
      const f32x2 uv = __builtin_elementwise_fma(cu, s.xx, f32x2{acc[t][8 + 2 * j], acc[t][9 + 2 * j]});
      gv.x = fmaxf(gv.x, 0.f); gv.y = fmaxf(gv.y, 0.f);
      const f32x2 hv = (gv * s.yy) * uv;
      o[j] = pack_bf(hv.x, hv.y);
    }
    *(u32x4*)(ws + WS_HF + ((size_t)(panel * 4 + tt) * 112 + slice * 7 + wt) * 1024 + lane * 16) = o;
  }
  STAMP(1, 9);
}

constexpr int F2_WIN = 5;

__global__ __launch_bounds__(256, 1) void k_ffn2(char* __restrict__ ws, u16* __restrict__ out) {
  __shared__ __attribute__((aligned(16))) char lds[4 * 3 * 4096];
  STAMP_DECL;
  STAMP(2, 0);
  const int tid = threadIdx.x, lane = tid & 63;
  const int wave = __builtin_amdgcn_readfirstlane(tid >> 6);
  const int xcd = blockIdx.x & 7, bi = blockIdx.x >> 3;
  const int tgp = (xcd & 1) * 16 + (xcd >> 1) * 4 + (bi & 3), dp = bi >> 2;
  const s16x8* pa = (const s16x8*)(ws + WS_WD) + ((size_t)(2 * dp) * 112 + wave * 28) * 64 + lane;
  const s16x8* pb = (const s16x8*)(ws + WS_HF) + ((size_t)(2 * tgp) * 112 + wave * 28) * 64 + lane;
  s16x8 ra[F2_WIN][2], rb[F2_WIN][2];
#pragma unroll
  for (int k = 0; k < F2_WIN; ++k) {
    ra[k][0] = pa[k * 64]; rb[k][0] = pb[k * 64];
    ra[k][1] = pa[(112 + k) * 64]; rb[k][1] = pb[(112 + k) * 64];
  }
  const int hh = lane >> 5, tok = tgp * 64 + (wave & 1) * 32 + (lane & 31), d0 = dp * 64 + (wave >> 1) * 32 + 4 * hh;
  u16 hres[16];
#pragma unroll
  for (int i = 0; i < 16; ++i) hres[i] = *(const u16*)(ws + WS_HT + (tok >> 7) * (D * 256) + (d0 + (i & 3) + 8 * (i >> 2)) * 256 + (tok & 127) * 2);
  if (blockIdx.x == 0 && tid == 0) { u32* e = (u32*)(ws + WS_EPOCH); *e = next_tag(*e) ^ 0x5bd1e995u; }
  SCHED0();
  f32x16 acc[4];
#pragma unroll
  for (int a = 0; a < 4; ++a)
#pragma unroll
    for (int i = 0; i < 16; ++i) acc[a][i] = 0.f;
#pragma unroll
  for (int k = 0; k < 28; ++k) {
    const int s = k % F2_WIN;
#pragma unroll
    for (int a = 0; a < 4; ++a) acc[a] = MFMA32(ra[s][a >> 1], rb[s][a & 1], acc[a]);
    if (k + F2_WIN < 28) {
      ra[s][0] = pa[(k + F2_WIN) * 64]; rb[s][0] = pb[(k + F2_WIN) * 64];
      ra[s][1] = pa[(112 + k + F2_WIN) * 64]; rb[s][1] = pb[(112 + k + F2_WIN) * 64];
    }
    SCHED0();
  }
  STAMP(2, 1);
  f32x16 mine;
#pragma unroll
  for (int a = 0; a < 4; ++a) {
    if (a == wave) { mine = acc[a]; }
    else {
      const int rank = wave > a ? wave - 1 : wave;
      f32x4* pp = (f32x4*)(lds + (a * 3 + rank) * 4096) + lane;
#pragma unroll
      for (int g = 0; g < 4; ++g) pp[g * 64] = f32x4{acc[a][4 * g], acc[a][4 * g + 1], acc[a][4 * g + 2], acc[a][4 * g + 3]};
    }
  }
  __syncthreads();
  u32 pk[8];
#pragma unroll
  for (int g = 0; g < 4; ++g) {
    const f32x4* pp = (const f32x4*)(lds + wave * 3 * 4096) + g * 64 + lane;
    const f32x4 s = (pp[0] + pp[256]) + (pp[512] + f32x4{mine[4 * g], mine[4 * g + 1], mine[4 * g + 2], mine[4 * g + 3]});
    pk[2 * g] = pack_bf(bf1(hres[4 * g]) + rbf(s[0]), bf1(hres[4 * g + 1]) + rbf(s[1]));
    pk[2 * g + 1] = pack_bf(bf1(hres[4 * g + 2]) + rbf(s[2]), bf1(hres[4 * g + 3]) + rbf(s[3]));
  }
#pragma unroll
  for (int k = 0; k < 2; ++k) {
    const auto r0 = __builtin_amdgcn_permlane32_swap(pk[4 * k], pk[4 * k + 2], false, false);
    const auto r1 = __builtin_amdgcn_permlane32_swap(pk[4 * k + 1], pk[4 * k + 3], false, false);
    *(u32x4*)(out + (size_t)tok * D + dp * 64 + (wave >> 1) * 32 + 16 * k + 8 * hh) = u32x4{r0[0], r1[0], r0[1], r1[1]};
  }
  STAMP(2, 9);
}

extern "C" void kernel_launch(void* const* d_in, const int* in_sizes, int n_in,
                              void* d_out, int out_size, void* d_ws, size_t ws_size,
                              hipStream_t stream) {
  const u16* x = (const u16*)d_in[0];
  char* ws = (char*)d_ws;
  k_ssm<<<256, 512, 0, stream>>>(x, (const u16*)d_in[1], (const u16*)d_in[2], (const u16*)d_in[3], (const u16*)d_in[4],
                                 (const u16*)d_in[5], (const u16*)d_in[6], (const u16*)d_in[7], (const u16*)d_in[8],
                                 (const u16*)d_in[10], (const u16*)d_in[11], (const u16*)d_in[12], ws);
  k_ffn1<<<256, 512, 0, stream>>>(ws);
  k_ffn2<<<224, 256, 0, stream>>>(ws, (u16*)d_out);
}
```

```cpp
#include <hip/hip_runtime.h>
#include <hip/hip_bf16.h>
#include <stdint.h>

typedef unsigned short u16;
typedef unsigned int u32;
typedef unsigned long long u64;
typedef short s16x4 __attribute__((ext_vector_type(4)));
typedef short s16x8 __attribute__((ext_vector_type(8)));
typedef _Float16 h16x8 __attribute__((ext_vector_type(8)));
typedef _Float16 h16x2 __attribute__((ext_vector_type(2)));
typedef __bf16 bf16x2 __attribute__((ext_vector_type(2)));
typedef float f32x2 __attribute__((ext_vector_type(2)));
typedef float f32x4 __attribute__((ext_vector_type(4)));
typedef float f32x16 __attribute__((ext_vector_type(16)));
typedef u32 u32x2 __attribute__((ext_vector_type(2)));
typedef u32 u32x4 __attribute__((ext_vector_type(4)));
#define LDS3 __attribute__((address_space(3)))
#define GAS __attribute__((address_space(1)))
#define DI __device__ __forceinline__
#define SCHED0() __builtin_amdgcn_sched_barrier(0)

#define WT_AUX 16
constexpr int L = 2048, D = 448, NM = 256, F = 1792;

constexpr u32 WS_SG = 0;
constexpr u32 WS_EPOCH = 16384;
constexpr u32 WS_HT = 65536;
constexpr u32 WS_WD = WS_HT + D * L * 2;
constexpr u32 WS_HF = WS_WD + D * F * 2;
constexpr u32 WS_XG = WS_HF + L * F * 2;
constexpr u32 WS_WT = WS_XG + (D / 2) * L * 8;
constexpr u32 WS_CS = WS_WT + 2 * D * F * 2;
constexpr u32 WS_END = WS_CS + 16 * 14 * 896;

#define STAMP(kid, i) do {} while (0)
#define STAMP_DECL do {} while (0)

DI float bf_lo(u32 w) { return __builtin_bit_cast(float, w << 16); }
DI float bf_hi(u32 w) { return __builtin_bit_cast(float, w & 0xffff0000u); }
DI float bf1(u16 w) { return __builtin_bit_cast(float, (u32)w << 16); }
DI float rbf(float a) { return (float)(__bf16)a; }
DI u32 pack_bf(float a, float b) { bf16x2 v; v.x = (__bf16)a; v.y = (__bf16)b; return __builtin_bit_cast(u32, v); }
DI u32 pack_h(f32x2 a) { h16x2 v; v.x = (_Float16)a.x; v.y = (_Float16)a.y; return __builtin_bit_cast(u32, v); }
DI u32 lds_addr(const void* p) { return (u32)(uintptr_t)(const LDS3 void*)p; }
DI u32 next_tag(u32 e) { return e * 2654435761u + 0x9E3779B9u; }

DI void wave_reduce3(float& a, float& b, int& c) {
#define RED3(ctl) "v_add_f32_dpp %0, %0, %0 " ctl "\n\tv_add_f32_dpp %1, %1, %1 " ctl "\n\tv_max_i32_dpp %2, %2, %2 " ctl "\n\t"
  asm("s_nop 1\n\t"
      RED3("row_shr:1 row_mask:0xf bank_mask:0xf bound_ctrl:1") RED3("row_shr:2 row_mask:0xf bank_mask:0xf bound_ctrl:1")
      RED3("row_shr:4 row_mask:0xf bank_mask:0xf bound_ctrl:1") RED3("row_shr:8 row_mask:0xf bank_mask:0xf bound_ctrl:1")
      RED3("row_bcast:15 row_mask:0xa bank_mask:0xf") RED3("row_bcast:31 row_mask:0xc bank_mask:0xf")
      "s_nop 0"
      : "+v"(a), "+v"(b), "+v"(c));
#undef RED3
}
DI void st128_dev(const char* base, u32 off, u32x4 v) {
  asm volatile("global_store_dwordx4 %0, %1, %2 sc1\n\ts_nop 1" :: "v"(off), "v"(v), "s"(base) : "memory");
}
DI float lane63(float v) { return __builtin_bit_cast(float, __builtin_amdgcn_readlane(__builtin_bit_cast(int, v), 63)); }
DI f32x2 cmul(f32x2 a, f32x2 b) {
  f32x2 t, o;
  asm("v_pk_mul_f32 %0, %1, %2 op_sel_hi:[0,1]" : "=v"(t) : "v"(a), "v"(b));
  asm("v_pk_fma_f32 %0, %1, %2, %3 op_sel:[1,1,0] op_sel_hi:[1,0,1] neg_lo:[0,1,0]" : "=v"(o) : "v"(a), "v"(b), "v"(t));
  return o;
}
DI u32 finish_stats(float mean, float var) { return pack_bf(mean, __builtin_amdgcn_sqrtf(rbf(rbf(fmaxf(var, 0.f)) + 1e-5f))); }
DI float dot_bf(u32 a, u32 b, float c) {
  return __builtin_amdgcn_fdot2_f32_bf16(__builtin_bit_cast(bf16x2, a), __builtin_bit_cast(bf16x2, b), c, false);
}

constexpr int KG_ROW = 1040;
constexpr int KG_CH = 53 * KG_ROW;
constexpr int KC_STRIDE = 4160;
constexpr int KC_CH = 4 * KC_STRIDE;
constexpr int SL_RED = 2 * KC_CH;
constexpr int SL_U = 2 * KG_CH;
constexpr int U_CH = (256 + L) * 2;
constexpr int SL_X = SL_U + 2 * U_CH;
constexpr int XR_ROW = 912;
constexpr int SL_T = SL_X + 2 * L * 2;
constexpr int SL_FLAG = SL_T + 770 * 4;
constexpr int SL_W = SL_T + 772 * 4;
constexpr int SL_TOTAL = SL_W + 48 * 448;
static_assert(SL_RED + 8 * 2048 <= SL_U && 8 * XR_ROW <= 2 * L * 2 && SL_W % 16 == 0 && SL_TOTAL <= 163840 && KG_CH + 768 < 65536, "lds map");

struct GatherTab { u32 a[256 * 12]; };
constexpr GatherTab make_gather_tab() {
  GatherTab t = {};
  for (int tc = 0; tc < 256; ++tc)
    for (int j = 0; j < 12; ++j) {
      const int i = 2047 - 8 * tc - j;
      int slot = 768;
      if (i >= 0) {
        const int s = i < 256 ? 0 : i < 512 ? 1 : i < 1024 ? 2 : 3;
        const int lag = s ? ((i + (1 << (s - 1)) - 1 + ((i >> s) & 1)) >> s) << s : i;
        const int lo = (lag & 255) >> 3, b = lag >> 8, r = lag & 7;
        const int row = b == 0 ? r : b == 1 ? 8 + (r >> 1) : b < 4 ? 12 + 2 * (b - 2) + (r >> 2) : 12 + b;
        slot = row * 32 + lo;
      }
      t.a[tc * 12 + j] = SL_T + 4 * slot;
    }
  return t;
}
static __device__ const GatherTab gather_tab = make_gather_tab();

DI s16x8 tr2(u32 a, int o0, int o1) {
  const s16x4 lo = __builtin_amdgcn_ds_read_tr16_b64_v4i16((LDS3 s16x4*)(uintptr_t)(a + o0));
  const s16x4 hi = __builtin_amdgcn_ds_read_tr16_b64_v4i16((LDS3 s16x4*)(uintptr_t)(a + o1));
  return __builtin_shufflevector(lo, hi, 0, 1, 2, 3, 4, 5, 6, 7);
}
template <int N> DI void wait_vm() { asm volatile("s_waitcnt vmcnt(%c0)" :: "i"(N) : "memory"); }
DI float dot_ones(s16x8 f, float s) {
  const u32x4 w4 = __builtin_bit_cast(u32x4, f);
#pragma unroll
  for (int i = 0; i < 4; ++i) s = dot_bf(w4[i], 0x3f803f80u, s);
  return s;
}
DI float half_sum(float v) {
  const u32 w = __builtin_bit_cast(u32, v);
  const auto r = __builtin_amdgcn_permlane32_swap(w, w, false, false);
  const u32 r0 = r[0], r1 = r[1];
  return __builtin_bit_cast(float, r0) + __builtin_bit_cast(float, r1);
}
DI void set_m0(u32 v) { asm volatile("s_mov_b32 m0, %0\n\ts_nop 0" :: "s"(v) : "memory"); }
template <int R0, int R1, int R2, int R3, int R4, int R5>
DI void put6(u32 v0, u32 v1, u32 v2, u32 v3, u32 v4, u32 v5) {
  asm volatile("ds_write_addtid_b32 %0 offset:%c6\n\tds_write_addtid_b32 %1 offset:%c7\n\tds_write_addtid_b32 %2 offset:%c8\n\t"
               "ds_write_addtid_b32 %3 offset:%c9\n\tds_write_addtid_b32 %4 offset:%c10\n\tds_write_addtid_b32 %5 offset:%c11"
               :: "v"(v0), "v"(v1), "v"(v2), "v"(v3), "v"(v4), "v"(v5),
                  "i"(R0 * KG_ROW), "i"(R1 * KG_ROW), "i"(R2 * KG_ROW), "i"(R3 * KG_ROW), "i"(R4 * KG_ROW), "i"(R5 * KG_ROW)
               : "memory");
}

#define MFMA16(a, b, c) __builtin_amdgcn_mfma_f32_16x16x32_bf16((a), (b), (c), 0, 0, 0)
#define MFMA32(a, b, c) __builtin_amdgcn_mfma_f32_32x32x16_bf16((a), (b), (c), 0, 0, 0)

template <int TG> DI constexpr int conv_tile(int i) { return TG == 0 ? (i == 0 ? 7 : i == 1 ? 5 : i == 2 ? 2 : 0) : (i == 0 ? 6 : i == 1 ? 4 : i == 2 ? 3 : 1); }

struct ConvSet { u32x2 al[8], ah[8]; s16x8 b[8]; };
template <int RR, int P> DI void conv_rd(ConvSet& s, u32 ka, u32 ub) {
  asm volatile("ds_read_b64 %0, %3 offset:%c5\n\tds_read_b64 %1, %3 offset:%c6\n\tds_read_b128 %2, %4 offset:%c7"
               : "=&v"(s.al[P]), "=&v"(s.ah[P]), "=&v"(s.b[P])
               : "v"(ka), "v"(ub), "i"(512 * (7 - P) + 64 * (3 - RR)), "i"(512 * (7 - P) + 64 * (3 - RR) + 8), "i"(512 * P + 64 * (3 - RR)));
}
DI s16x8 conv_a(const ConvSet& s, int p) {
  return __builtin_bit_cast(s16x8, __builtin_shufflevector(s.al[p], s.ah[p], 0, 1, 2, 3));
}
template <int TG, int RR, bool PRE> DI void conv_step(const ConvSet& c, ConvSet& n, u32 ka, u32 ub, f32x4 (&acc)[4]) {
  constexpr int NP = TG == 0 ? 8 : 7;
  asm volatile("s_waitcnt lgkmcnt(0)" ::: "memory");
  SCHED0();
#define CV_MM(i, p) acc[i] = MFMA16(conv_a(c, p), c.b[conv_tile<TG>(i) - (p)], acc[i])
#define CV_RD(p) do { if (PRE && (p) < NP) conv_rd<PRE ? RR + 1 : RR, (p)>(n, ka, ub); SCHED0(); } while (0)
  CV_MM(0, 0); CV_RD(0); CV_MM(1, 0); CV_RD(1);
  CV_MM(0, 1); CV_RD(2); CV_MM(1, 1); CV_RD(3);
  CV_MM(0, 2); CV_RD(4); CV_MM(1, 2); CV_RD(5);
  CV_MM(0, 3); CV_RD(6); CV_MM(1, 3); CV_RD(7);
  CV_MM(0, 4); CV_MM(1, 4); CV_MM(0, 5); CV_MM(2, 0); CV_MM(0, 6); CV_MM(2, 1); CV_MM(3, 0); CV_MM(2, 2);
  if (TG == 0) { CV_MM(0, 7); CV_MM(1, 5); } else { CV_MM(2, 3); CV_MM(3, 1); }
  SCHED0();
#undef CV_MM
#undef CV_RD
}
template <int TG> DI void conv_body(u32 ka, u32 ub, f32x4 (&acc)[4]) {
  ConvSet s0, s1;
  conv_rd<0, 0>(s0, ka, ub); conv_rd<0, 1>(s0, ka, ub); conv_rd<0, 2>(s0, ka, ub); conv_rd<0, 3>(s0, ka, ub);
  conv_rd<0, 4>(s0, ka, ub); conv_rd<0, 5>(s0, ka, ub); conv_rd<0, 6>(s0, ka, ub);
  if (TG == 0) conv_rd<0, 7>(s0, ka, ub);
  conv_step<TG, 0, true>(s0, s1, ka, ub, acc);
  conv_step<TG, 1, true>(s1, s0, ka, ub, acc);
  conv_step<TG, 2, true>(s0, s1, ka, ub, acc);
  conv_step<TG, 3, false>(s1, s0, ka, ub, acc);
}

__global__ __launch_bounds__(512, 2) void k_ssm(
    const u16* __restrict__ x, const u16* __restrict__ A_re, const u16* __restrict__ A_im,
    const u16* __restrict__ B_re, const u16* __restrict__ B_im, const u16* __restrict__ C_re,
    const u16* __restrict__ C_im, const u16* __restrict__ log_dt, const u16* __restrict__ Dskip,
    const u16* __restrict__ Wg, const u16* __restrict__ Wu, const u16* __restrict__ Wd,
    char* __restrict__ ws) {
  __shared__ __attribute__((aligned(16))) char lds[SL_TOTAL];
  STAMP_DECL;
  STAMP(0, 0);
  const int tid = threadIdx.x, lane = tid & 63;
  const int wave = __builtin_amdgcn_readfirstlane(tid >> 6);
  const int xcd = blockIdx.x & 7, idx = blockIdx.x >> 3;
  u32x4 cpf0, cpf1;
  {
    const GAS char* pc; u32 n1;
    asm volatile("s_getpc_b64 %0\n.Lssm_code_pc:\n\ts_mov_b32 %1, .Lffn2_code_end-.Lssm_code_pc-16" : "=s"(pc), "=s"(n1));
    cpf0 = *(const GAS u32x4*)(pc + (u32)(tid * 16));
    cpf1 = *(const GAS u32x4*)(pc + min(8192u + (idx & 3) * 8192u + tid * 16u, n1));
  }
  const bool chore = xcd == 7;
  const __amdgpu_buffer_rsrc_t rs = __builtin_amdgcn_make_buffer_rsrc((void*)ws, 0, (int)WS_END, 0x00020000);

  const int cp = chore ? 0 : xcd * 32 + idx;
  const int ch = wave >> 2, wq = wave & 3;
  const int d = 2 * cp + ch;

  const int row = blockIdx.x * 8 + wave;
  u32x4 xr = {0u, 0u, 0u, 0u};
  if (lane < 56) xr = *(const u32x4*)((const char*)(x + row * D) + lane * 16);
  const u32 epoch = __builtin_amdgcn_raw_buffer_load_b32(rs, 0, WS_EPOCH, 16);
  const int pb = d * NM + wq * 64;
  const u16 v_are = (A_re + pb)[lane], v_aim = (A_im + pb)[lane], v_bre = (B_re + pb)[lane], v_bim = (B_im + pb)[lane];
  const u16 v_cre = (C_re + pb)[lane], v_cim = (C_im + pb)[lane];
  const u16 v_ldt = log_dt[d], v_dsk = Dskip[d];
  const int tc = tid & 255;
  u32x4 ga0 = *(const u32x4*)(gather_tab.a + tc * 12), ga1 = *(const u32x4*)(gather_tab.a + tc * 12 + 4),
        ga2 = *(const u32x4*)(gather_tab.a + tc * 12 + 8);
  const bool tiler = idx < 28;
  const int wu = (xcd & 1) * 28 + idx, wslice = 4 * (xcd >> 1) + wu / 14, wchunk = wu % 14, wv = xcd * 28 + idx;
  const u32 wo_gu = lane * 16 + ((lane * 37) >> 9) * (F * 2 - 224), wo_dn = lane * 16 + (lane >= 28 ? 448 : 0);
  SCHED0();

  *(u32x4*)(lds + SL_X + wave * XR_ROW + min(lane, 56) * 16) = xr;
  const u32 tag = next_tag(epoch);
  u32 pa = v_are, pi_ = v_aim, pbr = v_bre, pbi = v_bim, pcr = v_cre, pci = v_cim, pl = v_ldt, pd = v_dsk;
  asm volatile("" : "+v"(pa), "+v"(pi_), "+v"(pbr), "+v"(pbi), "+v"(pcr), "+v"(pci), "+v"(pl), "+v"(pd), "+v"(ga0), "+v"(ga1), "+v"(ga2));
  asm volatile("" :: "v"(cpf0), "v"(cpf1));
  __syncthreads();
  if (tid < 448) {
    const u32* xr_l = (const u32*)(lds + SL_X + (tid & 3) * 2 * XR_ROW) + (tid >> 2);
    const u32 a0 = xr_l[0], a1 = xr_l[XR_ROW / 4], a2 = xr_l[112], a3 = xr_l[XR_ROW / 4 + 112];
    const u32 o = (tid >> 2) * (L * 8) + (tid & 3) * 16;
    st128_dev(ws + WS_XG + blockIdx.x * 64, o, u32x4{a0, tag, a1, tag});
    st128_dev(ws + WS_XG + 112 * (L * 8) + blockIdx.x * 64, o, u32x4{a2, tag, a3, tag});
  }
  STAMP(0, 1);
  f32x2 cb;
  float inv_sc, p2, rv;
  {
    float s = 0.f, q = 0.f;
#pragma unroll
    for (int i = 0; i < 4; ++i) { s = dot_bf(xr[i], 0x3f803f80u, s); q = dot_bf(xr[i], xr[i], q); }
    const float dt = rbf(__builtin_amdgcn_exp2f(bf_lo(pl) * 1.4426950408889634f));
    p2 = (dt * bf_lo(pa)) * 1.4426950408889634f;
    rv = (dt * bf_lo(pi_)) * 0.15915494309189535f;
    cb = cmul(f32x2{bf_lo(pcr), bf_lo(pci)}, f32x2{bf_lo(pbr), bf_lo(pbi)} * dt);
    const float cbx = cb.x, cby = cb.y;
    int mx = max(__builtin_bit_cast(int, cbx) & 0x7fffffff, __builtin_bit_cast(int, cby) & 0x7fffffff);
    wave_reduce3(s, q, mx);
    if (lane == 63) {
      const float mean = s * (1.0f / D);
      __builtin_amdgcn_raw_buffer_store_b64(u32x2{finish_stats(mean, q * (1.0f / D) - mean * mean), tag}, rs, 0, WS_SG + row * 8, 16);
    }
    int eb = __builtin_amdgcn_readlane(mx, 63) >> 23;
    eb = __builtin_amdgcn_readfirstlane(eb < 27 ? 27 : (eb > 227 ? 227 : eb));
    inv_sc = __builtin_bit_cast(float, (eb - 9) << 23);
    cb *= __builtin_bit_cast(float, (263 - eb) << 23);
  }
  if (tiler) {
    asm volatile("s_mov_b32 m0, %0\n\ts_mov_b64 exec, %7\n\t"
                 "global_load_lds_dwordx4 %2, %4\n\tglobal_load_lds_dwordx4 %2, %5 offset:896\n\t"
                 "s_mov_b32 m0, %1\n\ts_nop 0\n\tglobal_load_lds_dwordx4 %3, %6\n\t"
                 "s_mov_b64 exec, -1"
                 :: "s"(lds_addr(lds) + SL_W + wave * 1792), "s"(lds_addr(lds) + SL_W + 32 * 448 + wave * 896), "v"(wo_gu), "v"(wo_dn),
                   "s"((const char*)Wg + (wchunk * 32 + wave * 4) * (F * 2) + wslice * 224),
                   "s"((const char*)Wu + (wchunk * 32 + wave * 4) * (F * 2) + wslice * 224 - 896),
                   "s"((const char*)Wd + ((wv >> 1) * 16 + wave * 2) * (D * 2) + (wv & 1) * 448), "s"(0x00ffffffffffffffull)
                 : "memory");
  }
  auto tile_weights = [&]() {
    if (tiler && wave < 7) {
      const int hh = lane >> 5, blk = (lane >> 4) & 1, q = (lane & 15) >> 2, p = lane & 3;
      const u32 a = lds_addr(lds) + SL_W + wave * 32 + hh * 3584 + q * 224 + blk * 896 + 8 * p;
      const s16x8 f0 = tr2(a, 0, 1792), f1 = tr2(a, 4 * 1792, 5 * 1792);
      const u32 b = lds_addr(lds) + SL_W + 32 * 448 + wave * 64 + (4 * hh + q) * 448 + blk * 32 + 8 * p;
      const s16x8 fd = tr2(b, 0, 8 * 448);
      const char* wt = ws + WS_WT + ((wslice * 14 + wchunk) * 14 + wave) * 1024;
      st128_dev(wt, lane * 16, __builtin_bit_cast(u32x4, f0));
      st128_dev(wt + 7 * 1024, lane * 16, __builtin_bit_cast(u32x4, f1));
      st128_dev(ws + WS_WD + (((wv & 1) * 7 + wave) * 112 + (wv >> 1)) * 1024, lane * 16, __builtin_bit_cast(u32x4, fd));
      const float cs = half_sum(dot_ones(f1, dot_ones(f0, 0.f)));
      if (lane < 32) __builtin_amdgcn_raw_buffer_store_b32(__builtin_bit_cast(u32, cs), rs, lane * 4, WS_CS + (wslice * 14 + wchunk) * 896 + wave * 128, WT_AUX);
    }
  };
  STAMP(0, 2);
  if (chore) {
    wait_vm<0>();
    __syncthreads();
    tile_weights();
    STAMP(0, 9);
    return;
  }

  {
    set_m0(lds_addr(lds) + ch * KG_CH + wq * 256);
    auto cpow = [&](float mp, float mr) {
      const float mag = __builtin_amdgcn_exp2f(p2 * mp);
      const float t = __builtin_amdgcn_fractf(rv * mr);
      return f32x2{__builtin_amdgcn_cosf(t), __builtin_amdgcn_sinf(t)} * mag;
    };
    const f32x2 z1 = cpow(1.f, 1.f), c8 = cpow(8.f, -8.f), w256 = cpow(256.f, 256.f);
    const f32x2 z2 = cmul(z1, z1), c16 = cmul(c8, c8);
    const f32x2 z4 = cmul(z2, z2), c32 = cmul(c16, c16);
    f32x2 e0 = c32, e1 = c8, e2 = c16, e3 = cmul(c16, c8);
    f32x2 fa = cb, fb = cmul(cb, w256), fc = fb, f512, f768;
#define STEP() do { fa = cmul(fa, z1); fb = cmul(fb, w256); e1 = cmul(e1, c32); e2 = cmul(e2, c32); e3 = cmul(e3, c32); } while (0)
#define PUT(k, rb, E0) put6<k, rb, 21 + 4 * k, 22 + 4 * k, 23 + 4 * k, 24 + 4 * k>(pack_h(fa), pack_h(fb), E0, pack_h(e1), pack_h(e2), pack_h(e3))
    PUT(0, 8, 0x3c00u);
    STEP(); f512 = fb; PUT(1, 12, pack_h(e0));
    STEP(); e0 = cmul(e0, c32); f768 = fb; PUT(2, 14, pack_h(e0));
    STEP(); e0 = cmul(e0, c32); PUT(3, 16, pack_h(e0));
    STEP(); e0 = cmul(e0, c32); PUT(4, 17, pack_h(e0));
    STEP(); e0 = cmul(e0, c32); PUT(5, 18, pack_h(e0));
    STEP(); e0 = cmul(e0, c32); PUT(6, 19, pack_h(e0));
    STEP(); e0 = cmul(e0, c32); PUT(7, 20, pack_h(e0));
    const f32x2 g1 = cmul(fc, z2), g2 = cmul(fc, z4), g3 = cmul(g1, z4);
    const u32 h772 = pack_h(cmul(f768, z4));
    put6<9, 10, 11, 13, 15, 15>(pack_h(g1), pack_h(g2), pack_h(g3), pack_h(cmul(f512, z4)), h772, h772);
#undef STEP
#undef PUT
    if (wq == 3) *(u32x2*)(lds + SL_U + ch * U_CH + lane * 8) = u32x2{0u, 0u};
    if (wave == 2 && lane == 0) *(u32x4*)(lds + SL_T + 768 * 4) = u32x4{0u, 0u, 0u, 0u};
  }
  STAMP(0, 3);
  {
    char* kg = lds + ch * KG_CH + wq * 256;
    const char* fa = kg + (lane & 31) * KG_ROW + (lane >> 5) * 16;
    h16x8 a[8], b[8];
    asm volatile("s_waitcnt lgkmcnt(0)" ::: "memory");
#pragma unroll
    for (int s = 0; s < 8; ++s) { a[s] = *(const h16x8*)(fa + 32 * s); b[s] = *(const h16x8*)(fa + 21 * KG_ROW + 32 * s); }
    SCHED0();
    f32x16 acc;
#pragma unroll
    for (int i = 0; i < 16; ++i) acc[i] = 0.f;
#pragma unroll
    for (int s = 0; s < 8; ++s) acc = __builtin_amdgcn_mfma_f32_32x32x16_f16(a[s], b[s], acc, 0, 0, 0);
    char* pt = kg + (lane >> 4) * KG_ROW + (lane & 15) * 16;
#pragma unroll
    for (int q = 0; q < 3; ++q)
      *(f32x4*)(pt + 4 * q * KG_ROW) = f32x4{acc[4 * q], acc[4 * q + 1], acc[4 * q + 2], acc[4 * q + 3]} * inv_sc;
  }
  STAMP(0, 4);
  __syncthreads();
  if (wq < 3) {
    const f32x4* pt = (const f32x4*)(lds + ch * KG_CH + 4 * wq * KG_ROW + (lane >> 4) * KG_ROW + (lane & 15) * 16);
    const f32x4 s = (pt[0] + pt[16]) + (pt[32] + pt[48]);
    const u32 p01 = pack_bf(s[0], s[1]), p23 = pack_bf(s[2], s[3]);
    u16* T = (u16*)(lds + SL_T + wq * 1024 + ch * 2 + (lane >> 5) * 512 + (lane & 31) * 4);
    T[0] = (u16)p01; T[64] = (u16)(p01 >> 16); T[128] = (u16)p23; T[192] = (u16)(p23 >> 16);
  }
  const u32 xo = WS_XG + cp * (L * 8);
  u32x4 gx0 = __builtin_amdgcn_raw_buffer_load_b128(rs, tid * 32, xo, 16), gx1 = __builtin_amdgcn_raw_buffer_load_b128(rs, tid * 32 + 16, xo, 16);
  u32x4 gs0 = __builtin_amdgcn_raw_buffer_load_b128(rs, tid * 32, WS_SG, 16), gs1 = __builtin_amdgcn_raw_buffer_load_b128(rs, tid * 32 + 16, WS_SG, 16);
  __syncthreads();
  {
    u32 e[11];
#pragma unroll
    for (int j = 0; j < 11; ++j) {
      const u32 a = j < 4 ? ga0[j & 3] : j < 8 ? ga1[j & 3] : ga2[j & 3];
      e[j] = *(const LDS3 u32*)(uintptr_t)(a + lds_addr(lds));
    }
    const u32 sel = ch ? 0x07060302u : 0x05040100u;
    char* kc = lds + ch * KC_CH + tc * 16;
#pragma unroll
    for (int s = 0; s < 4; ++s)
      *(u32x4*)(kc + s * KC_STRIDE) = u32x4{__builtin_amdgcn_perm(e[s + 1], e[s], sel), __builtin_amdgcn_perm(e[s + 3], e[s + 2], sel),
                                            __builtin_amdgcn_perm(e[s + 5], e[s + 4], sel), __builtin_amdgcn_perm(e[s + 7], e[s + 6], sel)};
    if (wq == 0 && lane < 8) *(u32x4*)(lds + ch * KC_CH + (lane >> 1) * KC_STRIDE + (256 + (lane & 1)) * 16) = u32x4{0u, 0u, 0u, 0u};
  }
  STAMP(0, 5);
  {
    bool clean = true;
    for (u32 spins = 0;; ++spins) {
      const bool ok = gx0[1] == tag && gx0[3] == tag && gx1[1] == tag && gx1[3] == tag &&
                      gs0[1] == tag && gs0[3] == tag && gs1[1] == tag && gs1[3] == tag;
      if (__all(ok)) break;
      if (spins >= 4096u) { clean = false; break; }
      __builtin_amdgcn_s_sleep(2);
      asm volatile("" ::: "memory");
      gx0 = __builtin_amdgcn_raw_buffer_load_b128(rs, tid * 32, xo, 16); gx1 = __builtin_amdgcn_raw_buffer_load_b128(rs, tid * 32 + 16, xo, 16);
      gs0 = __builtin_amdgcn_raw_buffer_load_b128(rs, tid * 32, WS_SG, 16); gs1 = __builtin_amdgcn_raw_buffer_load_b128(rs, tid * 32 + 16, WS_SG, 16);
    }
    u32 xs[4] = {gx0[0], gx0[2], gx1[0], gx1[2]}, st[4] = {gs0[0], gs0[2], gs1[0], gs1[2]};
    if (!clean) {
      *(u32*)(lds + SL_FLAG) = 1u;
#pragma unroll 1
      for (int i = 0; i < 4; ++i) {
        const u32* xrow = (const u32*)(x + (4 * tid + i) * D);
        float s = 0.f, q = 0.f;
#pragma unroll 1
        for (int j = 0; j < D / 2; ++j) { const u32 w = xrow[j]; s = dot_bf(w, 0x3f803f80u, s); q = dot_bf(w, w, q); }
        const float mean = s * (1.0f / D);
        const u32 sv = finish_stats(mean, q * (1.0f / D) - mean * mean), xv = xrow[cp];
        if (i == 0) { xs[0] = xv; st[0] = sv; } else if (i == 1) { xs[1] = xv; st[1] = sv; }
        else if (i == 2) { xs[2] = xv; st[2] = sv; } else { xs[3] = xv; st[3] = sv; }
      }
    }
    f32x2 uv[4];
#pragma unroll
    for (int i = 0; i < 4; ++i)
      uv[i] = (f32x2{bf_lo(xs[i]), bf_hi(xs[i])} - bf_lo(st[i])) * __builtin_amdgcn_rcpf(bf_hi(st[i]));
    char* up = lds + SL_U + tid * 8;
    *(u32x2*)(up + 512) = u32x2{pack_bf(uv[0].x, uv[1].x), pack_bf(uv[2].x, uv[3].x)};
    *(u32x2*)(up + 512 + U_CH) = u32x2{pack_bf(uv[0].y, uv[1].y), pack_bf(uv[2].y, uv[3].y)};
    *(u32x2*)(up + 2 * U_CH) = u32x2{__builtin_amdgcn_perm(xs[1], xs[0], 0x05040100u), __builtin_amdgcn_perm(xs[3], xs[2], 0x05040100u)};
    *(u32x2*)(up + 2 * U_CH + L * 2) = u32x2{__builtin_amdgcn_perm(xs[1], xs[0], 0x07060302u), __builtin_amdgcn_perm(xs[3], xs[2], 0x07060302u)};
  }
  STAMP(0, 6);
  wait_vm<0>();
  __syncthreads();
  if (blockIdx.x == 0 && tid == 0 && *(const u32*)(lds + SL_FLAG) == 0u) *(u32*)(ws + WS_EPOCH) = tag;
  tile_weights();
  STAMP(0, 7);
  const int tg = wq & 1, rh = wq >> 1;
  f32x4 acc[4];
#pragma unroll
  for (int i = 0; i < 4; ++i) acc[i] = f32x4{0.f, 0.f, 0.f, 0.f};
  {
    const int b = lane & 15, g = lane >> 4, r3 = 8 * rh + 6;
    const u32 gg = 16 * (g & 1) - 32 * (g >> 1);
    const u32 ka = lds_addr(lds) + ch * KC_CH + 3 * KC_STRIDE + (2044 - 16 * r3 - 256 * 7) * 2 - (b & 3) * KC_STRIDE - 8 * (b >> 2) + gg;
    const u32 ub = lds_addr(lds) + SL_U + ch * U_CH + 512 - 32 * r3 + 32 * b + gg;
    if (tg == 0) conv_body<0>(ka, ub, acc);
    else conv_body<1>(ka, ub, acc);
  }
  STAMP(0, 8);
  {
    f32x4* red = (f32x4*)(lds + SL_RED) + lane;
    f32x4 y[2];
    if (rh) { red[wave * 128] = acc[0]; red[wave * 128 + 64] = acc[3]; y[0] = acc[1]; y[1] = acc[2]; }
    else { red[wave * 128] = acc[1]; red[wave * 128 + 64] = acc[2]; y[0] = acc[0]; y[1] = acc[3]; }
    const int t0 = rh ? 5 - tg : 7 - tg, t1 = rh ? 2 + tg : tg;
    const int lo = 32 * (lane & 15) + 8 * (lane >> 4);
    const char* up = lds + SL_U + ch * U_CH + 512 + lo;
    const char* xp = lds + SL_X + ch * L * 2 + lo;
    const u32x2 uu0 = *(const u32x2*)(up + 512 * t0), uu1 = *(const u32x2*)(up + 512 * t1);
    const u32x2 xx0 = *(const u32x2*)(xp + 512 * t0), xx1 = *(const u32x2*)(xp + 512 * t1);
    const float dsk = bf_lo(pd);
    f32x2 b0[2], b1[2];
#pragma unroll
    for (int j = 0; j < 2; ++j) {
      b0[j] = __builtin_elementwise_fma(f32x2{bf_lo(uu0[j]), bf_hi(uu0[j])}, f32x2{dsk, dsk}, f32x2{bf_lo(xx0[j]), bf_hi(xx0[j])}) + f32x2{y[0][2 * j], y[0][2 * j + 1]};
      b1[j] = __builtin_elementwise_fma(f32x2{bf_lo(uu1[j]), bf_hi(uu1[j])}, f32x2{dsk, dsk}, f32x2{bf_lo(xx1[j]), bf_hi(xx1[j])}) + f32x2{y[1][2 * j], y[1][2 * j + 1]};
    }
    __syncthreads();
    const f32x4 o0 = red[(wave ^ 2) * 128], o1 = red[(wave ^ 2) * 128 + 64];
    const u32 hb = WS_HT + d * 256, ho = (lo & 255) + (lo >> 8) * (D * 256);
    __builtin_amdgcn_raw_buffer_store_b64(u32x2{pack_bf(b0[0].x + o0[0], b0[0].y + o0[1]), pack_bf(b0[1].x + o0[2], b0[1].y + o0[3])}, rs, ho, hb + 2 * D * 256 * t0, 0);
    __builtin_amdgcn_raw_buffer_store_b64(u32x2{pack_bf(b1[0].x + o1[0], b1[0].y + o1[1]), pack_bf(b1[1].x + o1[2], b1[1].y + o1[3])}, rs, ho, hb + 2 * D * 256 * t1, 0);
  }
  STAMP(0, 9);
}

constexpr int F1_H = 32 * 256;
constexpr int F1_STAGE = F1_H + 14 * 1024;
constexpr int F1_NST = 7;
constexpr int F1_CS = F1_NST * F1_STAGE;
constexpr int F1_ST = F1_CS + 896;
constexpr int F1_TOTAL = F1_ST + 1024;

DI constexpr int f1_pre(int c) { return c < 0 ? 4 : (2 * c + 6 < c + 7 ? 2 * c + 6 : (c + 7 < 14 ? c + 7 : 14)); }

constexpr int F1_PF = 13;
constexpr int F1_LASTB = 9;

template <int C, class S, class X> DI void f1_loader_steps(S& stage, X& stats) {
  if constexpr (C < 14) {
#pragma unroll
    for (int s = f1_pre(C - 1); s < f1_pre(C); ++s) stage(s);
    stats(C);
    if constexpr (C <= F1_LASTB) {
      wait_vm<(C == F1_LASTB ? 0 : (f1_pre(C) - (C + 2)) * 6) + (C >= 8 ? F1_PF : 0)>();
      __builtin_amdgcn_s_barrier();
      asm volatile("" ::: "memory");
    }
    f1_loader_steps<C + 1>(stage, stats);
  }
}

struct F1Frag { s16x8 a[4], b[2]; };

__global__ __launch_bounds__(512, 2) void k_ffn1(char* __restrict__ ws) {
  __shared__ __attribute__((aligned(16))) char lds[F1_TOTAL];
  STAMP_DECL;
  STAMP(1, 0);
  const int tid = threadIdx.x, lane = tid & 63;
  const int wave = __builtin_amdgcn_readfirstlane(tid >> 6);
  const int xcd = blockIdx.x & 7, bi = blockIdx.x >> 3;
  const int panel = (xcd & 1) * 8 + (bi & 7), slice = (xcd >> 1) * 4 + (bi >> 3);

  if (wave >= 4) {
    const int lw = wave - 4;
    const char* hb = ws + WS_HT + panel * (D * 256);
    const char* wb = ws + WS_WT + slice * (14 * 14 * 1024);
    const int r = lane >> 4;
    const u32 ho = (u32)((lw * 8 + r) * 256 + (((lane & 15) ^ (r << 2)) * 16));
    const u32 wo = lw * 3584 + lane * 16;
    const u32 lbase = lds_addr(lds) + lw * 2048;
    auto stage = [&](int c) {
      u32 keep;
      asm volatile(
          "s_mov_b32 %0, m0\n\t"
          "s_mov_b32 m0, %1\n\t"
          "s_nop 0\n\t"
          "global_load_lds_dwordx4 %3, %5\n\t"
          "global_load_lds_dwordx4 %3, %5 offset:1024\n\t"
          "s_mov_b32 m0, %2\n\t"
          "s_nop 0\n\t"
          "global_load_lds_dwordx4 %4, %6\n\t"
          "global_load_lds_dwordx4 %4, %6 offset:1024\n\t"
          "global_load_lds_dwordx4 %4, %6 offset:2048\n\t"
          "s_mov_b64 exec, %7\n\t"
          "global_load_lds_dwordx4 %4, %6 offset:3072\n\t"
          "s_mov_b64 exec, -1\n\t"
          "s_mov_b32 m0, %0"
          : "=&s"(keep)
          : "s"(lbase + (c % F1_NST) * F1_STAGE), "s"(lbase + (c % F1_NST) * F1_STAGE + F1_H + lw * (3584 - 2048)),
            "v"(ho), "v"(wo), "s"(hb + c * 32 * 256), "s"(wb + c * 14 * 1024), "s"(0xffffffffull)
          : "memory");
    };
    float v[14];
    auto stats = [&](int c) {
      if (c == 8) {
      const char* wd = ws + WS_WD + bi * 50176 + lw * 12544;
      u32 keep;
      asm volatile("s_mov_b32 %0, m0\n\ts_mov_b32 m0, %1\n\ts_nop 0\n\t"
                   "global_load_lds_dwordx4 %2, %3\n\tglobal_load_lds_dwordx4 %2, %3 offset:1024\n\t"
                   "global_load_lds_dwordx4 %2, %3 offset:2048\n\tglobal_load_lds_dwordx4 %2, %3 offset:3072\n\t"
                   "global_load_lds_dwordx4 %2, %4\n\tglobal_load_lds_dwordx4 %2, %4 offset:1024\n\t"
                   "global_load_lds_dwordx4 %2, %4 offset:2048\n\tglobal_load_lds_dwordx4 %2, %4 offset:3072\n\t"
                   "global_load_lds_dwordx4 %2, %5\n\tglobal_load_lds_dwordx4 %2, %5 offset:1024\n\t"
                   "global_load_lds_dwordx4 %2, %5 offset:2048\n\tglobal_load_lds_dwordx4 %2, %5 offset:3072\n\t"
                   "s_mov_b64 exec, 0xffff\n\t"
                   "global_load_lds_dwordx4 %2, %6\n\t"
                   "s_mov_b64 exec, -1\n\ts_mov_b32 m0, %0"
                   : "=&s"(keep)
                   : "s"(lds_addr(lds) + lw * 4096), "v"(lane * 16), "s"(wd), "s"(wd + 4096), "s"(wd + 8192), "s"(wd + 12288)
                   : "memory");
    }
      if (c == F1_LASTB + 1) {
        const int ci = tid - 256 < 224 ? tid - 256 : 223;
        const float* cp = (const float*)(ws + WS_CS + slice * (14 * 896)) + ci;
#pragma unroll
        for (int i = 0; i < 14; ++i) v[i] = cp[i * 224];
      }
    };
    stage(0); stage(1); stage(2); stage(3);
    wait_vm<18>();
    __builtin_amdgcn_s_barrier();
    asm volatile("" ::: "memory");
    f1_loader_steps<0>(stage, stats);
    float csum = 0.f;
#pragma unroll
    for (int i = 0; i < 14; ++i) csum += v[i];
    if (tid - 256 < 224) *(float*)(lds + F1_CS + (tid - 256) * 4) = csum;
    __syncthreads();
    wait_vm<0>();
    STAMP(1, 9);
    return;
  }

  const int th = wave >> 1, g = wave & 1;
  const int hh = lane >> 5, blk = (lane >> 4) & 1, q = (lane & 15) >> 2, p = lane & 3;
  const int town = 2 * th + g, toth = 2 * th + 1 - g;
  u32 bO[4], bX[4], bA[4];
#pragma unroll
  for (int i = 0; i < 4; ++i) {
    const u32 o = lds_addr(lds) + i * 2 * F1_STAGE;
    bO[i] = o + (8 * hh + q) * 256 + ((town ^ q) * 64) + blk * 32 + 8 * p;
    bX[i] = o + (8 * hh + q) * 256 + ((toth ^ q) * 64) + blk * 32 + 8 * p;
    bA[i] = o + F1_H + lane * 16;
    asm volatile("" : "+v"(bO[i]), "+v"(bX[i]), "+v"(bA[i]));
  }
  const u32 g3 = g * 3072;
  f32x16 acc[7];
#pragma unroll
  for (int t = 0; t < 7; ++t)
#pragma unroll
    for (int i = 0; i < 16; ++i) acc[t][i] = 0.f;
  float ss0 = 0.f, ss1 = 0.f, sq0 = 0.f, sq1 = 0.f;
  u32 bG[4];
#pragma unroll
  for (int i = 0; i < 4; ++i) { bG[i] = bA[i] + g3; asm volatile("" : "+v"(bG[i])); }
  auto rd = [&](int c, int s, F1Frag& f) {
    const int st = c % F1_NST, i = st >> 1, o = (st & 1) * F1_STAGE + s * 4096, oa = (st & 1) * F1_STAGE + s * 7168;
    f.b[0] = tr2(bO[i], o, o + 1024);
    f.a[0] = *(const LDS3 s16x8*)(uintptr_t)(bG[i] + oa);
    f.b[1] = tr2(bX[i], o, o + 1024);
    f.a[1] = *(const LDS3 s16x8*)(uintptr_t)(bG[i] + oa + 1024);
    f.a[2] = *(const LDS3 s16x8*)(uintptr_t)(bG[i] + oa + 2048);
    f.a[3] = *(const LDS3 s16x8*)(uintptr_t)(bA[i] + oa + 6144);
  };
  auto mm = [&](const F1Frag& f) {
    acc[0] = MFMA32(f.a[0], f.b[0], acc[0]);
    acc[3] = MFMA32(f.a[0], f.b[1], acc[3]);
    acc[1] = MFMA32(f.a[1], f.b[0], acc[1]);
    acc[4] = MFMA32(f.a[1], f.b[1], acc[4]);
    acc[2] = MFMA32(f.a[2], f.b[0], acc[2]);
    acc[5] = MFMA32(f.a[2], f.b[1], acc[5]);
    acc[6] = MFMA32(f.a[3], f.b[0], acc[6]);
    const u32x4 bw = __builtin_bit_cast(u32x4, f.b[0]);
#pragma unroll
    for (int i = 0; i < 4; ++i) {
      const float lo = bf_lo(bw[i]), hi = bf_hi(bw[i]);
      ss0 += lo; sq0 = __builtin_fmaf(lo, lo, sq0);
      ss1 += hi; sq1 = __builtin_fmaf(hi, hi, sq1);
      asm("" : "+v"(ss0)); asm("" : "+v"(sq0)); asm("" : "+v"(ss1)); asm("" : "+v"(sq1));
    }
  };
  auto interleave = [&]() {
#pragma unroll
    for (int i = 0; i < 4; ++i) {
      __builtin_amdgcn_sched_group_barrier(0x8, 1, 0);
      __builtin_amdgcn_sched_group_barrier(0x100, 2, 0);
      __builtin_amdgcn_sched_group_barrier(0x2, 3, 0);
    }
#pragma unroll
    for (int i = 0; i < 3; ++i) {
      __builtin_amdgcn_sched_group_barrier(0x8, 1, 0);
      __builtin_amdgcn_sched_group_barrier(0x2, 4, 0);
    }
    SCHED0();
  };
  F1Frag f0, f1;
  __builtin_amdgcn_s_barrier();
  asm volatile("" ::: "memory");
  STAMP(1, 1);
  rd(0, 0, f0);
  SCHED0();
#pragma unroll
  for (int c = 0; c < 14; ++c) {
    rd(c, 1, f1);
    mm(f0);
    interleave();
    if (c <= F1_LASTB) {
      asm volatile("s_waitcnt lgkmcnt(0)" ::: "memory");
      __builtin_amdgcn_s_barrier();
      asm volatile("" ::: "memory");
      SCHED0();
    }
    if (c < 13) rd(c + 1, 0, f0);
    mm(f1);
    interleave();
  }
  STAMP(1, 3);
  f32x2 so;
  {
    const float mean = half_sum(ss0 + ss1) * (1.0f / D);
    const float var = fmaxf(half_sum(sq0 + sq1) * (1.0f / D) - mean * mean, 0.f);
    const float r = __builtin_amdgcn_rcpf(rbf(__builtin_amdgcn_sqrtf(rbf(rbf(var) + 1e-5f))));
    so = f32x2{-rbf(mean), r * r};
  }
  if (lane < 32) *(f32x2*)(lds + F1_ST + (town * 32 + lane) * 8) = so;
  __syncthreads();
  const f32x2 sx = *(const f32x2*)(lds + F1_ST + (toth * 32 + (lane & 31)) * 8);
  const float* cs = (const float*)(lds + F1_CS) + 4 * hh;
#pragma unroll
  for (int t = 0; t < 7; ++t) {
    const int wt = t == 6 ? 6 : 3 * g + t % 3;
    const f32x2 s = (t >= 3 && t < 6) ? sx : so;
    const int tt = (t >= 3 && t < 6) ? toth : town;
    const float* c4 = cs + wt * 32;
    const f32x4 g0 = *(const f32x4*)(c4), g1 = *(const f32x4*)(c4 + 8), u0 = *(const f32x4*)(c4 + 16), u1 = *(const f32x4*)(c4 + 24);
    u32x4 o;
#pragma unroll
    for (int j = 0; j < 4; ++j) {
      const f32x2 cg = j < 2 ? f32x2{g0[2 * j], g0[2 * j + 1]} : f32x2{g1[2 * j - 4], g1[2 * j - 3]};
      const f32x2 cu = j < 2 ? f32x2{u0[2 * j], u0[2 * j + 1]} : f32x2{u1[2 * j - 4], u1[2 * j - 3]};
      f32x2 gv = __builtin_elementwise_fma(cg, s.xx, f32x2{acc[t][2 * j], acc[t][2 * j + 1]});
      const f32x2 uv = __builtin_elementwise_fma(cu, s.xx, f32x2{acc[t][8 + 2 * j], acc[t][9 + 2 * j]});
      gv.x = fmaxf(gv.x, 0.f); gv.y = fmaxf(gv.y, 0.f);
      const f32x2 hv = (gv * s.yy) * uv;
      o[j] = pack_bf(hv.x, hv.y);
    }
    *(u32x4*)(ws + WS_HF + ((size_t)(panel * 4 + tt) * 112 + slice * 7 + wt) * 1024 + lane * 16) = o;
  }
  STAMP(1, 9);
}

constexpr int F2_WIN = 5;

__global__ __launch_bounds__(256, 1) void k_ffn2(char* __restrict__ ws, u16* __restrict__ out) {
  __shared__ __attribute__((aligned(16))) char lds[4 * 3 * 4096];
  STAMP_DECL;
  STAMP(2, 0);
  const int tid = threadIdx.x, lane = tid & 63;
  const int wave = __builtin_amdgcn_readfirstlane(tid >> 6);
  const int xcd = blockIdx.x & 7, bi = blockIdx.x >> 3;
  const int tgp = (xcd & 1) * 16 + (xcd >> 1) * 4 + (bi & 3), dp = bi >> 2;
  const s16x8* pa = (const s16x8*)(ws + WS_WD) + ((size_t)(2 * dp) * 112 + wave * 28) * 64 + lane;
  const s16x8* pb = (const s16x8*)(ws + WS_HF) + ((size_t)(2 * tgp) * 112 + wave * 28) * 64 + lane;
  s16x8 ra[F2_WIN][2], rb[F2_WIN][2];
#pragma unroll
  for (int k = 0; k < F2_WIN; ++k) {
    ra[k][0] = pa[k * 64]; rb[k][0] = pb[k * 64];
    ra[k][1] = pa[(112 + k) * 64]; rb[k][1] = pb[(112 + k) * 64];
  }
  const int hh = lane >> 5, tok = tgp * 64 + (wave & 1) * 32 + (lane & 31), d0 = dp * 64 + (wave >> 1) * 32 + 4 * hh;
  u16 hres[16];
#pragma unroll
  for (int i = 0; i < 16; ++i) hres[i] = *(const u16*)(ws + WS_HT + (tok >> 7) * (D * 256) + (d0 + (i & 3) + 8 * (i >> 2)) * 256 + (tok & 127) * 2);
  if (blockIdx.x == 0 && tid == 0) { u32* e = (u32*)(ws + WS_EPOCH); *e = next_tag(*e) ^ 0x5bd1e995u; }
  SCHED0();
  f32x16 acc[4];
#pragma unroll
  for (int a = 0; a < 4; ++a)
#pragma unroll
    for (int i = 0; i < 16; ++i) acc[a][i] = 0.f;
#pragma unroll
  for (int k = 0; k < 28; ++k) {
    const int s = k % F2_WIN;
#pragma unroll
    for (int a = 0; a < 4; ++a) acc[a] = MFMA32(ra[s][a >> 1], rb[s][a & 1], acc[a]);
    if (k + F2_WIN < 28) {
      ra[s][0] = pa[(k + F2_WIN) * 64]; rb[s][0] = pb[(k + F2_WIN) * 64];
      ra[s][1] = pa[(112 + k + F2_WIN) * 64]; rb[s][1] = pb[(112 + k + F2_WIN) * 64];
    }
    SCHED0();
  }
  STAMP(2, 1);
  f32x16 mine;
#pragma unroll
  for (int a = 0; a < 4; ++a) {
    if (a == wave) { mine = acc[a]; }
    else {
      const int rank = wave > a ? wave - 1 : wave;
      f32x4* pp = (f32x4*)(lds + (a * 3 + rank) * 4096) + lane;
#pragma unroll
      for (int g = 0; g < 4; ++g) pp[g * 64] = f32x4{acc[a][4 * g], acc[a][4 * g + 1], acc[a][4 * g + 2], acc[a][4 * g + 3]};
    }
  }
  __syncthreads();
  u32 pk[8];
#pragma unroll
  for (int g = 0; g < 4; ++g) {
    const f32x4* pp = (const f32x4*)(lds + wave * 3 * 4096) + g * 64 + lane;
    const f32x4 s = (pp[0] + pp[256]) + (pp[512] + f32x4{mine[4 * g], mine[4 * g + 1], mine[4 * g + 2], mine[4 * g + 3]});
    pk[2 * g] = pack_bf(bf1(hres[4 * g]) + rbf(s[0]), bf1(hres[4 * g + 1]) + rbf(s[1]));
    pk[2 * g + 1] = pack_bf(bf1(hres[4 * g + 2]) + rbf(s[2]), bf1(hres[4 * g + 3]) + rbf(s[3]));
  }
#pragma unroll
  for (int k = 0; k < 2; ++k) {
    const auto r0 = __builtin_amdgcn_permlane32_swap(pk[4 * k], pk[4 * k + 2], false, false);
    const auto r1 = __builtin_amdgcn_permlane32_swap(pk[4 * k + 1], pk[4 * k + 3], false, false);
    *(u32x4*)(out + (size_t)tok * D + dp * 64 + (wave >> 1) * 32 + 16 * k + 8 * hh) = u32x4{r0[0], r1[0], r0[1], r1[1]};
  }
  STAMP(2, 9);
  asm volatile(".Lffn2_code_end:");
}

extern "C" void kernel_launch(void* const* d_in, const int* in_sizes, int n_in,
                              void* d_out, int out_size, void* d_ws, size_t ws_size,
                              hipStream_t stream) {
  const u16* x = (const u16*)d_in[0];
  char* ws = (char*)d_ws;
  k_ssm<<<256, 512, 0, stream>>>(x, (const u16*)d_in[1], (const u16*)d_in[2], (const u16*)d_in[3], (const u16*)d_in[4],
                                 (const u16*)d_in[5], (const u16*)d_in[6], (const u16*)d_in[7], (const u16*)d_in[8],
                                 (const u16*)d_in[10], (const u16*)d_in[11], (const u16*)d_in[12], ws);
  k_ffn1<<<256, 512, 0, stream>>>(ws);
  k_ffn2<<<224, 256, 0, stream>>>(ws, (u16*)d_out);
}
```

```cpp
#include <hip/hip_runtime.h>
#include <hip/hip_bf16.h>
#include <stdint.h>

typedef unsigned short u16;
typedef unsigned int u32;
typedef unsigned long long u64;
typedef short s16x4 __attribute__((ext_vector_type(4)));
typedef short s16x8 __attribute__((ext_vector_type(8)));
typedef _Float16 h16x8 __attribute__((ext_vector_type(8)));
typedef _Float16 h16x2 __attribute__((ext_vector_type(2)));
typedef __bf16 bf16x2 __attribute__((ext_vector_type(2)));
typedef float f32x2 __attribute__((ext_vector_type(2)));
typedef float f32x4 __attribute__((ext_vector_type(4)));
typedef float f32x16 __attribute__((ext_vector_type(16)));
typedef u32 u32x2 __attribute__((ext_vector_type(2)));
typedef u32 u32x4 __attribute__((ext_vector_type(4)));
#define LDS3 __attribute__((address_space(3)))
#define GAS __attribute__((address_space(1)))
#define DI __device__ __forceinline__
#define SCHED0() __builtin_amdgcn_sched_barrier(0)

#define WT_AUX 16
#define TILE_AT 1
#define POLL_AT 2
constexpr int L = 2048, D = 448, NM = 256, F = 1792;

constexpr u32 WS_SG = 0;
constexpr u32 WS_EPOCH = 16384;
constexpr u32 WS_HT = 65536;
constexpr u32 WS_WD = WS_HT + D * L * 2;
constexpr u32 WS_HF = WS_WD + D * F * 2;
constexpr u32 WS_XG = WS_HF + L * F * 2;
constexpr u32 WS_WT = WS_XG + (D / 2) * L * 8;
constexpr u32 WS_CS = WS_WT + 2 * D * F * 2;
constexpr u32 WS_PS = WS_CS + 16 * 14 * 896;
constexpr u32 WS_END = WS_PS + 16 * 14 * 1024;

#define STAMP(kid, i) do {} while (0)
#define STAMP_DECL do {} while (0)

#define FST(i) do {} while (0)
#define FST_OUT() do {} while (0)
DI float bf_lo(u32 w) { return __builtin_bit_cast(float, w << 16); }
DI float bf_hi(u32 w) { return __builtin_bit_cast(float, w & 0xffff0000u); }
DI float bf1(u16 w) { return __builtin_bit_cast(float, (u32)w << 16); }
DI float rbf(float a) { return (float)(__bf16)a; }
DI u32 pack_bf(float a, float b) { bf16x2 v; v.x = (__bf16)a; v.y = (__bf16)b; return __builtin_bit_cast(u32, v); }
DI u32 pack_h(f32x2 a) { h16x2 v; v.x = (_Float16)a.x; v.y = (_Float16)a.y; return __builtin_bit_cast(u32, v); }
DI u32 lds_addr(const void* p) { return (u32)(uintptr_t)(const LDS3 void*)p; }
DI float relu(float v) { return __builtin_bit_cast(float, max(__builtin_bit_cast(int, v), 0)); }
DI u32 next_tag(u32 e) { return e * 2654435761u + 0x9E3779B9u; }

DI void wave_reduce3(float& a, float& b, int& c) {
#define RED3(ctl) "v_add_f32_dpp %0, %0, %0 " ctl "\n\tv_add_f32_dpp %1, %1, %1 " ctl "\n\tv_max_i32_dpp %2, %2, %2 " ctl "\n\t"
  asm("s_nop 1\n\t"
      RED3("row_shr:1 row_mask:0xf bank_mask:0xf bound_ctrl:1") RED3("row_shr:2 row_mask:0xf bank_mask:0xf bound_ctrl:1")
      RED3("row_shr:4 row_mask:0xf bank_mask:0xf bound_ctrl:1") RED3("row_shr:8 row_mask:0xf bank_mask:0xf bound_ctrl:1")
      RED3("row_bcast:15 row_mask:0xa bank_mask:0xf") RED3("row_bcast:31 row_mask:0xc bank_mask:0xf")
      "s_nop 0"
      : "+v"(a), "+v"(b), "+v"(c));
#undef RED3
}
DI void st128_dev(const char* base, u32 off, u32x4 v) {
  asm volatile("global_store_dwordx4 %0, %1, %2 sc1\n\ts_nop 1" :: "v"(off), "v"(v), "s"(base) : "memory");
}
DI void st128_w(const char* base, u32 off, u32x4 v) {
  st128_dev(base, off, v);
}
DI float lane63(float v) { return __builtin_bit_cast(float, __builtin_amdgcn_readlane(__builtin_bit_cast(int, v), 63)); }
DI f32x2 cmul(f32x2 a, f32x2 b) {
  f32x2 t, o;
  asm("v_pk_mul_f32 %0, %1, %2 op_sel_hi:[0,1]" : "=v"(t) : "v"(a), "v"(b));
  asm("v_pk_fma_f32 %0, %1, %2, %3 op_sel:[1,1,0] op_sel_hi:[1,0,1] neg_lo:[0,1,0]" : "=v"(o) : "v"(a), "v"(b), "v"(t));
  return o;
}
DI u32 finish_stats(float mean, float var) { return pack_bf(mean, __builtin_amdgcn_sqrtf(rbf(rbf(fmaxf(var, 0.f)) + 1e-5f))); }
DI float dot_bf(u32 a, u32 b, float c) {
  return __builtin_amdgcn_fdot2_f32_bf16(__builtin_bit_cast(bf16x2, a), __builtin_bit_cast(bf16x2, b), c, false);
}

constexpr int KG_ROW = 1040;
constexpr int KG_CH = 53 * KG_ROW;
constexpr int KC_STRIDE = 4160;
constexpr int KC_CH = KG_CH;
constexpr int SL_RED = 4 * KC_STRIDE;
constexpr int SL_U = 2 * KG_CH;
constexpr int U_CH = (256 + L) * 2;
constexpr int SL_X = SL_U + 2 * U_CH;
constexpr int XR_ROW = 912;
constexpr int SL_XX = SL_X + 8 * XR_ROW;
constexpr int SL_T = SL_XX + 2 * L * 2;
constexpr int SL_FLAG = SL_T + 769 * 4;
constexpr int SL_CNT = SL_T + 770 * 4;
constexpr int SL_W = SL_T + 772 * 4;
constexpr int SL_TOTAL = SL_W + 48 * 448;
static_assert(SL_RED + 4 * 2048 <= KG_CH && KG_CH % 16 == 0 && SL_W % 16 == 0 && SL_TOTAL <= 163840 && KG_CH + 768 < 65536, "lds map");

struct GatherTab { u32 a[256 * 12]; };
constexpr GatherTab make_gather_tab() {
  GatherTab t = {};
  for (int tc = 0; tc < 256; ++tc)
    for (int j = 0; j < 12; ++j) {
      const int i = 2047 - 8 * tc - j;
      int slot = 768;
      if (i >= 0) {
        const int s = i < 256 ? 0 : i < 512 ? 1 : i < 1024 ? 2 : 3;
        const int lag = s ? ((i + (1 << (s - 1)) - 1 + ((i >> s) & 1)) >> s) << s : i;
        const int lo = (lag & 255) >> 3, b = lag >> 8, r = lag & 7;
        const int row = b == 0 ? r : b == 1 ? 8 + (r >> 1) : b < 4 ? 12 + 2 * (b - 2) + (r >> 2) : 12 + b;
        slot = row * 32 + lo;
      }
      t.a[tc * 12 + j] = SL_T + 4 * slot;
    }
  return t;
}
static __device__ const GatherTab gather_tab = make_gather_tab();

DI s16x8 tr2(u32 a, int o0, int o1) {
  const s16x4 lo = __builtin_amdgcn_ds_read_tr16_b64_v4i16((LDS3 s16x4*)(uintptr_t)(a + o0));
  const s16x4 hi = __builtin_amdgcn_ds_read_tr16_b64_v4i16((LDS3 s16x4*)(uintptr_t)(a + o1));
  return __builtin_shufflevector(lo, hi, 0, 1, 2, 3, 4, 5, 6, 7);
}
template <int N> DI void wait_vm() { asm volatile("s_waitcnt vmcnt(%c0)" :: "i"(N) : "memory"); }
DI float dot_ones(s16x8 f, float s) {
  const u32x4 w4 = __builtin_bit_cast(u32x4, f);
#pragma unroll
  for (int i = 0; i < 4; ++i) s = dot_bf(w4[i], 0x3f803f80u, s);
  return s;
}
DI float half_sum(float v) {
  const u32 w = __builtin_bit_cast(u32, v);
  const auto r = __builtin_amdgcn_permlane32_swap(w, w, false, false);
  const u32 r0 = r[0], r1 = r[1];
  return __builtin_bit_cast(float, r0) + __builtin_bit_cast(float, r1);
}
DI void set_m0(u32 v) { asm volatile("s_mov_b32 m0, %0\n\ts_nop 0" :: "s"(v) : "memory"); }
template <int R0, int R1, int R2, int R3, int R4, int R5>
DI void put6(u32 v0, u32 v1, u32 v2, u32 v3, u32 v4, u32 v5) {
  asm volatile("ds_write_addtid_b32 %0 offset:%c6\n\tds_write_addtid_b32 %1 offset:%c7\n\tds_write_addtid_b32 %2 offset:%c8\n\t"
               "ds_write_addtid_b32 %3 offset:%c9\n\tds_write_addtid_b32 %4 offset:%c10\n\tds_write_addtid_b32 %5 offset:%c11"
               :: "v"(v0), "v"(v1), "v"(v2), "v"(v3), "v"(v4), "v"(v5),
                  "i"(R0 * KG_ROW), "i"(R1 * KG_ROW), "i"(R2 * KG_ROW), "i"(R3 * KG_ROW), "i"(R4 * KG_ROW), "i"(R5 * KG_ROW)
               : "memory");
}
template <int K, int RB>
DI void kg_step(f32x2& fa, f32x2& fb, f32x2& e0, f32x2& e1, f32x2& e2, f32x2& e3, f32x2 z, f32x2 w, f32x2 c) {
  f32x2 t0, t1, t2, t3, t4, t5;
  u32 h0, h1, h2, h3, h4, h5;
#define KG_M(t, a, b) "v_pk_mul_f32 " t ", " a ", " b " op_sel_hi:[0,1]\n\t"
#define KG_F(a, b, t) "v_pk_fma_f32 " a ", " a ", " b ", " t " op_sel:[1,1,0] op_sel_hi:[1,0,1] neg_lo:[0,1,0]\n\t"
  asm volatile(KG_M("%6", "%0", "%12") KG_M("%7", "%1", "%13") KG_M("%8", "%2", "%14") KG_M("%9", "%3", "%14") KG_M("%10", "%4", "%14") KG_M("%11", "%5", "%14")
               KG_F("%0", "%12", "%6") KG_F("%1", "%13", "%7") KG_F("%2", "%14", "%8") KG_F("%3", "%14", "%9") KG_F("%4", "%14", "%10") KG_F("%5", "%14", "%11")
               : "+v"(fa), "+v"(fb), "+v"(e0), "+v"(e1), "+v"(e2), "+v"(e3),
                 "=&v"(t0), "=&v"(t1), "=&v"(t2), "=&v"(t3), "=&v"(t4), "=&v"(t5)
               : "v"(z), "v"(w), "v"(c));
#undef KG_M
#undef KG_F
  h0 = pack_h(fa); h1 = pack_h(fb); h2 = pack_h(e0); h3 = pack_h(e1); h4 = pack_h(e2); h5 = pack_h(e3);
  put6<K, RB, 21 + 4 * K, 22 + 4 * K, 23 + 4 * K, 24 + 4 * K>(h0, h1, h2, h3, h4, h5);
}
DI void group_barrier(u32 cnt_addr, u32 target) {
  u32 one = 1, v, sv;
  asm volatile("s_waitcnt lgkmcnt(0)\n\t"
               "s_mov_b64 exec, 1\n\t"
               "ds_add_u32 %2, %3\n\t"
               "s_mov_b64 exec, -1\n"
               "1:\n\t"
               "ds_read_b32 %0, %2\n\t"
               "s_waitcnt lgkmcnt(0)\n\t"
               "v_readfirstlane_b32 %1, %0\n\t"
               "s_cmp_lt_u32 %1, %4\n\t"
               "s_cbranch_scc1 1b"
               : "=&v"(v), "=&s"(sv) : "v"(cnt_addr), "v"(one), "s"(target) : "memory", "scc");
}

#define MFMA16(a, b, c) __builtin_amdgcn_mfma_f32_16x16x32_bf16((a), (b), (c), 0, 0, 0)
#define MFMA32(a, b, c) __builtin_amdgcn_mfma_f32_32x32x16_bf16((a), (b), (c), 0, 0, 0)

template <int TG> DI constexpr int conv_tile(int i) { return TG == 0 ? (i == 0 ? 7 : i == 1 ? 5 : i == 2 ? 2 : 0) : (i == 0 ? 6 : i == 1 ? 4 : i == 2 ? 3 : 1); }

struct ConvSet { u32x2 al[8], ah[8]; s16x8 b[8]; };
template <int RR, int P> DI void conv_rd(ConvSet& s, u32 ka, u32 ub) {
  asm volatile("ds_read_b64 %0, %3 offset:%c5\n\tds_read_b64 %1, %3 offset:%c6\n\tds_read_b128 %2, %4 offset:%c7"
               : "=&v"(s.al[P]), "=&v"(s.ah[P]), "=&v"(s.b[P])
               : "v"(ka), "v"(ub), "i"(512 * (7 - P) + 64 * (3 - RR)), "i"(512 * (7 - P) + 64 * (3 - RR) + 8), "i"(512 * P + 64 * (3 - RR)));
}
DI s16x8 conv_a(const ConvSet& s, int p) {
  return __builtin_bit_cast(s16x8, __builtin_shufflevector(s.al[p], s.ah[p], 0, 1, 2, 3));
}
template <int TG, int RR, bool PRE> DI void conv_step(const ConvSet& c, ConvSet& n, u32 ka, u32 ub, f32x4 (&acc)[4]) {
  constexpr int NP = TG == 0 ? 8 : 7;
  asm volatile("s_waitcnt lgkmcnt(0)" ::: "memory");
  SCHED0();
#define CV_MM(i, p) acc[i] = MFMA16(conv_a(c, p), c.b[conv_tile<TG>(i) - (p)], acc[i])
#define CV_RD(p) do { if (PRE && (p) < NP) conv_rd<PRE ? RR + 1 : RR, (p)>(n, ka, ub); SCHED0(); } while (0)
  CV_MM(0, 0); CV_RD(0); CV_MM(1, 0); CV_RD(1);
  CV_MM(0, 1); CV_RD(2); CV_MM(1, 1); CV_RD(3);
  CV_MM(0, 2); CV_RD(4); CV_MM(1, 2); CV_RD(5);
  CV_MM(0, 3); CV_RD(6); CV_MM(1, 3); CV_RD(7);
  CV_MM(0, 4); CV_MM(1, 4); CV_MM(0, 5); CV_MM(2, 0); CV_MM(0, 6); CV_MM(2, 1); CV_MM(3, 0); CV_MM(2, 2);
  if (TG == 0) { CV_MM(0, 7); CV_MM(1, 5); } else { CV_MM(2, 3); CV_MM(3, 1); }
  SCHED0();
#undef CV_MM
#undef CV_RD
}
template <int TG> DI void conv_body(u32 ka, u32 ub, f32x4 (&acc)[4]) {
  ConvSet s0, s1;
  conv_rd<0, 0>(s0, ka, ub); conv_rd<0, 1>(s0, ka, ub); conv_rd<0, 2>(s0, ka, ub); conv_rd<0, 3>(s0, ka, ub);
  conv_rd<0, 4>(s0, ka, ub); conv_rd<0, 5>(s0, ka, ub); conv_rd<0, 6>(s0, ka, ub);
  if (TG == 0) conv_rd<0, 7>(s0, ka, ub);
  conv_step<TG, 0, true>(s0, s1, ka, ub, acc);
  conv_step<TG, 1, true>(s1, s0, ka, ub, acc);
  conv_step<TG, 2, true>(s0, s1, ka, ub, acc);
  conv_step<TG, 3, false>(s1, s0, ka, ub, acc);
}

__global__ __launch_bounds__(512, 2) void k_ssm(
    const u16* __restrict__ x, const u16* __restrict__ A_re, const u16* __restrict__ A_im,
    const u16* __restrict__ B_re, const u16* __restrict__ B_im, const u16* __restrict__ C_re,
    const u16* __restrict__ C_im, const u16* __restrict__ log_dt, const u16* __restrict__ Dskip,
    const u16* __restrict__ Wg, const u16* __restrict__ Wu, const u16* __restrict__ Wd,
    char* __restrict__ ws) {
  __shared__ __attribute__((aligned(16))) char lds[SL_TOTAL];
  STAMP_DECL;
  STAMP(0, 0);
  const int tid = threadIdx.x, lane = tid & 63;
  const int wave = __builtin_amdgcn_readfirstlane(tid >> 6);
  const int xcd = blockIdx.x & 7, idx = blockIdx.x >> 3;
  u32x4 cpf0, cpf1;
  {
    const GAS char* pc; u32 n1;
    asm volatile("s_getpc_b64 %0\n.Lssm_code_pc:\n\ts_mov_b32 %1, .Lffn2_code_end-.Lssm_code_pc-16" : "=s"(pc), "=s"(n1));
    cpf0 = *(const GAS u32x4*)(pc + (u32)(tid * 16));
    cpf1 = *(const GAS u32x4*)(pc + min(8192u + (idx & 3) * 8192u + tid * 16u, n1));
  }
  const bool chore = xcd == 7;
  const __amdgpu_buffer_rsrc_t rs = __builtin_amdgcn_make_buffer_rsrc((void*)ws, 0, (int)WS_END, 0x00020000);

  const int cp = chore ? 0 : xcd * 32 + idx;
  const int ch = wave >> 2, wq = wave & 3;
  const int d = 2 * cp + ch;

  const int row = blockIdx.x * 8 + wave;
  u32x4 xr = {0u, 0u, 0u, 0u};
  if (lane < 56) xr = *(const u32x4*)((const char*)(x + row * D) + lane * 16);
  const u32 epoch = __builtin_amdgcn_raw_buffer_load_b32(rs, 0, WS_EPOCH, 16);
  const int pb = d * NM + wq * 64;
  const u16 v_are = (A_re + pb)[lane], v_aim = (A_im + pb)[lane], v_bre = (B_re + pb)[lane], v_bim = (B_im + pb)[lane];
  const u16 v_cre = (C_re + pb)[lane], v_cim = (C_im + pb)[lane];
  const u16 v_ldt = log_dt[d], v_dsk = Dskip[d];
  const int tc = tid & 255;
  u32x4 ga0 = *(const u32x4*)(gather_tab.a + tc * 12), ga1 = *(const u32x4*)(gather_tab.a + tc * 12 + 4),
        ga2 = *(const u32x4*)(gather_tab.a + tc * 12 + 8);
  SCHED0();
  const bool tiler = idx < 28 && wave < 4;
  const int wu = (xcd & 1) * 28 + idx, wslice = 4 * (xcd >> 1) + wu / 14, wchunk = wu % 14, wv = xcd * 28 + idx;
  u32 wo_gu = lane * 16 + ((lane * 37) >> 9) * (F * 2 - 224), wo_dn = lane * 16 + (lane >= 28 ? 448 : 0);
  const char* wg_src = (const char*)Wg + (wchunk * 32 + wq * 8) * (F * 2) + wslice * 224;
  const char* wu_src = (const char*)Wu + (wchunk * 32 + wq * 8) * (F * 2) + wslice * 224 - 896;
  const char* wd_src = (const char*)Wd + ((wv >> 1) * 16 + wq * 4) * (D * 2) + (wv & 1) * 448;
  const char* wt_dst = ws + WS_WT + (wslice * 14 + wchunk) * 14 * 1024;
  const char* wd_dst = ws + WS_WD + ((wv & 1) * 7 * 112 + (wv >> 1)) * 1024;
  u32 cs_dst = WS_CS + (wslice * 14 + wchunk) * 896;
  const char* xg_dst = ws + WS_XG + blockIdx.x * 64;
  u32 xg_off = (tid >> 2) * (L * 8) + (tid & 3) * 16, xg_lds = lds_addr(lds) + SL_X + (tid & 3) * 2 * XR_ROW + (tid >> 2) * 4;
  u32 cv_ka, cv_ub, ep_lo;
  {
    const int b = lane & 15, g = lane >> 4, r3 = 8 * (wq >> 1) + 6;
    const u32 gg = 16 * (g & 1) - 32 * (g >> 1);
    cv_ka = lds_addr(lds) + ch * KC_CH + 3 * KC_STRIDE + (2044 - 16 * r3 - 256 * 7) * 2 - (b & 3) * KC_STRIDE - 8 * (b >> 2) + gg;
    cv_ub = lds_addr(lds) + SL_U + ch * U_CH + 512 - 32 * r3 + 32 * b + gg;
    ep_lo = 32 * b + 8 * g;
  }
  u32 cnt = lds_addr(lds) + SL_CNT + ch * 4;
  asm volatile("" : "+v"(wo_gu), "+v"(wo_dn), "+v"(xg_off), "+v"(xg_lds), "+v"(cv_ka), "+v"(cv_ub), "+v"(ep_lo), "+v"(cnt));
  asm volatile("" : "+s"(wg_src), "+s"(wu_src), "+s"(wd_src), "+s"(wt_dst), "+s"(wd_dst), "+s"(cs_dst), "+s"(xg_dst));
  if (tid == 0) *(u32x4*)(lds + SL_T + 768 * 4) = u32x4{0u, 0u, 0u, 0u};
  if (wq == 3) *(u32x2*)(lds + SL_U + ch * U_CH + lane * 8) = u32x2{0u, 0u};
  if (wave < 4) __builtin_amdgcn_s_setprio(1);
  SCHED0();

  *(u32x4*)(lds + SL_X + wave * XR_ROW + min(lane, 56) * 16) = xr;
  const u32 tag = next_tag(epoch);
  u32 pa = v_are, pi_ = v_aim, pbr = v_bre, pbi = v_bim, pcr = v_cre, pci = v_cim, pl = v_ldt, pd = v_dsk;
  asm volatile("" : "+v"(pa), "+v"(pi_), "+v"(pbr), "+v"(pbi), "+v"(pcr), "+v"(pci), "+v"(pl), "+v"(pd), "+v"(ga0), "+v"(ga1), "+v"(ga2));
  asm volatile("" :: "v"(cpf0), "v"(cpf1));
  __syncthreads();
  if (wave < 7) {
    const LDS3 u32* xr_l = (const LDS3 u32*)(uintptr_t)xg_lds;
    const u32 a0 = xr_l[0], a1 = xr_l[XR_ROW / 4], a2 = xr_l[112], a3 = xr_l[XR_ROW / 4 + 112];
    st128_dev(xg_dst, xg_off, u32x4{a0, tag, a1, tag});
    st128_dev(xg_dst + 112 * (L * 8), xg_off, u32x4{a2, tag, a3, tag});
  }
  STAMP(0, 1);
  f32x2 cb;
  float inv_sc, p2, rv;
  {
    float s = 0.f, q = 0.f;
#pragma unroll
    for (int i = 0; i < 4; ++i) { s = dot_bf(xr[i], 0x3f803f80u, s); q = dot_bf(xr[i], xr[i], q); }
    const float dt = rbf(__builtin_amdgcn_exp2f(bf_lo(pl) * 1.4426950408889634f));
    p2 = (dt * bf_lo(pa)) * 1.4426950408889634f;
    rv = (dt * bf_lo(pi_)) * 0.15915494309189535f;
    cb = cmul(f32x2{bf_lo(pcr), bf_lo(pci)}, f32x2{bf_lo(pbr), bf_lo(pbi)} * dt);
    const float cbx = cb.x, cby = cb.y;
    int mx = max(__builtin_bit_cast(int, cbx) & 0x7fffffff, __builtin_bit_cast(int, cby) & 0x7fffffff);
    wave_reduce3(s, q, mx);
    if (lane == 63) {
      const float mean = s * (1.0f / D);
      __builtin_amdgcn_raw_buffer_store_b64(u32x2{finish_stats(mean, q * (1.0f / D) - mean * mean), tag}, rs, 0, WS_SG + row * 8, 16);
    }
    u32 eb = (u32)__builtin_amdgcn_readlane(mx, 63) >> 23;
    asm("s_max_u32 %0, %0, 27\n\ts_min_u32 %0, %0, 227" : "+s"(eb) :: "scc");
    inv_sc = __builtin_bit_cast(float, (eb - 9) << 23);
    cb *= __builtin_bit_cast(float, (263 - eb) << 23);
  }
  if (tiler) {
    asm volatile("s_mov_b32 m0, %0\n\ts_mov_b64 exec, %9\n\t"
                 "global_load_lds_dwordx4 %2, %4\n\tglobal_load_lds_dwordx4 %2, %5 offset:896\n\t"
                 "global_load_lds_dwordx4 %2, %6 offset:1792\n\tglobal_load_lds_dwordx4 %2, %7 offset:2688\n\t"
                 "s_mov_b32 m0, %1\n\ts_nop 0\n\t"
                 "global_load_lds_dwordx4 %3, %8\n\tglobal_load_lds_dwordx4 %3, %10 offset:896\n\t"
                 "s_mov_b64 exec, -1"
                 :: "s"(lds_addr(lds) + SL_W + wq * 3584), "s"(lds_addr(lds) + SL_W + 32 * 448 + wq * 1792), "v"(wo_gu), "v"(wo_dn),
                    "s"(wg_src), "s"(wu_src), "s"(wg_src + 4 * (F * 2) - 1792), "s"(wu_src + 4 * (F * 2) - 1792), "s"(wd_src),
                    "s"(0x00ffffffffffffffull), "s"(wd_src + 2 * (D * 2) - 896)
                 : "memory");
  }
  auto tile_weights = [&]() {
    if (tiler) {
      const int hh = lane >> 5, blk = (lane >> 4) & 1, q = (lane & 15) >> 2, p = lane & 3;
      const u32 a = lds_addr(lds) + SL_W + wq * 64 + hh * 3584 + q * 224 + blk * 896 + 8 * p;
      const u32 b = lds_addr(lds) + SL_W + 32 * 448 + wq * 128 + (4 * hh + q) * 448 + blk * 32 + 8 * p;
#pragma unroll
      for (int i = 0; i < 2; ++i) {
        if (i == 0 || wq < 3) {
          const int t = 2 * wq + i;
          const s16x8 f0 = tr2(a + 32 * i, 0, 1792), f1 = tr2(a + 32 * i, 4 * 1792, 5 * 1792), fd = tr2(b + 64 * i, 0, 8 * 448);
          st128_w(wt_dst + t * 1024, lane * 16, __builtin_bit_cast(u32x4, f0));
          st128_w(wt_dst + (t + 7) * 1024, lane * 16, __builtin_bit_cast(u32x4, f1));
          st128_w(wd_dst + t * 112 * 1024, lane * 16, __builtin_bit_cast(u32x4, fd));
          const float cs = half_sum(dot_ones(f1, dot_ones(f0, 0.f)));
          if (lane < 32) __builtin_amdgcn_raw_buffer_store_b32(__builtin_bit_cast(u32, cs), rs, lane * 4, cs_dst + t * 128, 16);
        }
      }
    }
  };
  STAMP(0, 2);
  if (chore) {
    wait_vm<0>();
    __syncthreads();
    tile_weights();
    STAMP(0, 9);
    return;
  }

  {
    set_m0(lds_addr(lds) + ch * KG_CH + wq * 256);
    auto cpow = [&](float mp, float mr) {
      const float mag = __builtin_amdgcn_exp2f(p2 * mp);
      const float t = __builtin_amdgcn_fractf(rv * mr);
      return f32x2{__builtin_amdgcn_cosf(t), __builtin_amdgcn_sinf(t)} * mag;
    };
    const f32x2 z1 = cpow(1.f, 1.f), c8 = cpow(8.f, -8.f), w256 = cpow(256.f, 256.f);
    const f32x2 z2 = cmul(z1, z1), c16 = cmul(c8, c8);
    const f32x2 z4 = cmul(z2, z2), c32 = cmul(c16, c16);
    f32x2 e0 = f32x2{1.f, 0.f}, e1 = c8, e2 = c16, e3 = cmul(c16, c8);
    f32x2 fa = cb, fb = cmul(cb, w256);
    put6<0, 8, 21, 22, 23, 24>(pack_h(fa), pack_h(fb), 0x3c00u, pack_h(e1), pack_h(e2), pack_h(e3));
    const f32x2 g1 = cmul(fb, z2), g2 = cmul(fb, z4);
    kg_step<1, 12>(fa, fb, e0, e1, e2, e3, z1, w256, c32);
    const f32x2 g3 = cmul(g1, z4), g4 = cmul(fb, z4);
    kg_step<2, 14>(fa, fb, e0, e1, e2, e3, z1, w256, c32);
    const u32 h772 = pack_h(cmul(fb, z4));
    put6<9, 10, 11, 13, 15, 15>(pack_h(g1), pack_h(g2), pack_h(g3), pack_h(g4), h772, h772);
    kg_step<3, 16>(fa, fb, e0, e1, e2, e3, z1, w256, c32);
    kg_step<4, 17>(fa, fb, e0, e1, e2, e3, z1, w256, c32);
    kg_step<5, 18>(fa, fb, e0, e1, e2, e3, z1, w256, c32);
    kg_step<6, 19>(fa, fb, e0, e1, e2, e3, z1, w256, c32);
    kg_step<7, 20>(fa, fb, e0, e1, e2, e3, z1, w256, c32);
  }
  STAMP(0, 3);
  {
    char* kg = lds + ch * KG_CH + wq * 256;
    const char* fa = kg + (lane & 31) * KG_ROW + (lane >> 5) * 16;
    h16x8 a[8], b[8];
    asm volatile("s_waitcnt lgkmcnt(0)" ::: "memory");
#pragma unroll
    for (int s = 0; s < 8; ++s) { a[s] = *(const h16x8*)(fa + 32 * s); b[s] = *(const h16x8*)(fa + 21 * KG_ROW + 32 * s); }
    SCHED0();
    f32x16 acc;
#pragma unroll
    for (int i = 0; i < 16; ++i) acc[i] = 0.f;
#pragma unroll
    for (int s = 0; s < 8; ++s) acc = __builtin_amdgcn_mfma_f32_32x32x16_f16(a[s], b[s], acc, 0, 0, 0);
    char* pt = kg + (lane >> 4) * KG_ROW + (lane & 15) * 16;
#pragma unroll
    for (int q = 0; q < 3; ++q)
      *(f32x4*)(pt + 4 * q * KG_ROW) = f32x4{acc[4 * q], acc[4 * q + 1], acc[4 * q + 2], acc[4 * q + 3]} * inv_sc;
  }
  const u32 xo = WS_XG + cp * (L * 8);
  u32x4 gx[4], gs[4];
  auto poll = [&]() {
#pragma unroll
    for (int i = 0; i < 4; ++i) {
      gx[i] = __builtin_amdgcn_raw_buffer_load_b128(rs, tc * 16, xo + 4096 * i, 16);
      gs[i] = __builtin_amdgcn_raw_buffer_load_b128(rs, tc * 16, WS_SG + 4096 * i, 16);
    }
  };
  if (POLL_AT == 0) poll();
  STAMP(0, 4);
  group_barrier(cnt, 4);
  if (wq < 3) {
    const f32x4* pt = (const f32x4*)(lds + ch * KG_CH + 4 * wq * KG_ROW + (lane >> 4) * KG_ROW + (lane & 15) * 16);
    const f32x4 s = (pt[0] + pt[16]) + (pt[32] + pt[48]);
    const u32 p01 = pack_bf(s[0], s[1]), p23 = pack_bf(s[2], s[3]);
    u16* T = (u16*)(lds + SL_T + wq * 1024 + ch * 2 + (lane >> 5) * 512 + (lane & 31) * 4);
    T[0] = (u16)p01; T[64] = (u16)(p01 >> 16); T[128] = (u16)p23; T[192] = (u16)(p23 >> 16);
  }
  if (POLL_AT == 1) poll();
  group_barrier(cnt, 8);
  if (POLL_AT == 2) poll();
  const u32 sel = ch ? 0x07060302u : 0x05040100u;
  {
    u32 e[11];
#pragma unroll
    for (int j = 0; j < 11; ++j) {
      const u32 a = j < 4 ? ga0[j & 3] : j < 8 ? ga1[j & 3] : ga2[j & 3];
      e[j] = *(const LDS3 u32*)(uintptr_t)(a + lds_addr(lds));
    }
    char* kc = lds + ch * KC_CH + tc * 16;
#pragma unroll
    for (int s = 0; s < 4; ++s)
      *(u32x4*)(kc + s * KC_STRIDE) = u32x4{__builtin_amdgcn_perm(e[s + 1], e[s], sel), __builtin_amdgcn_perm(e[s + 3], e[s + 2], sel),
                                            __builtin_amdgcn_perm(e[s + 5], e[s + 4], sel), __builtin_amdgcn_perm(e[s + 7], e[s + 6], sel)};
    if (wq == 0 && lane < 8) *(u32x4*)(lds + ch * KC_CH + (lane >> 1) * KC_STRIDE + (256 + (lane & 1)) * 16) = u32x4{0u, 0u, 0u, 0u};
  }
  STAMP(0, 5);
  if (POLL_AT == 3) poll();
  {
    bool clean = true;
    for (u32 spins = 0;; ++spins) {
      u32 bad = 0;
#pragma unroll
      for (int i = 0; i < 4; ++i) bad |= ((gx[i][1] ^ tag) | (gx[i][3] ^ tag)) | ((gs[i][1] ^ tag) | (gs[i][3] ^ tag));
      if (__all(bad == 0u)) break;
      if (spins >= 4096u) { clean = false; break; }
      __builtin_amdgcn_s_sleep(4);
      asm volatile("" ::: "memory");
      poll();
    }
    u32 xs[8], st[8];
#pragma unroll
    for (int i = 0; i < 8; ++i) { xs[i] = gx[i >> 1][2 * (i & 1)]; st[i] = gs[i >> 1][2 * (i & 1)]; }
    if (!clean) {
      *(u32*)(lds + SL_FLAG) = 1u;
#pragma unroll 1
      for (int i = 0; i < 8; ++i) {
        const u32* xrow = (const u32*)(x + (512 * (i >> 1) + 2 * tc + (i & 1)) * D);
        float s = 0.f, q = 0.f;
#pragma unroll 1
        for (int j = 0; j < D / 2; ++j) { const u32 w = xrow[j]; s = dot_bf(w, 0x3f803f80u, s); q = dot_bf(w, w, q); }
        const float mean = s * (1.0f / D);
#pragma unroll
        for (int k = 0; k < 7; ++k) { st[k] = st[k + 1]; xs[k] = xs[k + 1]; }
        st[7] = finish_stats(mean, q * (1.0f / D) - mean * mean);
        xs[7] = xrow[cp];
      }
    }
    u32x4 xw, uw;
#pragma unroll
    for (int i = 0; i < 4; ++i) {
      xw[i] = __builtin_amdgcn_perm(xs[2 * i + 1], xs[2 * i], sel);
      const f32x2 u2 = (f32x2{bf_lo(xw[i]), bf_hi(xw[i])} - f32x2{bf_lo(st[2 * i]), bf_lo(st[2 * i + 1])}) *
                       f32x2{__builtin_amdgcn_rcpf(bf_hi(st[2 * i])), __builtin_amdgcn_rcpf(bf_hi(st[2 * i + 1]))};
      uw[i] = pack_bf(u2.x, u2.y);
    }
    u32* up = (u32*)(lds + SL_U + ch * U_CH + 512) + tc;
    u32* xp = (u32*)(lds + SL_XX + ch * L * 2) + tc;
#pragma unroll
    for (int i = 0; i < 4; ++i) { up[256 * i] = uw[i]; xp[256 * i] = xw[i]; }
  }
  STAMP(0, 6);
  wait_vm<0>();
  group_barrier(cnt, 12);
  if (blockIdx.x == 0 && tid == 0 && *(const u32*)(lds + SL_FLAG) == 0u) *(u32*)(ws + WS_EPOCH) = tag;
  if (TILE_AT == 0) tile_weights();
  STAMP(0, 7);
  const int tg = wq & 1, rh = wq >> 1;
  f32x4 acc[4];
#pragma unroll
  for (int i = 0; i < 4; ++i) acc[i] = f32x4{0.f, 0.f, 0.f, 0.f};
  if (tg == 0) conv_body<0>(cv_ka, cv_ub, acc);
  else conv_body<1>(cv_ka, cv_ub, acc);
  STAMP(0, 8);
  {
    f32x4* red = (f32x4*)(lds + ch * KG_CH + SL_RED) + lane;
    f32x4 y[2];
    if (rh) { red[wq * 128] = acc[0]; red[wq * 128 + 64] = acc[3]; y[0] = acc[1]; y[1] = acc[2]; }
    else { red[wq * 128] = acc[1]; red[wq * 128 + 64] = acc[2]; y[0] = acc[0]; y[1] = acc[3]; }
    const int t0 = rh ? 5 - tg : 7 - tg, t1 = rh ? 2 + tg : tg;
    const char* up = lds + SL_U + ch * U_CH + 512 + ep_lo;
    const char* xp = lds + SL_XX + ch * L * 2 + ep_lo;
    const u32x2 uu0 = *(const u32x2*)(up + 512 * t0), uu1 = *(const u32x2*)(up + 512 * t1);
    const u32x2 xx0 = *(const u32x2*)(xp + 512 * t0), xx1 = *(const u32x2*)(xp + 512 * t1);
    const float dsk = bf_lo(pd);
    f32x2 b0[2], b1[2];
#pragma unroll
    for (int j = 0; j < 2; ++j) {
      b0[j] = __builtin_elementwise_fma(f32x2{bf_lo(uu0[j]), bf_hi(uu0[j])}, f32x2{dsk, dsk}, f32x2{bf_lo(xx0[j]), bf_hi(xx0[j])}) + f32x2{y[0][2 * j], y[0][2 * j + 1]};
      b1[j] = __builtin_elementwise_fma(f32x2{bf_lo(uu1[j]), bf_hi(uu1[j])}, f32x2{dsk, dsk}, f32x2{bf_lo(xx1[j]), bf_hi(xx1[j])}) + f32x2{y[1][2 * j], y[1][2 * j + 1]};
    }
    asm volatile("" : "+v"(b0[0]), "+v"(b0[1]), "+v"(b1[0]), "+v"(b1[1]));
    group_barrier(cnt, 16);
    const f32x4 o0 = red[(wq ^ 2) * 128], o1 = red[(wq ^ 2) * 128 + 64];
    const u32 hb = WS_HT + d * 256, ho = (ep_lo & 255) + (ep_lo >> 8) * (D * 256);
    __builtin_amdgcn_raw_buffer_store_b64(u32x2{pack_bf(b0[0].x + o0[0], b0[0].y + o0[1]), pack_bf(b0[1].x + o0[2], b0[1].y + o0[3])}, rs, ho, hb + 2 * D * 256 * t0, 0);
    __builtin_amdgcn_raw_buffer_store_b64(u32x2{pack_bf(b1[0].x + o1[0], b1[0].y + o1[1]), pack_bf(b1[1].x + o1[2], b1[1].y + o1[3])}, rs, ho, hb + 2 * D * 256 * t1, 0);
  }
  __builtin_amdgcn_s_setprio(0);
  if (TILE_AT == 1) tile_weights();
  STAMP(0, 9);
}

constexpr int F1_H = 32 * 256;
constexpr int F1_STAGE = F1_H + 14 * 1024;
constexpr int F1_NST = 6;
constexpr int F1_CS = F1_NST * F1_STAGE;
constexpr int F1_ST = F1_CS + 7 * 256;
constexpr int F1_DUMP = F1_ST + 512;
constexpr int F1_TOTAL = F1_DUMP + 2048;
#define F1_PRE 3
#define F1_LASTB 11
#define F1_PF0 8
#define F1_UNR 2
#define F1_PFN 1
#define F1_G 1
static_assert(F1_PRE + F1_G <= F1_NST && F1_PRE >= F1_G && (F1_LASTB + 1) % F1_G == 0 && F1_PRE + F1_LASTB + 1 >= 14, "ring");
DI constexpr int f1_pf(int j) { return j >= F1_PF0 ? F1_G * F1_PFN : 0; }
DI constexpr int f1_pf_idx(int j) { int n = 0; for (int i = 0; i < j; ++i) n += f1_pf(i); return n; }
DI constexpr int f1_younger(int s, int c) {
  int n = s < F1_PRE ? (F1_PRE - 1 - s) * 6 : 0;
  for (int j = 0; j <= c; ++j) {
    for (int i = 0; i < F1_G; ++i) { const int t = F1_PRE + F1_G * j + i; if (t < 14 && t > s) n += 6; }
    if (F1_PRE + F1_G * j + F1_G - 1 >= s) n += f1_pf(j);
  }
  return n;
}
template <int J, class S, class X, class Y> DI void f1_loader_steps(S& stage, X& pf, Y& part, u64 (&fs_)[32]) {
  constexpr int C = F1_G * (J + 1) - 1;
  if constexpr (C <= F1_LASTB) {
#pragma unroll
    for (int i = 0; i < F1_G; ++i) if (F1_PRE + F1_G * J + i < 14) stage(F1_PRE + F1_G * J + i);
#pragma unroll
    for (int i = 0; i < f1_pf(J); ++i) pf(f1_pf_idx(J) + i < 13 ? f1_pf_idx(J) + i : 13);
    part(C - F1_G + 1, C);
    FST(2 * J + 2);
    wait_vm<f1_younger(C == F1_LASTB ? 13 : C + F1_G, J)>();
    FST(2 * J + 3);
    __builtin_amdgcn_s_barrier();
    asm volatile("" ::: "memory");
    f1_loader_steps<J + 1>(stage, pf, part, fs_);
  } else {
    part(C - F1_G + 1, 13);
    if (F1_PF0 < 12) {
#pragma unroll
      for (int i = f1_pf_idx(J); i < 14; ++i) pf(i);
    }
  }
}

struct F1Frag { s16x8 a[4], b[2]; };

__global__ __launch_bounds__(512, 2) void k_ffn1(char* __restrict__ ws) {
  __shared__ __attribute__((aligned(16))) char lds[F1_TOTAL];
  STAMP_DECL;
  STAMP(1, 0);
  u64 fs_[32] = {};
  FST(31);
  const int tid = threadIdx.x, lane = tid & 63;
  const int wave = __builtin_amdgcn_readfirstlane(tid >> 6);
  const int xcd = blockIdx.x & 7, bi = blockIdx.x >> 3;
  const int panel = (xcd & 1) * 8 + (bi & 7), slice = (xcd >> 1) * 4 + (bi >> 3);

  if (wave >= 4) {
    const int lw = wave - 4;
    const char* hb = ws + WS_HT + panel * (D * 256);
    const char* wb = ws + WS_WT + slice * (14 * 14 * 1024);
    const int r = lane >> 4;
    const u32 ho = (u32)((lw * 8 + r) * 256 + (((lane & 15) ^ (r << 2)) * 16));
    const u32 wo = lw * 3584 + lane * 16;
    const u32 lbase = lds_addr(lds) + lw * 2048;
    auto stage = [&](int c) {
      asm volatile(
          "s_mov_b32 m0, %0\n\t"
          "s_nop 0\n\t"
          "global_load_lds_dwordx4 %2, %4\n\t"
          "global_load_lds_dwordx4 %2, %4 offset:1024\n\t"
          "s_mov_b32 m0, %1\n\t"
          "s_nop 0\n\t"
          "global_load_lds_dwordx4 %3, %5\n\t"
          "global_load_lds_dwordx4 %3, %5 offset:1024\n\t"
          "global_load_lds_dwordx4 %3, %5 offset:2048\n\t"
          "s_mov_b64 exec, %6\n\t"
          "global_load_lds_dwordx4 %3, %5 offset:3072\n\t"
          "s_mov_b64 exec, -1"
          :: "s"(lbase + (c % F1_NST) * F1_STAGE), "s"(lbase + (c % F1_NST) * F1_STAGE + F1_H + lw * (3584 - 2048)),
             "v"(ho), "v"(wo), "s"(hb + c * 32 * 256), "s"(wb + c * 14 * 1024), "s"(0xffffffffull)
          : "memory");
    };
    auto pf = [&](int i) {
      asm volatile("s_mov_b32 m0, %0\n\ts_mov_b64 exec, %3\n\t"
                   "global_load_lds_dwordx4 %1, %2\n\t"
                   "s_mov_b64 exec, -1"
                   :: "s"(lds_addr(lds) + F1_DUMP), "v"(lane * 16), "s"(ws + WS_WD + bi * 50176 + lw * 12544 + i * 896), "s"(0x00ffffffffffffffull)
                   : "memory");
    };
    f32x2 psum = {0.f, 0.f};
    auto part = [&](int c0, int c1) {
      if (slice >= c0 && slice <= c1) {
        const int q = (lane & 15) >> 2;
        const u32 a = lds_addr(lds) + (slice % F1_NST) * F1_STAGE + (8 * (lane >> 5) + q) * 256 + ((lw ^ q) * 64) + ((lane >> 4) & 1) * 32 + 8 * (lane & 3);
        const u32x4 n0 = __builtin_bit_cast(u32x4, tr2(a, 0, 1024)), n1 = __builtin_bit_cast(u32x4, tr2(a, 4096, 5120));
        float s0 = 0.f, s1 = 0.f, q0 = 0.f, q1 = 0.f;
#pragma unroll
        for (int i = 0; i < 8; ++i) {
          const u32 w = i < 4 ? n0[i & 3] : n1[i & 3];
          const float lo = bf_lo(w), hi = bf_hi(w);
          s0 += lo; q0 = __builtin_fmaf(lo, lo, q0);
          s1 += hi; q1 = __builtin_fmaf(hi, hi, q1);
          asm("" : "+v"(s0)); asm("" : "+v"(q0)); asm("" : "+v"(s1)); asm("" : "+v"(q1));
        }
        psum = f32x2{s0 + s1, q0 + q1};
        asm volatile("" : "+v"(psum));
      }
    };
    float v[14];
    {
      const u32 co = min(tid - 256, 223) * 4;
      const char* cp = ws + WS_CS + slice * (14 * 896);
      asm volatile("global_load_dword %0, %14, %15\n\tglobal_load_dword %1, %14, %15 offset:896\n\tglobal_load_dword %2, %14, %15 offset:1792\n\t"
                   "global_load_dword %3, %14, %15 offset:2688\n\tglobal_load_dword %4, %14, %15 offset:3584\n\t"
                   "global_load_dword %5, %14, %16\n\tglobal_load_dword %6, %14, %16 offset:896\n\tglobal_load_dword %7, %14, %16 offset:1792\n\t"
                   "global_load_dword %8, %14, %16 offset:2688\n\tglobal_load_dword %9, %14, %16 offset:3584\n\t"
                   "global_load_dword %10, %14, %17\n\tglobal_load_dword %11, %14, %17 offset:896\n\tglobal_load_dword %12, %14, %17 offset:1792\n\t"
                   "global_load_dword %13, %14, %17 offset:2688"
                   : "=&v"(v[0]), "=&v"(v[1]), "=&v"(v[2]), "=&v"(v[3]), "=&v"(v[4]), "=&v"(v[5]), "=&v"(v[6]),
                     "=&v"(v[7]), "=&v"(v[8]), "=&v"(v[9]), "=&v"(v[10]), "=&v"(v[11]), "=&v"(v[12]), "=&v"(v[13])
                   : "v"(co), "s"(cp), "s"(cp + 4480), "s"(cp + 8960) : "memory");
    }
#pragma unroll
    for (int c = 0; c < F1_PRE; ++c) stage(c);
    FST(0);
    asm volatile("s_waitcnt vmcnt(%c14)"
                 : "+v"(v[0]), "+v"(v[1]), "+v"(v[2]), "+v"(v[3]), "+v"(v[4]), "+v"(v[5]), "+v"(v[6]),
                   "+v"(v[7]), "+v"(v[8]), "+v"(v[9]), "+v"(v[10]), "+v"(v[11]), "+v"(v[12]), "+v"(v[13])
                 : "i"((F1_PRE - F1_G) * 6) : "memory");
    FST(1);
    __builtin_amdgcn_s_barrier();
    asm volatile("" ::: "memory");
    f1_loader_steps<0>(stage, pf, part, fs_);
    FST_OUT();
    {
      const float csum = (((v[0] + v[1]) + (v[2] + v[3])) + ((v[4] + v[5]) + (v[6] + v[7]))) + (((v[8] + v[9]) + (v[10] + v[11])) + (v[12] + v[13]));
      const int i = tid - 256;
      u32* ca = (u32*)(lds + F1_CS) + (i >> 5) * 64 + (i & 31);
      if (i < 224) { ca[0] = pack_bf(csum, csum - rbf(csum)); ca[32] = 0u; }
    }
    __syncthreads();
    if (slice < 14) {
      const float a = half_sum(psum.x), b = half_sum(psum.y);
      if (lane < 32) *(f32x2*)(ws + WS_PS + ((panel * 14 + slice) * 128 + lw * 32 + lane) * 8) = f32x2{a, b};
    }
    wait_vm<0>();
    STAMP(1, 9);
    return;
  }

  const int th = wave >> 1, g = wave & 1;
  const int hh = lane >> 5, blk = (lane >> 4) & 1, q = (lane & 15) >> 2, p = lane & 3;
  const int town = 2 * th + g, toth = 2 * th + 1 - g;
  u32 bO = lds_addr(lds) + (8 * hh + q) * 256 + ((town ^ q) * 64) + blk * 32 + 8 * p;
  u32 bX = bO ^ 64;
  u32 bA = lds_addr(lds) + F1_H + lane * 16;
  u32 bG = bA + g * 3072;
  f32x16 acc[7];
#pragma unroll
  for (int t = 0; t < 7; ++t)
#pragma unroll
    for (int i = 0; i < 16; ++i) acc[t][i] = 0.f;
  float ss0 = 0.f, ss1 = 0.f;
  auto rd = [&](int s, F1Frag& f) {
    f.b[0] = tr2(bO, s * 4096, s * 4096 + 1024);
    f.a[0] = *(const LDS3 s16x8*)(uintptr_t)(bG + s * 7168);
    f.b[1] = tr2(bX, s * 4096, s * 4096 + 1024);
    f.a[1] = *(const LDS3 s16x8*)(uintptr_t)(bG + s * 7168 + 1024);
    f.a[2] = *(const LDS3 s16x8*)(uintptr_t)(bG + s * 7168 + 2048);
    f.a[3] = *(const LDS3 s16x8*)(uintptr_t)(bA + s * 7168 + 6144);
  };
  auto mm = [&](const F1Frag& f) {
    acc[0] = MFMA32(f.a[0], f.b[0], acc[0]);
    acc[3] = MFMA32(f.a[0], f.b[1], acc[3]);
    acc[1] = MFMA32(f.a[1], f.b[0], acc[1]);
    acc[4] = MFMA32(f.a[1], f.b[1], acc[4]);
    acc[2] = MFMA32(f.a[2], f.b[0], acc[2]);
    acc[5] = MFMA32(f.a[2], f.b[1], acc[5]);
    acc[6] = MFMA32(f.a[3], f.b[0], acc[6]);
    const u32x4 bw = __builtin_bit_cast(u32x4, f.b[0]);
#pragma unroll
    for (int i = 0; i < 4; ++i) {
      ss0 += bf_lo(bw[i]); ss1 += bf_hi(bw[i]);
      asm("" : "+v"(ss0)); asm("" : "+v"(ss1));
    }
  };
  auto interleave = [&](int extra) {
    __builtin_amdgcn_sched_group_barrier(0x8, 1, 0);
    __builtin_amdgcn_sched_group_barrier(0x100, 3, 0);
    __builtin_amdgcn_sched_group_barrier(0x2, 1, 0);
    __builtin_amdgcn_sched_group_barrier(0x8, 1, 0);
    __builtin_amdgcn_sched_group_barrier(0x100, 3, 0);
    __builtin_amdgcn_sched_group_barrier(0x2, 1, 0);
    __builtin_amdgcn_sched_group_barrier(0x8, 1, 0);
    __builtin_amdgcn_sched_group_barrier(0x100, 2, 0);
    __builtin_amdgcn_sched_group_barrier(0x2, 2, 0);
#pragma unroll
    for (int i = 0; i < 3; ++i) {
      __builtin_amdgcn_sched_group_barrier(0x8, 1, 0);
      if (extra) __builtin_amdgcn_sched_group_barrier(0x2, 5, 0); else __builtin_amdgcn_sched_group_barrier(0x2, 4, 0);
    }
    __builtin_amdgcn_sched_group_barrier(0x8, 1, 0);
    SCHED0();
  };
  F1Frag f0, f1;
  __builtin_amdgcn_s_barrier();
  asm volatile("" ::: "memory");
  STAMP(1, 1);
  rd(0, f0);
  SCHED0();
  static_assert(F1_NST == 6 && F1_LASTB == 11, "the loop below walks the ring twice, then two more chunks");
  auto chunk = [&](int step, bool bar, bool next) {
    rd(1, f1);
    mm(f0);
    bO += step; bX += step; bA += step; bG += step;
    interleave(1);
    asm volatile("" : "+v"(bO), "+v"(bX), "+v"(bA), "+v"(bG));
    if (bar) {
      asm volatile("s_waitcnt lgkmcnt(0)" ::: "memory");
      __builtin_amdgcn_s_barrier();
      asm volatile("" ::: "memory");
    }
    SCHED0();
    if (next) rd(0, f0);
    mm(f1);
    interleave(0);
  };
#pragma unroll F1_UNR
  for (int it = 0; it < 2; ++it) {
#pragma unroll
    for (int j = 0; j < 6; ++j) chunk(j == 5 ? -5 * F1_STAGE : F1_STAGE, (j & (F1_G - 1)) == F1_G - 1, true);
  }
  chunk(F1_STAGE, false, true);
  chunk(0, false, false);
  STAMP(1, 3);
  const float nmean = half_sum(ss0 + ss1) * (-1.0f / D);
  const u32 mo = pack_bf(nmean, nmean);
  if (lane < 32) *(u32*)(lds + F1_ST + (town * 32 + lane) * 4) = mo;
  __syncthreads();
  const u32 mx = *(const u32*)(lds + F1_ST + (toth * 32 + (lane & 31)) * 4);
  const u32* ca = (const u32*)(lds + F1_CS) + lane;
  const u32 c0 = ca[g * 192], c1 = ca[g * 192 + 64], c2 = ca[g * 192 + 128], c6 = ca[384];
#pragma unroll
  for (int t = 0; t < 7; ++t) {
    const u32 cw = t == 6 ? c6 : t % 3 == 0 ? c0 : t % 3 == 1 ? c1 : c2;
    acc[t] = MFMA32(__builtin_bit_cast(s16x8, u32x4{cw, 0u, 0u, 0u}), __builtin_bit_cast(s16x8, u32x4{(t >= 3 && t < 6) ? mx : mo, 0u, 0u, 0u}), acc[t]);
  }
  char* dsto = ws + WS_HF + ((panel * 4 + town) * 112 + slice * 7) * 1024;
  char* dstx = ws + WS_HF + ((panel * 4 + toth) * 112 + slice * 7) * 1024;
#pragma unroll
  for (int t = 0; t < 7; ++t) {
    u32x4 o;
#pragma unroll
    for (int j = 0; j < 4; ++j) {
      const f32x2 hv = f32x2{relu(acc[t][2 * j]), relu(acc[t][2 * j + 1])} * f32x2{acc[t][8 + 2 * j], acc[t][9 + 2 * j]};
      o[j] = pack_bf(hv.x, hv.y);
    }
    char* dst = (t >= 3 && t < 6) ? dstx : dsto;
    *(u32x4*)(dst + (u32)((t == 6 ? 6144 : g * 3072 + (t % 3) * 1024) + lane * 16)) = o;
  }
  STAMP(1, 9);
}

#define F2_WIN 5
constexpr int F2_AUX = 4 * 3 * 4096;

__global__ __launch_bounds__(256, 2) void k_ffn2(char* __restrict__ ws, u16* __restrict__ out) {
  __shared__ __attribute__((aligned(16))) char lds[F2_AUX + 4 * 6144];
  STAMP_DECL;
  STAMP(2, 0);
  const int tid = threadIdx.x, lane = tid & 63;
  const int wave = __builtin_amdgcn_readfirstlane(tid >> 6);
  const int xcd = blockIdx.x & 7, bi = blockIdx.x >> 3;
  const int tgp = (xcd & 1) * 16 + (xcd >> 1) * 4 + (bi & 3), dp = bi >> 2;
  const __amdgpu_buffer_rsrc_t rs = __builtin_amdgcn_make_buffer_rsrc((void*)ws, 0, (int)WS_END, 0x00020000);
  const u32 sa = WS_WD + (dp * 224 + wave * 28) * 1024, sb = WS_HF + (tgp * 224 + wave * 28) * 1024, vo = lane * 16;
  s16x8 ra[F2_WIN][2], rb[F2_WIN][2];
  auto ld = [&](int k) {
    const int s = k % F2_WIN, o = (k & 3) * 1024, q = (k >> 2) * 4096;
    ra[s][0] = __builtin_bit_cast(s16x8, __builtin_amdgcn_raw_buffer_load_b128(rs, vo + o, sa + q, 0));
    rb[s][0] = __builtin_bit_cast(s16x8, __builtin_amdgcn_raw_buffer_load_b128(rs, vo + o, sb + q, 0));
    ra[s][1] = __builtin_bit_cast(s16x8, __builtin_amdgcn_raw_buffer_load_b128(rs, vo + o, sa + q + 112 * 1024, 0));
    rb[s][1] = __builtin_bit_cast(s16x8, __builtin_amdgcn_raw_buffer_load_b128(rs, vo + o, sb + q + 112 * 1024, 0));
  };
#pragma unroll
  for (int k = 0; k < F2_WIN; ++k) { ld(k); SCHED0(); }
  const int tok0 = tgp * 64 + (wave & 1) * 32, d0 = dp * 64 + (wave >> 1) * 32;
  const u32 aux = lds_addr(lds) + F2_AUX + wave * 6144;
  {
    const char* hr = ws + WS_HT + (tok0 >> 7) * (D * 256) + d0 * 256 + (tok0 & 127) * 2;
    const char* pr = ws + WS_PS + (tok0 >> 7) * (14 * 1024) + (tok0 & 127) * 8;
    asm volatile("s_mov_b32 m0, %0\n\ts_nop 0\n\t"
                 "global_load_lds_dwordx4 %2, %4\n\tglobal_load_lds_dwordx4 %2, %5 offset:1024\n\t"
                 "global_load_lds_dwordx4 %3, %6 offset:2048\n\tglobal_load_lds_dwordx4 %3, %7 offset:3072\n\t"
                 "s_mov_b32 m0, %1\n\ts_nop 0\n\t"
                 "global_load_lds_dwordx4 %3, %8\n\t"
                 "s_mov_b64 exec, %10\n\t"
                 "global_load_lds_dwordx4 %3, %9 offset:1024\n\t"
                 "s_mov_b64 exec, -1"
                 :: "s"(aux), "s"(aux + 4096), "v"((lane >> 2) * 256 + (lane & 3) * 16), "v"((lane >> 4) * 1024 + (lane & 15) * 16),
                    "s"(hr), "s"(hr + 4096 - 1024), "s"(pr - 2048), "s"(pr + 4096 - 3072), "s"(pr + 8192), "s"(pr + 12288 - 1024), "s"(0xffffffffull)
                 : "memory");
  }
  SCHED0();
  f32x16 acc[4];
#pragma unroll
  for (int a = 0; a < 4; ++a)
#pragma unroll
    for (int i = 0; i < 16; ++i) acc[a][i] = 0.f;
  float rs2 = 0.f;
  f32x2 hres[8];
#pragma unroll
  for (int k = 0; k < 28; ++k) {
    const int s = k % F2_WIN;
#pragma unroll
    for (int a = 0; a < 4; ++a) acc[a] = MFMA32(ra[s][a >> 1], rb[s][a & 1], acc[a]);
    SCHED0();
    if (k + F2_WIN < 28) ld(k + F2_WIN);
    if (k == F2_WIN + 1) {
      asm volatile("" ::: "memory");
      const LDS3 f32x2* ps = (const LDS3 f32x2*)(uintptr_t)(aux + 2048 + (lane & 31) * 8);
      const f32x2 t = (((ps[0] + ps[32]) + (ps[64] + ps[96])) + ((ps[128] + ps[160]) + (ps[192] + ps[224]))) +
                      (((ps[256] + ps[288]) + (ps[320] + ps[352])) + (ps[384] + ps[416]));
      const float mean = t.x * (1.0f / D);
      const float var = fmaxf(t.y * (1.0f / D) - mean * mean, 0.f);
      const float r = __builtin_amdgcn_rcpf(rbf(__builtin_amdgcn_sqrtf(rbf(rbf(var) + 1e-5f))));
      rs2 = r * r;
      asm volatile("" : "+v"(rs2));
    }
    if (k == F2_WIN + 4) {
      const u32 a = aux + (4 * (lane >> 5) + ((lane & 15) >> 2)) * 64 + ((lane >> 4) & 1) * 32 + (lane & 3) * 8;
#pragma unroll
      for (int g = 0; g < 4; ++g) {
        const u32x2 w = __builtin_bit_cast(u32x2, __builtin_amdgcn_ds_read_tr16_b64_v4i16((LDS3 s16x4*)(uintptr_t)(a + g * 512)));
        hres[2 * g] = f32x2{bf_lo(w[0]), bf_hi(w[0])};
        hres[2 * g + 1] = f32x2{bf_lo(w[1]), bf_hi(w[1])};
        asm volatile("" : "+v"(hres[2 * g]), "+v"(hres[2 * g + 1]));
      }
    }
    SCHED0();
  }
  STAMP(2, 1);
  f32x16 mine;
#pragma unroll
  for (int a = 0; a < 4; ++a) {
    if (a == wave) { mine = acc[a]; }
    else {
      const int rank = wave > a ? wave - 1 : wave;
      f32x4* pp = (f32x4*)(lds + (a * 3 + rank) * 4096) + lane;
#pragma unroll
      for (int g = 0; g < 4; ++g) pp[g * 64] = f32x4{acc[a][4 * g], acc[a][4 * g + 1], acc[a][4 * g + 2], acc[a][4 * g + 3]};
    }
  }
  __syncthreads();
  u32 pk[8];
#pragma unroll
  for (int g = 0; g < 4; ++g) {
    const f32x4* pp = (const f32x4*)(lds + wave * 3 * 4096) + g * 64 + lane;
    const f32x4 s = (pp[0] + pp[256]) + (pp[512] + f32x4{mine[4 * g], mine[4 * g + 1], mine[4 * g + 2], mine[4 * g + 3]});
    const f32x2 o0 = __builtin_elementwise_fma(f32x2{s[0], s[1]}, f32x2{rs2, rs2}, hres[2 * g]);
    const f32x2 o1 = __builtin_elementwise_fma(f32x2{s[2], s[3]}, f32x2{rs2, rs2}, hres[2 * g + 1]);
    pk[2 * g] = pack_bf(o0.x, o0.y);
    pk[2 * g + 1] = pack_bf(o1.x, o1.y);
  }
  char* ob = (char*)out + tok0 * (D * 2) + d0 * 2;
#pragma unroll
  for (int k = 0; k < 2; ++k) {
    const auto r0 = __builtin_amdgcn_permlane32_swap(pk[4 * k], pk[4 * k + 2], false, false);
    const auto r1 = __builtin_amdgcn_permlane32_swap(pk[4 * k + 1], pk[4 * k + 3], false, false);
    *(u32x4*)(ob + (u32)((lane & 31) * (D * 2) + (lane >> 5) * 16 + 32 * k)) = u32x4{r0[0], r1[0], r0[1], r1[1]};
  }
  if (blockIdx.x == 0 && tid == 0) { u32* e = (u32*)(ws + WS_EPOCH); *e = next_tag(*e) ^ 0x5bd1e995u; }
  STAMP(2, 9);
  asm volatile(".Lffn2_code_end:");
}

extern "C" void kernel_launch(void* const* d_in, const int* in_sizes, int n_in,
                              void* d_out, int out_size, void* d_ws, size_t ws_size,
                              hipStream_t stream) {
  const u16* x = (const u16*)d_in[0];
  char* ws = (char*)d_ws;
  k_ssm<<<256, 512, 0, stream>>>(x, (const u16*)d_in[1], (const u16*)d_in[2], (const u16*)d_in[3], (const u16*)d_in[4],
                                 (const u16*)d_in[5], (const u16*)d_in[6], (const u16*)d_in[7], (const u16*)d_in[8],
                                 (const u16*)d_in[10], (const u16*)d_in[11], (const u16*)d_in[12], ws);
  k_ffn1<<<256, 512, 0, stream>>>(ws);
  k_ffn2<<<224, 256, 0, stream>>>(ws, (u16*)d_out);
}
```

```cpp
#include <hip/hip_runtime.h>
#include <hip/hip_bf16.h>
#include <stdint.h>
#define POLL_AUX 16

typedef unsigned short u16;
typedef unsigned int u32;
typedef unsigned long long u64;
typedef short s16x4 __attribute__((ext_vector_type(4)));
typedef short s16x8 __attribute__((ext_vector_type(8)));
typedef _Float16 h16x8 __attribute__((ext_vector_type(8)));
typedef _Float16 h16x2 __attribute__((ext_vector_type(2)));
typedef __bf16 bf16x2 __attribute__((ext_vector_type(2)));
typedef float f32x2 __attribute__((ext_vector_type(2)));
typedef float f32x4 __attribute__((ext_vector_type(4)));
typedef float f32x16 __attribute__((ext_vector_type(16)));
typedef u32 u32x2 __attribute__((ext_vector_type(2)));
typedef u32 u32x4 __attribute__((ext_vector_type(4)));
#define LDS3 __attribute__((address_space(3)))
#define GAS __attribute__((address_space(1)))
#define DI __device__ __forceinline__
#define SCHED0() __builtin_amdgcn_sched_barrier(0)

#define WT_AUX 16
#define TILE_AT 0
#define POLL_AT 2
constexpr int L = 2048, D = 448, NM = 256, F = 1792;

constexpr u32 WS_SG = 0;
constexpr u32 WS_EPOCH = 16384;
constexpr u32 WS_HT = 65536;
constexpr u32 WS_WD = WS_HT + D * L * 2;
constexpr u32 WS_HF = WS_WD + D * F * 2;
constexpr u32 WS_XG = WS_HF + L * F * 2;
constexpr u32 WS_WT = WS_XG + (D / 2) * L * 8;
constexpr u32 WS_CS = WS_WT + 2 * D * F * 2;
constexpr u32 WS_PS = WS_CS + 16 * 14 * 896;
constexpr u32 WS_END = WS_PS + 16 * 14 * 1024;

#define STAMP(kid, i) do {} while (0)
#define STAMP_DECL do {} while (0)

#define FST(i) do {} while (0)
#define FST_OUT() do {} while (0)
DI float bf_lo(u32 w) { return __builtin_bit_cast(float, w << 16); }
DI float bf_hi(u32 w) { return __builtin_bit_cast(float, w & 0xffff0000u); }
DI float bf1(u16 w) { return __builtin_bit_cast(float, (u32)w << 16); }
DI float rbf(float a) { return (float)(__bf16)a; }
DI u32 pack_bf(float a, float b) { bf16x2 v; v.x = (__bf16)a; v.y = (__bf16)b; return __builtin_bit_cast(u32, v); }
DI u32 pack_h(f32x2 a) { h16x2 v; v.x = (_Float16)a.x; v.y = (_Float16)a.y; return __builtin_bit_cast(u32, v); }
DI u32 lds_addr(const void* p) { return (u32)(uintptr_t)(const LDS3 void*)p; }
DI float relu(float v) { return __builtin_bit_cast(float, max(__builtin_bit_cast(int, v), 0)); }
DI u32 next_tag(u32 e) { return e * 2654435761u + 0x9E3779B9u; }

DI void wave_reduce3(float& a, float& b, int& c) {
#define RED3(ctl) "v_add_f32_dpp %0, %0, %0 " ctl "\n\tv_add_f32_dpp %1, %1, %1 " ctl "\n\tv_max_i32_dpp %2, %2, %2 " ctl "\n\t"
  asm("s_nop 1\n\t"
      RED3("row_shr:1 row_mask:0xf bank_mask:0xf bound_ctrl:1") RED3("row_shr:2 row_mask:0xf bank_mask:0xf bound_ctrl:1")
      RED3("row_shr:4 row_mask:0xf bank_mask:0xf bound_ctrl:1") RED3("row_shr:8 row_mask:0xf bank_mask:0xf bound_ctrl:1")
      RED3("row_bcast:15 row_mask:0xa bank_mask:0xf") RED3("row_bcast:31 row_mask:0xc bank_mask:0xf")
      "s_nop 0"
      : "+v"(a), "+v"(b), "+v"(c));
#undef RED3
}
DI void st128_dev(const char* base, u32 off, u32x4 v) {
  asm volatile("global_store_dwordx4 %0, %1, %2 sc1\n\ts_nop 1" :: "v"(off), "v"(v), "s"(base) : "memory");
}
DI void st128_w(const char* base, u32 off, u32x4 v) {
  st128_dev(base, off, v);
}
DI float lane63(float v) { return __builtin_bit_cast(float, __builtin_amdgcn_readlane(__builtin_bit_cast(int, v), 63)); }
DI f32x2 cmul(f32x2 a, f32x2 b) {
  f32x2 t, o;
  asm("v_pk_mul_f32 %0, %1, %2 op_sel_hi:[0,1]" : "=v"(t) : "v"(a), "v"(b));
  asm("v_pk_fma_f32 %0, %1, %2, %3 op_sel:[1,1,0] op_sel_hi:[1,0,1] neg_lo:[0,1,0]" : "=v"(o) : "v"(a), "v"(b), "v"(t));
  return o;
}
DI u32 finish_stats(float mean, float var) { return pack_bf(mean, __builtin_amdgcn_sqrtf(rbf(rbf(fmaxf(var, 0.f)) + 1e-5f))); }
DI float dot_bf(u32 a, u32 b, float c) {
  return __builtin_amdgcn_fdot2_f32_bf16(__builtin_bit_cast(bf16x2, a), __builtin_bit_cast(bf16x2, b), c, false);
}

constexpr int KG_ROW = 1040;
constexpr int KG_CH = 53 * KG_ROW;
constexpr int KC_STRIDE = 4160;
constexpr int KC_CH = KG_CH;
constexpr int SL_RED = 4 * KC_STRIDE;
constexpr int SL_U = 2 * KG_CH;
constexpr int U_CH = (256 + L) * 2;
constexpr int SL_X = SL_U + 2 * U_CH;
constexpr int XR_ROW = 912;
constexpr int SL_XX = SL_X + 8 * XR_ROW;
constexpr int SL_T = SL_XX + 2 * L * 2;
constexpr int SL_FLAG = SL_T + 769 * 4;
constexpr int SL_CNT = SL_T + 770 * 4;
constexpr int SL_W = SL_T + 772 * 4;
constexpr int SL_TOTAL = SL_W + 48 * 448;
static_assert(SL_RED + 4 * 2048 <= KG_CH && KG_CH % 16 == 0 && SL_W % 16 == 0 && SL_TOTAL <= 163840 && KG_CH + 768 < 65536, "lds map");

struct GatherTab { u32 a[256 * 12]; };
constexpr GatherTab make_gather_tab() {
  GatherTab t = {};
  for (int tc = 0; tc < 256; ++tc)
    for (int j = 0; j < 12; ++j) {
      const int i = 2047 - 8 * tc - j;
      int slot = 768;
      if (i >= 0) {
        const int s = i < 256 ? 0 : i < 512 ? 1 : i < 1024 ? 2 : 3;
        const int lag = s ? ((i + (1 << (s - 1)) - 1 + ((i >> s) & 1)) >> s) << s : i;
        const int lo = (lag & 255) >> 3, b = lag >> 8, r = lag & 7;
        const int row = b == 0 ? r : b == 1 ? 8 + (r >> 1) : b < 4 ? 12 + 2 * (b - 2) + (r >> 2) : 12 + b;
        slot = row * 32 + lo;
      }
      t.a[tc * 12 + j] = SL_T + 4 * slot;
    }
  return t;
}
static __device__ const GatherTab gather_tab = make_gather_tab();

DI s16x8 tr2(u32 a, int o0, int o1) {
  const s16x4 lo = __builtin_amdgcn_ds_read_tr16_b64_v4i16((LDS3 s16x4*)(uintptr_t)(a + o0));
  const s16x4 hi = __builtin_amdgcn_ds_read_tr16_b64_v4i16((LDS3 s16x4*)(uintptr_t)(a + o1));
  return __builtin_shufflevector(lo, hi, 0, 1, 2, 3, 4, 5, 6, 7);
}
template <int N> DI void wait_vm() { asm volatile("s_waitcnt vmcnt(%c0)" :: "i"(N) : "memory"); }
DI float dot_ones(s16x8 f, float s) {
  const u32x4 w4 = __builtin_bit_cast(u32x4, f);
#pragma unroll
  for (int i = 0; i < 4; ++i) s = dot_bf(w4[i], 0x3f803f80u, s);
  return s;
}
DI float half_sum(float v) {
  const u32 w = __builtin_bit_cast(u32, v);
  const auto r = __builtin_amdgcn_permlane32_swap(w, w, false, false);
  const u32 r0 = r[0], r1 = r[1];
  return __builtin_bit_cast(float, r0) + __builtin_bit_cast(float, r1);
}
DI void set_m0(u32 v) { asm volatile("s_mov_b32 m0, %0\n\ts_nop 0" :: "s"(v) : "memory"); }
template <int R0, int R1, int R2, int R3, int R4, int R5>
DI void put6(u32 v0, u32 v1, u32 v2, u32 v3, u32 v4, u32 v5) {
  asm volatile("ds_write_addtid_b32 %0 offset:%c6\n\tds_write_addtid_b32 %1 offset:%c7\n\tds_write_addtid_b32 %2 offset:%c8\n\t"
               "ds_write_addtid_b32 %3 offset:%c9\n\tds_write_addtid_b32 %4 offset:%c10\n\tds_write_addtid_b32 %5 offset:%c11"
               :: "v"(v0), "v"(v1), "v"(v2), "v"(v3), "v"(v4), "v"(v5),
                  "i"(R0 * KG_ROW), "i"(R1 * KG_ROW), "i"(R2 * KG_ROW), "i"(R3 * KG_ROW), "i"(R4 * KG_ROW), "i"(R5 * KG_ROW)
               : "memory");
}
template <int K, int RB>
DI void kg_step(f32x2& fa, f32x2& fb, f32x2& e0, f32x2& e1, f32x2& e2, f32x2& e3, f32x2 z, f32x2 w, f32x2 c) {
  f32x2 t0, t1, t2, t3, t4, t5;
  u32 h0, h1, h2, h3, h4, h5;
#define KG_M(t, a, b) "v_pk_mul_f32 " t ", " a ", " b " op_sel_hi:[0,1]\n\t"
#define KG_F(a, b, t) "v_pk_fma_f32 " a ", " a ", " b ", " t " op_sel:[1,1,0] op_sel_hi:[1,0,1] neg_lo:[0,1,0]\n\t"
  asm volatile(KG_M("%6", "%0", "%12") KG_M("%7", "%1", "%13") KG_M("%8", "%2", "%14") KG_M("%9", "%3", "%14") KG_M("%10", "%4", "%14") KG_M("%11", "%5", "%14")
               KG_F("%0", "%12", "%6") KG_F("%1", "%13", "%7") KG_F("%2", "%14", "%8") KG_F("%3", "%14", "%9") KG_F("%4", "%14", "%10") KG_F("%5", "%14", "%11")
               : "+v"(fa), "+v"(fb), "+v"(e0), "+v"(e1), "+v"(e2), "+v"(e3),
                 "=&v"(t0), "=&v"(t1), "=&v"(t2), "=&v"(t3), "=&v"(t4), "=&v"(t5)
               : "v"(z), "v"(w), "v"(c));
#undef KG_M
#undef KG_F
  h0 = pack_h(fa); h1 = pack_h(fb); h2 = pack_h(e0); h3 = pack_h(e1); h4 = pack_h(e2); h5 = pack_h(e3);
  put6<K, RB, 21 + 4 * K, 22 + 4 * K, 23 + 4 * K, 24 + 4 * K>(h0, h1, h2, h3, h4, h5);
}
DI void group_barrier(u32 cnt_addr, u32 target) {
  u32 one = 1, v, sv;
  asm volatile("s_waitcnt lgkmcnt(0)\n\t"
               "s_mov_b64 exec, 1\n\t"
               "ds_add_u32 %2, %3\n\t"
               "s_mov_b64 exec, -1\n"
               "1:\n\t"
               "ds_read_b32 %0, %2\n\t"
               "s_waitcnt lgkmcnt(0)\n\t"
               "v_readfirstlane_b32 %1, %0\n\t"
               "s_cmp_lt_u32 %1, %4\n\t"
               "s_cbranch_scc1 1b"
               : "=&v"(v), "=&s"(sv) : "v"(cnt_addr), "v"(one), "s"(target) : "memory", "scc");
}

#define MFMA16(a, b, c) __builtin_amdgcn_mfma_f32_16x16x32_bf16((a), (b), (c), 0, 0, 0)
#define MFMA32(a, b, c) __builtin_amdgcn_mfma_f32_32x32x16_bf16((a), (b), (c), 0, 0, 0)

template <int TG> DI constexpr int conv_tile(int i) { return TG == 0 ? (i == 0 ? 7 : i == 1 ? 5 : i == 2 ? 2 : 0) : (i == 0 ? 6 : i == 1 ? 4 : i == 2 ? 3 : 1); }

struct ConvSet { u32x2 al[8], ah[8]; s16x8 b[8]; };
template <int RR, int P> DI void conv_rd(ConvSet& s, u32 ka, u32 ub) {
  asm volatile("ds_read_b64 %0, %3 offset:%c5\n\tds_read_b64 %1, %3 offset:%c6\n\tds_read_b128 %2, %4 offset:%c7"
               : "=&v"(s.al[P]), "=&v"(s.ah[P]), "=&v"(s.b[P])
               : "v"(ka), "v"(ub), "i"(512 * (7 - P) + 64 * (3 - RR)), "i"(512 * (7 - P) + 64 * (3 - RR) + 8), "i"(512 * P + 64 * (3 - RR)));
}
DI s16x8 conv_a(const ConvSet& s, int p) {
  return __builtin_bit_cast(s16x8, __builtin_shufflevector(s.al[p], s.ah[p], 0, 1, 2, 3));
}
template <int TG, int RR, bool PRE> DI void conv_step(const ConvSet& c, ConvSet& n, u32 ka, u32 ub, f32x4 (&acc)[4]) {
  constexpr int NP = TG == 0 ? 8 : 7;
  asm volatile("s_waitcnt lgkmcnt(0)" ::: "memory");
  SCHED0();
#define CV_MM(i, p) acc[i] = MFMA16(conv_a(c, p), c.b[conv_tile<TG>(i) - (p)], acc[i])
#define CV_RD(p) do { if (PRE && (p) < NP) conv_rd<PRE ? RR + 1 : RR, (p)>(n, ka, ub); SCHED0(); } while (0)
  CV_MM(0, 0); CV_RD(0); CV_MM(1, 0); CV_RD(1);
  CV_MM(0, 1); CV_RD(2); CV_MM(1, 1); CV_RD(3);
  CV_MM(0, 2); CV_RD(4); CV_MM(1, 2); CV_RD(5);
  CV_MM(0, 3); CV_RD(6); CV_MM(1, 3); CV_RD(7);
  CV_MM(0, 4); CV_MM(1, 4); CV_MM(0, 5); CV_MM(2, 0); CV_MM(0, 6); CV_MM(2, 1); CV_MM(3, 0); CV_MM(2, 2);
  if (TG == 0) { CV_MM(0, 7); CV_MM(1, 5); } else { CV_MM(2, 3); CV_MM(3, 1); }
  SCHED0();
#undef CV_MM
#undef CV_RD
}
template <int TG> DI void conv_body(u32 ka, u32 ub, f32x4 (&acc)[4]) {
  ConvSet s0, s1;
  conv_rd<0, 0>(s0, ka, ub); conv_rd<0, 1>(s0, ka, ub); conv_rd<0, 2>(s0, ka, ub); conv_rd<0, 3>(s0, ka, ub);
  conv_rd<0, 4>(s0, ka, ub); conv_rd<0, 5>(s0, ka, ub); conv_rd<0, 6>(s0, ka, ub);
  if (TG == 0) conv_rd<0, 7>(s0, ka, ub);
  conv_step<TG, 0, true>(s0, s1, ka, ub, acc);
  conv_step<TG, 1, true>(s1, s0, ka, ub, acc);
  conv_step<TG, 2, true>(s0, s1, ka, ub, acc);
  conv_step<TG, 3, false>(s1, s0, ka, ub, acc);
}

__global__ __launch_bounds__(512, 2) void k_ssm(
    const u16* __restrict__ x, const u16* __restrict__ A_re, const u16* __restrict__ A_im,
    const u16* __restrict__ B_re, const u16* __restrict__ B_im, const u16* __restrict__ C_re,
    const u16* __restrict__ C_im, const u16* __restrict__ log_dt, const u16* __restrict__ Dskip,
    const u16* __restrict__ Wg, const u16* __restrict__ Wu, const u16* __restrict__ Wd,
    char* __restrict__ ws, const char* __restrict__ outp) {
  __shared__ __attribute__((aligned(16))) char lds[SL_TOTAL];
  STAMP_DECL;
  STAMP(0, 0);
  const int tid = threadIdx.x, lane = tid & 63;
  const int wave = __builtin_amdgcn_readfirstlane(tid >> 6);
  const int xcd = blockIdx.x & 7, idx = blockIdx.x >> 3;
  u32x4 cpf0, cpf1;
  {
    const GAS char* pc; u32 n1;
    asm volatile("s_getpc_b64 %0\n.Lssm_code_pc:\n\ts_mov_b32 %1, .Lffn2_code_end-.Lssm_code_pc-16" : "=s"(pc), "=s"(n1));
    cpf0 = *(const GAS u32x4*)(pc + (u32)(tid * 16));
    cpf1 = *(const GAS u32x4*)(pc + min(8192u + (idx & 3) * 8192u + tid * 16u, n1));
  }
  const bool chore = xcd == 7;
  const __amdgpu_buffer_rsrc_t rs = __builtin_amdgcn_make_buffer_rsrc((void*)ws, 0, (int)WS_END, 0x00020000);

  const int cp = chore ? 0 : xcd * 32 + idx;
  const int ch = wave >> 2, wq = wave & 3;
  const int d = 2 * cp + ch;

  const int row = blockIdx.x * 8 + wave;
  u32x4 xr = {0u, 0u, 0u, 0u};
  if (lane < 56) xr = *(const u32x4*)((const char*)(x + row * D) + lane * 16);
  const u32 epoch = __builtin_amdgcn_raw_buffer_load_b32(rs, 0, WS_EPOCH, 16);
  const int pb = d * NM + wq * 64;
  const u16 v_are = (A_re + pb)[lane], v_aim = (A_im + pb)[lane], v_bre = (B_re + pb)[lane], v_bim = (B_im + pb)[lane];
  const u16 v_cre = (C_re + pb)[lane], v_cim = (C_im + pb)[lane];
  const u16 v_ldt = log_dt[d], v_dsk = Dskip[d];
  const int tc = tid & 255;
  u32x4 ga0 = *(const u32x4*)(gather_tab.a + tc * 12), ga1 = *(const u32x4*)(gather_tab.a + tc * 12 + 4),
        ga2 = *(const u32x4*)(gather_tab.a + tc * 12 + 8);
  SCHED0();
  const bool tiler = idx < 28 && wave < 4;
  const int wu = (xcd & 1) * 28 + idx, wslice = 4 * (xcd >> 1) + wu / 14, wchunk = wu % 14, wv = xcd * 28 + idx;
  u32 wo_gu = lane * 16 + ((lane * 37) >> 9) * (F * 2 - 224), wo_dn = lane * 16 + (lane >= 28 ? 448 : 0);
  const char* wg_src = (const char*)Wg + (wchunk * 32 + wq * 8) * (F * 2) + wslice * 224;
  const char* wu_src = (const char*)Wu + (wchunk * 32 + wq * 8) * (F * 2) + wslice * 224 - 896;
  const char* wd_src = (const char*)Wd + ((wv >> 1) * 16 + wq * 4) * (D * 2) + (wv & 1) * 448;
  const char* wt_dst = ws + WS_WT + (wslice * 14 + wchunk) * 14 * 1024;
  const char* wd_dst = ws + WS_WD + ((wv & 1) * 7 * 112 + (wv >> 1)) * 1024;
  u32 cs_dst = WS_CS + (wslice * 14 + wchunk) * 896;
  const char* xg_dst = ws + WS_XG + blockIdx.x * 64;
  u32 xg_off = (tid >> 2) * (L * 8) + (tid & 3) * 16, xg_lds = lds_addr(lds) + SL_X + (tid & 3) * 2 * XR_ROW + (tid >> 2) * 4;
  u32 cv_ka, cv_ub, ep_lo;
  {
    const int b = lane & 15, g = lane >> 4, r3 = 8 * (wq >> 1) + 6;
    const u32 gg = 16 * (g & 1) - 32 * (g >> 1);
    cv_ka = lds_addr(lds) + ch * KC_CH + 3 * KC_STRIDE + (2044 - 16 * r3 - 256 * 7) * 2 - (b & 3) * KC_STRIDE - 8 * (b >> 2) + gg;
    cv_ub = lds_addr(lds) + SL_U + ch * U_CH + 512 - 32 * r3 + 32 * b + gg;
    ep_lo = 32 * b + 8 * g;
  }
  u32 cnt = lds_addr(lds) + SL_CNT + ch * 4;
  asm volatile("" : "+v"(wo_gu), "+v"(wo_dn), "+v"(xg_off), "+v"(xg_lds), "+v"(cv_ka), "+v"(cv_ub), "+v"(ep_lo), "+v"(cnt));
  asm volatile("" : "+s"(wg_src), "+s"(wu_src), "+s"(wd_src), "+s"(wt_dst), "+s"(wd_dst), "+s"(cs_dst), "+s"(xg_dst));
  if (tid == 0) *(u32x4*)(lds + SL_T + 768 * 4) = u32x4{0u, 0u, 0u, 0u};
  if (wq == 3) *(u32x2*)(lds + SL_U + ch * U_CH + lane * 8) = u32x2{0u, 0u};
  if (wave < 4) __builtin_amdgcn_s_setprio(1);
  SCHED0();

  *(u32x4*)(lds + SL_X + wave * XR_ROW + min(lane, 56) * 16) = xr;
  const u32 tag = next_tag(epoch);
  u32 pa = v_are, pi_ = v_aim, pbr = v_bre, pbi = v_bim, pcr = v_cre, pci = v_cim, pl = v_ldt, pd = v_dsk;
  asm volatile("" : "+v"(pa), "+v"(pi_), "+v"(pbr), "+v"(pbi), "+v"(pcr), "+v"(pci), "+v"(pl), "+v"(pd), "+v"(ga0), "+v"(ga1), "+v"(ga2));
  asm volatile("" :: "v"(cpf0), "v"(cpf1));
  __syncthreads();
  if (wave < 7) {
    const LDS3 u32* xr_l = (const LDS3 u32*)(uintptr_t)xg_lds;
    const u32 a0 = xr_l[0], a1 = xr_l[XR_ROW / 4], a2 = xr_l[112], a3 = xr_l[XR_ROW / 4 + 112];
    st128_dev(xg_dst, xg_off, u32x4{a0, tag, a1, tag});
    st128_dev(xg_dst + 112 * (L * 8), xg_off, u32x4{a2, tag, a3, tag});
  }
  STAMP(0, 1);
  f32x2 cb;
  float inv_sc, p2, rv;
  {
    float s = 0.f, q = 0.f;
#pragma unroll
    for (int i = 0; i < 4; ++i) { s = dot_bf(xr[i], 0x3f803f80u, s); q = dot_bf(xr[i], xr[i], q); }
    const float dt = rbf(__builtin_amdgcn_exp2f(bf_lo(pl) * 1.4426950408889634f));
    p2 = (dt * bf_lo(pa)) * 1.4426950408889634f;
    rv = (dt * bf_lo(pi_)) * 0.15915494309189535f;
    cb = cmul(f32x2{bf_lo(pcr), bf_lo(pci)}, f32x2{bf_lo(pbr), bf_lo(pbi)} * dt);
    const float cbx = cb.x, cby = cb.y;
    int mx = max(__builtin_bit_cast(int, cbx) & 0x7fffffff, __builtin_bit_cast(int, cby) & 0x7fffffff);
    wave_reduce3(s, q, mx);
    if (lane == 63) {
      const float mean = s * (1.0f / D);
      __builtin_amdgcn_raw_buffer_store_b64(u32x2{finish_stats(mean, q * (1.0f / D) - mean * mean), tag}, rs, 0, WS_SG + row * 8, 16);
    }
    u32 eb = (u32)__builtin_amdgcn_readlane(mx, 63) >> 23;
    asm("s_max_u32 %0, %0, 27\n\ts_min_u32 %0, %0, 227" : "+s"(eb) :: "scc");
    inv_sc = __builtin_bit_cast(float, (eb - 9) << 23);
    cb *= __builtin_bit_cast(float, (263 - eb) << 23);
  }
  if (tiler) {
    asm volatile("s_mov_b32 m0, %0\n\ts_mov_b64 exec, %9\n\t"
                 "global_load_lds_dwordx4 %2, %4\n\tglobal_load_lds_dwordx4 %2, %5 offset:896\n\t"
                 "global_load_lds_dwordx4 %2, %6 offset:1792\n\tglobal_load_lds_dwordx4 %2, %7 offset:2688\n\t"
                 "s_mov_b32 m0, %1\n\ts_nop 0\n\t"
                 "global_load_lds_dwordx4 %3, %8\n\tglobal_load_lds_dwordx4 %3, %10 offset:896\n\t"
                 "s_mov_b64 exec, -1"
                 :: "s"(lds_addr(lds) + SL_W + wq * 3584), "s"(lds_addr(lds) + SL_W + 32 * 448 + wq * 1792), "v"(wo_gu), "v"(wo_dn),
                    "s"(wg_src), "s"(wu_src), "s"(wg_src + 4 * (F * 2) - 1792), "s"(wu_src + 4 * (F * 2) - 1792), "s"(wd_src),
                    "s"(0x00ffffffffffffffull), "s"(wd_src + 2 * (D * 2) - 896)
                 : "memory");
  }
  auto tile_weights = [&]() {
    if (tiler) {
      const int hh = lane >> 5, blk = (lane >> 4) & 1, q = (lane & 15) >> 2, p = lane & 3;
      const u32 a = lds_addr(lds) + SL_W + wq * 64 + hh * 3584 + q * 224 + blk * 896 + 8 * p;
      const u32 b = lds_addr(lds) + SL_W + 32 * 448 + wq * 128 + (4 * hh + q) * 448 + blk * 32 + 8 * p;
#pragma unroll
      for (int i = 0; i < 2; ++i) {
        if (i == 0 || wq < 3) {
          const int t = 2 * wq + i;
          const s16x8 f0 = tr2(a + 32 * i, 0, 1792), f1 = tr2(a + 32 * i, 4 * 1792, 5 * 1792), fd = tr2(b + 64 * i, 0, 8 * 448);
          st128_w(wt_dst + t * 1024, lane * 16, __builtin_bit_cast(u32x4, f0));
          st128_w(wt_dst + (t + 7) * 1024, lane * 16, __builtin_bit_cast(u32x4, f1));
          st128_w(wd_dst + t * 112 * 1024, lane * 16, __builtin_bit_cast(u32x4, fd));
          const float cs = half_sum(dot_ones(f1, dot_ones(f0, 0.f)));
          if (lane < 32) __builtin_amdgcn_raw_buffer_store_b32(__builtin_bit_cast(u32, cs), rs, lane * 4, cs_dst + t * 128, 16);
        }
      }
    }
  };
  STAMP(0, 2);
  if (chore) {
    wait_vm<0>();
    __syncthreads();
    tile_weights();
    STAMP(0, 9);
    return;
  }

  {
    set_m0(lds_addr(lds) + ch * KG_CH + wq * 256);
    auto cpow = [&](float mp, float mr) {
      const float mag = __builtin_amdgcn_exp2f(p2 * mp);
      const float t = __builtin_amdgcn_fractf(rv * mr);
      return f32x2{__builtin_amdgcn_cosf(t), __builtin_amdgcn_sinf(t)} * mag;
    };
    const f32x2 z1 = cpow(1.f, 1.f), c8 = cpow(8.f, -8.f), w256 = cpow(256.f, 256.f);
    const f32x2 z2 = cmul(z1, z1), c16 = cmul(c8, c8);
    const f32x2 z4 = cmul(z2, z2), c32 = cmul(c16, c16);
    f32x2 e0 = f32x2{1.f, 0.f}, e1 = c8, e2 = c16, e3 = cmul(c16, c8);
    f32x2 fa = cb, fb = cmul(cb, w256);
    put6<0, 8, 21, 22, 23, 24>(pack_h(fa), pack_h(fb), 0x3c00u, pack_h(e1), pack_h(e2), pack_h(e3));
    const f32x2 g1 = cmul(fb, z2), g2 = cmul(fb, z4);
    kg_step<1, 12>(fa, fb, e0, e1, e2, e3, z1, w256, c32);
    const f32x2 g3 = cmul(g1, z4), g4 = cmul(fb, z4);
    kg_step<2, 14>(fa, fb, e0, e1, e2, e3, z1, w256, c32);
    const u32 h772 = pack_h(cmul(fb, z4));
    put6<9, 10, 11, 13, 15, 15>(pack_h(g1), pack_h(g2), pack_h(g3), pack_h(g4), h772, h772);
    kg_step<3, 16>(fa, fb, e0, e1, e2, e3, z1, w256, c32);
    kg_step<4, 17>(fa, fb, e0, e1, e2, e3, z1, w256, c32);
    kg_step<5, 18>(fa, fb, e0, e1, e2, e3, z1, w256, c32);
    kg_step<6, 19>(fa, fb, e0, e1, e2, e3, z1, w256, c32);
    kg_step<7, 20>(fa, fb, e0, e1, e2, e3, z1, w256, c32);
  }
  STAMP(0, 3);
  {
    char* kg = lds + ch * KG_CH + wq * 256;
    const char* fa = kg + (lane & 31) * KG_ROW + (lane >> 5) * 16;
    h16x8 a[8], b[8];
    asm volatile("s_waitcnt lgkmcnt(0)" ::: "memory");
#pragma unroll
    for (int s = 0; s < 8; ++s) { a[s] = *(const h16x8*)(fa + 32 * s); b[s] = *(const h16x8*)(fa + 21 * KG_ROW + 32 * s); }
    SCHED0();
    f32x16 acc;
#pragma unroll
    for (int i = 0; i < 16; ++i) acc[i] = 0.f;
#pragma unroll
    for (int s = 0; s < 8; ++s) acc = __builtin_amdgcn_mfma_f32_32x32x16_f16(a[s], b[s], acc, 0, 0, 0);
    char* pt = kg + (lane >> 4) * KG_ROW + (lane & 15) * 16;
#pragma unroll
    for (int q = 0; q < 3; ++q)
      *(f32x4*)(pt + 4 * q * KG_ROW) = f32x4{acc[4 * q], acc[4 * q + 1], acc[4 * q + 2], acc[4 * q + 3]} * inv_sc;
  }
  const u32 xo = WS_XG + cp * (L * 8);
  u32x4 gx[4], gs[4];
  auto repoll = [&]() {
#pragma unroll
    for (int i = 0; i < 4; ++i) {
      gx[i] = __builtin_amdgcn_raw_buffer_load_b128(rs, tc * 16, xo + 4096 * i, 16);
      gs[i] = __builtin_amdgcn_raw_buffer_load_b128(rs, tc * 16, WS_SG + 4096 * i, 16);
    }
  };
  auto poll = [&]() {
#pragma unroll
    for (int i = 0; i < 4; ++i) {
      gx[i] = __builtin_amdgcn_raw_buffer_load_b128(rs, tc * 16, xo + 4096 * i, POLL_AUX);
      gs[i] = __builtin_amdgcn_raw_buffer_load_b128(rs, tc * 16, WS_SG + 4096 * i, POLL_AUX);
    }
  };
  if (POLL_AT == 0) poll();
  STAMP(0, 4);
  group_barrier(cnt, 4);
  if (wq < 3) {
    const f32x4* pt = (const f32x4*)(lds + ch * KG_CH + 4 * wq * KG_ROW + (lane >> 4) * KG_ROW + (lane & 15) * 16);
    const f32x4 s = (pt[0] + pt[16]) + (pt[32] + pt[48]);
    const u32 p01 = pack_bf(s[0], s[1]), p23 = pack_bf(s[2], s[3]);
    u16* T = (u16*)(lds + SL_T + wq * 1024 + ch * 2 + (lane >> 5) * 512 + (lane & 31) * 4);
    T[0] = (u16)p01; T[64] = (u16)(p01 >> 16); T[128] = (u16)p23; T[192] = (u16)(p23 >> 16);
  }
  if (POLL_AT == 1) poll();
  group_barrier(cnt, 8);
  if (POLL_AT == 2) poll();
  const u32 sel = ch ? 0x07060302u : 0x05040100u;
  {
    u32 e[11];
#pragma unroll
    for (int j = 0; j < 11; ++j) {
      const u32 a = j < 4 ? ga0[j & 3] : j < 8 ? ga1[j & 3] : ga2[j & 3];
      e[j] = *(const LDS3 u32*)(uintptr_t)(a + lds_addr(lds));
    }
    char* kc = lds + ch * KC_CH + tc * 16;
#pragma unroll
    for (int s = 0; s < 4; ++s)
      *(u32x4*)(kc + s * KC_STRIDE) = u32x4{__builtin_amdgcn_perm(e[s + 1], e[s], sel), __builtin_amdgcn_perm(e[s + 3], e[s + 2], sel),
                                            __builtin_amdgcn_perm(e[s + 5], e[s + 4], sel), __builtin_amdgcn_perm(e[s + 7], e[s + 6], sel)};
    if (wq == 0 && lane < 8) *(u32x4*)(lds + ch * KC_CH + (lane >> 1) * KC_STRIDE + (256 + (lane & 1)) * 16) = u32x4{0u, 0u, 0u, 0u};
  }
  STAMP(0, 5);
  if (POLL_AT == 3) poll();
  {
    bool clean = true;
    for (u32 spins = 0;; ++spins) {
      u32 bad = 0;
#pragma unroll
      for (int i = 0; i < 4; ++i) bad |= ((gx[i][1] ^ tag) | (gx[i][3] ^ tag)) | ((gs[i][1] ^ tag) | (gs[i][3] ^ tag));
      if (__all(bad == 0u)) break;
      if (spins >= 4096u) { clean = false; break; }
      __builtin_amdgcn_s_sleep(4);
      asm volatile("" ::: "memory");
      repoll();
    }
    u32 xs[8], st[8];
#pragma unroll
    for (int i = 0; i < 8; ++i) { xs[i] = gx[i >> 1][2 * (i & 1)]; st[i] = gs[i >> 1][2 * (i & 1)]; }
    if (!clean) {
      *(u32*)(lds + SL_FLAG) = 1u;
#pragma unroll 1
      for (int i = 0; i < 8; ++i) {
        const u32* xrow = (const u32*)(x + (512 * (i >> 1) + 2 * tc + (i & 1)) * D);
        float s = 0.f, q = 0.f;
#pragma unroll 1
        for (int j = 0; j < D / 2; ++j) { const u32 w = xrow[j]; s = dot_bf(w, 0x3f803f80u, s); q = dot_bf(w, w, q); }
        const float mean = s * (1.0f / D);
#pragma unroll
        for (int k = 0; k < 7; ++k) { st[k] = st[k + 1]; xs[k] = xs[k + 1]; }
        st[7] = finish_stats(mean, q * (1.0f / D) - mean * mean);
        xs[7] = xrow[cp];
      }
    }
    u32x4 xw, uw;
#pragma unroll
    for (int i = 0; i < 4; ++i) {
      xw[i] = __builtin_amdgcn_perm(xs[2 * i + 1], xs[2 * i], sel);
      const f32x2 u2 = (f32x2{bf_lo(xw[i]), bf_hi(xw[i])} - f32x2{bf_lo(st[2 * i]), bf_lo(st[2 * i + 1])}) *
                       f32x2{__builtin_amdgcn_rcpf(bf_hi(st[2 * i])), __builtin_amdgcn_rcpf(bf_hi(st[2 * i + 1]))};
      uw[i] = pack_bf(u2.x, u2.y);
    }
    u32* up = (u32*)(lds + SL_U + ch * U_CH + 512) + tc;
    u32* xp = (u32*)(lds + SL_XX + ch * L * 2) + tc;
#pragma unroll
    for (int i = 0; i < 4; ++i) { up[256 * i] = uw[i]; xp[256 * i] = xw[i]; }
  }
  STAMP(0, 6);
  wait_vm<0>();
  group_barrier(cnt, 12);
  if (blockIdx.x == 0 && tid == 0 && *(const u32*)(lds + SL_FLAG) == 0u) *(u32*)(ws + WS_EPOCH) = tag;
  if (TILE_AT == 0) tile_weights();
  STAMP(0, 7);
  const int tg = wq & 1, rh = wq >> 1;
  f32x4 acc[4];
#pragma unroll
  for (int i = 0; i < 4; ++i) acc[i] = f32x4{0.f, 0.f, 0.f, 0.f};
  if (tg == 0) conv_body<0>(cv_ka, cv_ub, acc);
  else conv_body<1>(cv_ka, cv_ub, acc);
  STAMP(0, 8);
  {
    f32x4* red = (f32x4*)(lds + ch * KG_CH + SL_RED) + lane;
    f32x4 y[2];
    if (rh) { red[wq * 128] = acc[0]; red[wq * 128 + 64] = acc[3]; y[0] = acc[1]; y[1] = acc[2]; }
    else { red[wq * 128] = acc[1]; red[wq * 128 + 64] = acc[2]; y[0] = acc[0]; y[1] = acc[3]; }
    const int t0 = rh ? 5 - tg : 7 - tg, t1 = rh ? 2 + tg : tg;
    const char* up = lds + SL_U + ch * U_CH + 512 + ep_lo;
    const char* xp = lds + SL_XX + ch * L * 2 + ep_lo;
    const u32x2 uu0 = *(const u32x2*)(up + 512 * t0), uu1 = *(const u32x2*)(up + 512 * t1);
    const u32x2 xx0 = *(const u32x2*)(xp + 512 * t0), xx1 = *(const u32x2*)(xp + 512 * t1);
    const float dsk = bf_lo(pd);
    f32x2 b0[2], b1[2];
#pragma unroll
    for (int j = 0; j < 2; ++j) {
      b0[j] = __builtin_elementwise_fma(f32x2{bf_lo(uu0[j]), bf_hi(uu0[j])}, f32x2{dsk, dsk}, f32x2{bf_lo(xx0[j]), bf_hi(xx0[j])}) + f32x2{y[0][2 * j], y[0][2 * j + 1]};
      b1[j] = __builtin_elementwise_fma(f32x2{bf_lo(uu1[j]), bf_hi(uu1[j])}, f32x2{dsk, dsk}, f32x2{bf_lo(xx1[j]), bf_hi(xx1[j])}) + f32x2{y[1][2 * j], y[1][2 * j + 1]};
    }
    asm volatile("" : "+v"(b0[0]), "+v"(b0[1]), "+v"(b1[0]), "+v"(b1[1]));
    group_barrier(cnt, 16);
    const f32x4 o0 = red[(wq ^ 2) * 128], o1 = red[(wq ^ 2) * 128 + 64];
    const u32 hb = WS_HT + d * 256, ho = (ep_lo & 255) + (ep_lo >> 8) * (D * 256);
    __builtin_amdgcn_raw_buffer_store_b64(u32x2{pack_bf(b0[0].x + o0[0], b0[0].y + o0[1]), pack_bf(b0[1].x + o0[2], b0[1].y + o0[3])}, rs, ho, hb + 2 * D * 256 * t0, 0);
    __builtin_amdgcn_raw_buffer_store_b64(u32x2{pack_bf(b1[0].x + o1[0], b1[0].y + o1[1]), pack_bf(b1[1].x + o1[2], b1[1].y + o1[3])}, rs, ho, hb + 2 * D * 256 * t1, 0);
  }
  __builtin_amdgcn_s_setprio(0);
  if (TILE_AT == 1) tile_weights();
  STAMP(0, 9);
}

constexpr int F1_H = 32 * 256;
constexpr int F1_STAGE = F1_H + 14 * 1024;
constexpr int F1_NST = 6;
constexpr int F1_CS = F1_NST * F1_STAGE;
constexpr int F1_ST = F1_CS + 7 * 256;
constexpr int F1_FL = F1_ST + 512;
constexpr int F1_DUMP = F1_FL + 16;
constexpr int F1_TOTAL = F1_DUMP + 2048;
constexpr int F1_PRE = 3;
#define F1_PF0 5
#define F1_UNR 2

struct F1Frag { s16x8 a[4], b[2]; };

__global__ __launch_bounds__(512, 2) void k_ffn1(char* __restrict__ ws) {
  __shared__ __attribute__((aligned(16))) char lds[F1_TOTAL];
  STAMP_DECL;
  STAMP(1, 0);
  const int tid = threadIdx.x, lane = tid & 63;
  const int wave = __builtin_amdgcn_readfirstlane(tid >> 6);
  const int xcd = blockIdx.x & 7, bi = blockIdx.x >> 3;
  const int panel = (xcd & 1) * 8 + (bi & 7), slice = (xcd >> 1) * 4 + (bi >> 3);

  if (wave >= 4) {
    const int lw = wave - 4;
    const char* hb = ws + WS_HT + panel * (D * 256);
    const char* wb = ws + WS_WT + slice * (14 * 14 * 1024);
    const int r = lane >> 4;
    const u32 ho = (u32)((lw * 8 + r) * 256 + (((lane & 15) ^ (r << 2)) * 16));
    const u32 wo = lw * 3584 + lane * 16;
    const u32 lbase = lds_addr(lds) + lw * 2048;
    auto stage = [&](int c, int slot) {
      asm volatile(
          "s_mov_b32 m0, %0\n\t"
          "s_nop 0\n\t"
          "global_load_lds_dwordx4 %2, %4\n\t"
          "global_load_lds_dwordx4 %2, %4 offset:1024\n\t"
          "s_mov_b32 m0, %1\n\t"
          "s_nop 0\n\t"
          "global_load_lds_dwordx4 %3, %5\n\t"
          "global_load_lds_dwordx4 %3, %5 offset:1024\n\t"
          "global_load_lds_dwordx4 %3, %5 offset:2048\n\t"
          "s_mov_b64 exec, %6\n\t"
          "global_load_lds_dwordx4 %3, %5 offset:3072\n\t"
          "s_mov_b64 exec, -1"
          :: "s"(lbase + slot * F1_STAGE), "s"(lbase + slot * F1_STAGE + F1_H + lw * (3584 - 2048)),
             "v"(ho), "v"(wo), "s"(hb + c * 32 * 256), "s"(wb + c * 14 * 1024), "s"(0xffffffffull)
          : "memory");
    };
    stage(0, 0);
    SCHED0();
    auto pf = [&](int i) {
      asm volatile("s_mov_b32 m0, %0\n\ts_mov_b64 exec, %3\n\t"
                   "global_load_lds_dwordx4 %1, %2\n\t"
                   "s_mov_b64 exec, -1"
                   :: "s"(lds_addr(lds) + F1_DUMP), "v"(lane * 16), "s"(ws + WS_WD + bi * 50176 + lw * 12544 + i * 896), "s"(0x00ffffffffffffffull)
                   : "memory");
    };
    f32x2 psum = {0.f, 0.f};
    float v[14];
    auto part = [&]() {
      {
        const int q = (lane & 15) >> 2;
        const u32 a = lds_addr(lds) + (slice % F1_NST) * F1_STAGE + (8 * (lane >> 5) + q) * 256 + ((lw ^ q) * 64) + ((lane >> 4) & 1) * 32 + 8 * (lane & 3);
        const u32x4 n0 = __builtin_bit_cast(u32x4, tr2(a, 0, 1024)), n1 = __builtin_bit_cast(u32x4, tr2(a, 4096, 5120));
        float s0 = 0.f, s1 = 0.f, q0 = 0.f, q1 = 0.f;
#pragma unroll
        for (int i = 0; i < 8; ++i) {
          const u32 w = i < 4 ? n0[i & 3] : n1[i & 3];
          const float lo = bf_lo(w), hi = bf_hi(w);
          s0 += lo; q0 = __builtin_fmaf(lo, lo, q0);
          s1 += hi; q1 = __builtin_fmaf(hi, hi, q1);
          asm("" : "+v"(s0)); asm("" : "+v"(q0)); asm("" : "+v"(s1)); asm("" : "+v"(q1));
        }
        psum = f32x2{s0 + s1, q0 + q1};
        asm volatile("" : "+v"(psum));
      }
    };
    {
      const u32 co = min(tid - 256, 223) * 4;
      const char* cp = ws + WS_CS + slice * (14 * 896);
      asm volatile("global_load_dword %0, %14, %15\n\tglobal_load_dword %1, %14, %15 offset:896\n\tglobal_load_dword %2, %14, %15 offset:1792\n\t"
                   "global_load_dword %3, %14, %15 offset:2688\n\tglobal_load_dword %4, %14, %15 offset:3584\n\t"
                   "global_load_dword %5, %14, %16\n\tglobal_load_dword %6, %14, %16 offset:896\n\tglobal_load_dword %7, %14, %16 offset:1792\n\t"
                   "global_load_dword %8, %14, %16 offset:2688\n\tglobal_load_dword %9, %14, %16 offset:3584\n\t"
                   "global_load_dword %10, %14, %17\n\tglobal_load_dword %11, %14, %17 offset:896\n\tglobal_load_dword %12, %14, %17 offset:1792\n\t"
                   "global_load_dword %13, %14, %17 offset:2688"
                   : "=&v"(v[0]), "=&v"(v[1]), "=&v"(v[2]), "=&v"(v[3]), "=&v"(v[4]), "=&v"(v[5]), "=&v"(v[6]),
                     "=&v"(v[7]), "=&v"(v[8]), "=&v"(v[9]), "=&v"(v[10]), "=&v"(v[11]), "=&v"(v[12]), "=&v"(v[13])
                   : "v"(co), "s"(cp), "s"(cp + 4480), "s"(cp + 8960) : "memory");
    }
    stage(1, 1); stage(2, 2);
    wait_vm<2 * 6 + 14>();
    __builtin_amdgcn_s_barrier();
    asm volatile("" ::: "memory");
    int slot = F1_PRE;
#pragma unroll 1
    for (int j = 0; j < 11; ++j) {
      stage(j + F1_PRE, slot);
      slot = slot == F1_NST - 1 ? 0 : slot + 1;
      if (j >= F1_PF0) pf(j - F1_PF0);
      if (j == 1) {
        asm volatile("" : "+v"(v[0]), "+v"(v[1]), "+v"(v[2]), "+v"(v[3]), "+v"(v[4]), "+v"(v[5]), "+v"(v[6]),
                     "+v"(v[7]), "+v"(v[8]), "+v"(v[9]), "+v"(v[10]), "+v"(v[11]), "+v"(v[12]), "+v"(v[13]));
        const float csum = (((v[0] + v[1]) + (v[2] + v[3])) + ((v[4] + v[5]) + (v[6] + v[7]))) + (((v[8] + v[9]) + (v[10] + v[11])) + (v[12] + v[13]));
        const int i = tid - 256;
        u32* ca = (u32*)(lds + F1_CS) + (i >> 5) * 64 + (i & 31);
        if (i < 224) { ca[0] = pack_bf(csum, csum - rbf(csum)); ca[32] = 0u; }
      }
      if (j == slice) part();
      asm volatile("s_waitcnt vmcnt(12) lgkmcnt(0)" ::: "memory");
      __builtin_amdgcn_s_barrier();
      asm volatile("" ::: "memory");
    }
    wait_vm<F1_PF0 <= 10 ? 1 : 0>();
    __builtin_amdgcn_s_barrier();
    asm volatile("" ::: "memory");
    if (slice >= 11 && slice < 14) part();
#pragma unroll
    for (int i = F1_PF0 <= 10 ? 11 - F1_PF0 : 14; i < 14; ++i) pf(i);
    if (slice < 14) {
      const float a = half_sum(psum.x), b = half_sum(psum.y);
      if (lane < 32) *(f32x2*)(ws + WS_PS + ((panel * 14 + slice) * 128 + lw * 32 + lane) * 8) = f32x2{a, b};
    }
    wait_vm<0>();
    STAMP(1, 9);
    return;
  }

  const int th = wave >> 1, g = wave & 1;
  const int hh = lane >> 5, blk = (lane >> 4) & 1, q = (lane & 15) >> 2, p = lane & 3;
  const int town = 2 * th + g, toth = 2 * th + 1 - g;
  u32 bO = lds_addr(lds) + (8 * hh + q) * 256 + ((town ^ q) * 64) + blk * 32 + 8 * p;
  u32 bX = bO ^ 64;
  u32 bA = lds_addr(lds) + F1_H + lane * 16;
  u32 bG = bA + g * 3072;
  f32x16 acc[7];
#pragma unroll
  for (int t = 0; t < 7; ++t)
#pragma unroll
    for (int i = 0; i < 16; ++i) acc[t][i] = 0.f;
  float ss0 = 0.f, ss1 = 0.f;
  auto rd = [&](int s, F1Frag& f) {
    f.b[0] = tr2(bO, s * 4096, s * 4096 + 1024);
    f.a[0] = *(const LDS3 s16x8*)(uintptr_t)(bG + s * 7168);
    f.b[1] = tr2(bX, s * 4096, s * 4096 + 1024);
    f.a[1] = *(const LDS3 s16x8*)(uintptr_t)(bG + s * 7168 + 1024);
    f.a[2] = *(const LDS3 s16x8*)(uintptr_t)(bG + s * 7168 + 2048);
    f.a[3] = *(const LDS3 s16x8*)(uintptr_t)(bA + s * 7168 + 6144);
  };
  auto msum = [&](const F1Frag& f) {
    const u32x4 bw = __builtin_bit_cast(u32x4, f.b[0]);
#pragma unroll
    for (int i = 0; i < 4; ++i) {
      ss0 += bf_lo(bw[i]); ss1 += bf_hi(bw[i]);
      asm("" : "+v"(ss0)); asm("" : "+v"(ss1));
    }
  };
  auto mm = [&](const F1Frag& f) {
    acc[0] = MFMA32(f.a[0], f.b[0], acc[0]);
    acc[3] = MFMA32(f.a[0], f.b[1], acc[3]);
    acc[1] = MFMA32(f.a[1], f.b[0], acc[1]);
    acc[4] = MFMA32(f.a[1], f.b[1], acc[4]);
    acc[2] = MFMA32(f.a[2], f.b[0], acc[2]);
    acc[5] = MFMA32(f.a[2], f.b[1], acc[5]);
    acc[6] = MFMA32(f.a[3], f.b[0], acc[6]);
    msum(f);
  };
  auto interleave = [&](int extra) {
    __builtin_amdgcn_sched_group_barrier(0x8, 1, 0);
    __builtin_amdgcn_sched_group_barrier(0x100, 3, 0);
    __builtin_amdgcn_sched_group_barrier(0x2, 1, 0);
    __builtin_amdgcn_sched_group_barrier(0x8, 1, 0);
    __builtin_amdgcn_sched_group_barrier(0x100, 3, 0);
    __builtin_amdgcn_sched_group_barrier(0x2, 1, 0);
    __builtin_amdgcn_sched_group_barrier(0x8, 1, 0);
    __builtin_amdgcn_sched_group_barrier(0x100, 2, 0);
    __builtin_amdgcn_sched_group_barrier(0x2, 2, 0);
#pragma unroll
    for (int i = 0; i < 3; ++i) {
      __builtin_amdgcn_sched_group_barrier(0x8, 1, 0);
      if (extra) __builtin_amdgcn_sched_group_barrier(0x2, 5, 0); else __builtin_amdgcn_sched_group_barrier(0x2, 4, 0);
    }
    __builtin_amdgcn_sched_group_barrier(0x8, 1, 0);
    SCHED0();
  };
  F1Frag f0, f1;
  if (lane == 0) *(u32*)(lds + F1_FL + wave * 4) = 0u;
  asm volatile("s_waitcnt lgkmcnt(0)" ::: "memory");
  __builtin_amdgcn_s_barrier();
  asm volatile("" ::: "memory");
  STAMP(1, 1);
  rd(0, f0);
  SCHED0();
  static_assert(F1_NST == 6, "the loop below walks the ring twice, then two more chunks");
  auto chunk = [&](int step, bool bar, bool next) {
    rd(1, f1);
    mm(f0);
    bO += step; bX += step; bA += step; bG += step;
    interleave(1);
    asm volatile("" : "+v"(bO), "+v"(bX), "+v"(bA), "+v"(bG));
    if (bar) {
      asm volatile("s_waitcnt lgkmcnt(0)" ::: "memory");
      __builtin_amdgcn_s_barrier();
      asm volatile("" ::: "memory");
    }
    SCHED0();
    if (next) rd(0, f0);
    mm(f1);
    interleave(0);
  };
#pragma unroll F1_UNR
  for (int it = 0; it < 2; ++it) {
#pragma unroll
    for (int j = 0; j < 6; ++j) chunk(j == 5 ? -5 * F1_STAGE : F1_STAGE, true, true);
  }
  chunk(F1_STAGE, false, true);
  STAMP(1, 3);
  rd(1, f1);
  const u32* ca = (const u32*)(lds + F1_CS) + lane;
  const u32 c0 = ca[g * 192], c1 = ca[g * 192 + 64], c2 = ca[g * 192 + 128], c6 = ca[384];
  msum(f0); msum(f1);
  const float nmean = half_sum(ss0 + ss1) * (-1.0f / D);
  const u32 mo = pack_bf(nmean, nmean);
  if (lane < 32) *(u32*)(lds + F1_ST + (town * 32 + lane) * 4) = mo;
  asm volatile("" ::: "memory");
  *(u32*)(lds + F1_FL + town * 4) = 1u;
  char* dsto = ws + WS_HF + ((panel * 4 + town) * 112 + slice * 7) * 1024;
  char* dstx = ws + WS_HF + ((panel * 4 + toth) * 112 + slice * 7) * 1024;
  auto fin = [&](int t, u32 mw) {
    const int ai = t == 6 ? 3 : t % 3, bi2 = (t >= 3 && t < 6) ? 1 : 0;
    const u32 cw = t == 6 ? c6 : t % 3 == 0 ? c0 : t % 3 == 1 ? c1 : c2;
    acc[t] = MFMA32(f0.a[ai], f0.b[bi2], acc[t]);
    acc[t] = MFMA32(f1.a[ai], f1.b[bi2], acc[t]);
    acc[t] = MFMA32(__builtin_bit_cast(s16x8, u32x4{cw, 0u, 0u, 0u}), __builtin_bit_cast(s16x8, u32x4{mw, 0u, 0u, 0u}), acc[t]);
  };
  auto put = [&](int t) {
    u32x4 o;
#pragma unroll
    for (int j = 0; j < 4; ++j) {
      const float h0 = relu(acc[t][2 * j]) * acc[t][8 + 2 * j], h1 = relu(acc[t][2 * j + 1]) * acc[t][9 + 2 * j];
      float a0 = h0, a1 = h1;
      asm("" : "+v"(a0)); asm("" : "+v"(a1));
      o[j] = pack_bf(a0, a1);
    }
    char* dst = (t >= 3 && t < 6) ? dstx : dsto;
    *(u32x4*)(dst + (u32)((t == 6 ? 6144 : g * 3072 + (t % 3) * 1024) + lane * 16)) = o;
  };
  auto mix = [&]() {
#pragma unroll
    for (int i = 0; i < 3; ++i) {
      __builtin_amdgcn_sched_group_barrier(0x8, 1, 0);
      __builtin_amdgcn_sched_group_barrier(0x2, 7, 0);
    }
    SCHED0();
  };
  SCHED0();
  fin(0, mo); fin(1, mo);
  SCHED0();
  fin(2, mo); put(0); mix();
  u32 mx;
  {
    u32 fl, sv;
    asm volatile("1:\n\tds_read_b32 %1, %3\n\tds_read_b32 %0, %4\n\ts_waitcnt lgkmcnt(0)\n\t"
                 "v_readfirstlane_b32 %2, %1\n\ts_cmp_eq_u32 %2, 0\n\ts_cbranch_scc1 1b"
                 : "=&v"(mx), "=&v"(fl), "=&s"(sv)
                 : "v"(lds_addr(lds) + F1_FL + toth * 4), "v"(lds_addr(lds) + F1_ST + (toth * 32 + (lane & 31)) * 4) : "memory", "scc");
  }
  SCHED0();
  fin(6, mo); put(1); mix();
  fin(3, mx); put(2); mix();
  fin(4, mx); put(6); mix();
  fin(5, mx); put(3); mix();
  put(4); put(5);
  STAMP(1, 9);
}

#define F2_WIN 5
constexpr int F2_AUX = 4 * 3 * 4096;

__global__ __launch_bounds__(256, 2) void k_ffn2(char* __restrict__ ws, u16* __restrict__ out) {
  __shared__ __attribute__((aligned(16))) char lds[F2_AUX + 4 * 6144];
  STAMP_DECL;
  STAMP(2, 0);
  const int tid = threadIdx.x, lane = tid & 63;
  const int wave = __builtin_amdgcn_readfirstlane(tid >> 6);
  const int xcd = blockIdx.x & 7, bi = blockIdx.x >> 3;
  const int tgp = (xcd & 1) * 16 + (xcd >> 1) * 4 + (bi & 3), dp = bi >> 2;
  const __amdgpu_buffer_rsrc_t rs = __builtin_amdgcn_make_buffer_rsrc((void*)ws, 0, (int)WS_END, 0x00020000);
  const u32 sa = WS_WD + (dp * 224 + wave * 28) * 1024, sb = WS_HF + (tgp * 224 + wave * 28) * 1024, vo = lane * 16;
  s16x8 ra[F2_WIN][2], rb[F2_WIN][2];
  auto ld = [&](int k) {
    const int s = k % F2_WIN, o = (k & 3) * 1024, q = (k >> 2) * 4096;
    ra[s][0] = __builtin_bit_cast(s16x8, __builtin_amdgcn_raw_buffer_load_b128(rs, vo + o, sa + q, 0));
    rb[s][0] = __builtin_bit_cast(s16x8, __builtin_amdgcn_raw_buffer_load_b128(rs, vo + o, sb + q, 0));
    ra[s][1] = __builtin_bit_cast(s16x8, __builtin_amdgcn_raw_buffer_load_b128(rs, vo + o, sa + q + 112 * 1024, 0));
    rb[s][1] = __builtin_bit_cast(s16x8, __builtin_amdgcn_raw_buffer_load_b128(rs, vo + o, sb + q + 112 * 1024, 0));
  };
#pragma unroll
  for (int k = 0; k < F2_WIN; ++k) { ld(k); SCHED0(); }
  const int tok0 = tgp * 64 + (wave & 1) * 32, d0 = dp * 64 + (wave >> 1) * 32;
  const u32 aux = lds_addr(lds) + F2_AUX + wave * 6144;
  {
    const char* hr = ws + WS_HT + (tok0 >> 7) * (D * 256) + d0 * 256 + (tok0 & 127) * 2;
    const char* pr = ws + WS_PS + (tok0 >> 7) * (14 * 1024) + (tok0 & 127) * 8;
    asm volatile("s_mov_b32 m0, %0\n\ts_nop 0\n\t"
                 "global_load_lds_dwordx4 %2, %4\n\tglobal_load_lds_dwordx4 %2, %5 offset:1024\n\t"
                 "global_load_lds_dwordx4 %3, %6 offset:2048\n\tglobal_load_lds_dwordx4 %3, %7 offset:3072\n\t"
                 "s_mov_b32 m0, %1\n\ts_nop 0\n\t"
                 "global_load_lds_dwordx4 %3, %8\n\t"
                 "s_mov_b64 exec, %10\n\t"
                 "global_load_lds_dwordx4 %3, %9 offset:1024\n\t"
                 "s_mov_b64 exec, -1"
                 :: "s"(aux), "s"(aux + 4096), "v"((lane >> 2) * 256 + (lane & 3) * 16), "v"((lane >> 4) * 1024 + (lane & 15) * 16),
                    "s"(hr), "s"(hr + 4096 - 1024), "s"(pr - 2048), "s"(pr + 4096 - 3072), "s"(pr + 8192), "s"(pr + 12288 - 1024), "s"(0xffffffffull)
                 : "memory");
  }
  SCHED0();
  f32x16 acc[4];
#pragma unroll
  for (int a = 0; a < 4; ++a)
#pragma unroll
    for (int i = 0; i < 16; ++i) acc[a][i] = 0.f;
  float rs2 = 0.f;
  f32x2 hres[8];
#pragma unroll
  for (int k = 0; k < 28; ++k) {
    const int s = k % F2_WIN;
#pragma unroll
    for (int a = 0; a < 4; ++a) acc[a] = MFMA32(ra[s][a >> 1], rb[s][a & 1], acc[a]);
    SCHED0();
    if (k + F2_WIN < 28) ld(k + F2_WIN);
    if (k == F2_WIN + 1) {
      asm volatile("" ::: "memory");
      const LDS3 f32x2* ps = (const LDS3 f32x2*)(uintptr_t)(aux + 2048 + (lane & 31) * 8);
      const f32x2 t = (((ps[0] + ps[32]) + (ps[64] + ps[96])) + ((ps[128] + ps[160]) + (ps[192] + ps[224]))) +
                      (((ps[256] + ps[288]) + (ps[320] + ps[352])) + (ps[384] + ps[416]));
      const float mean = t.x * (1.0f / D);
      const float var = fmaxf(t.y * (1.0f / D) - mean * mean, 0.f);
      const float r = __builtin_amdgcn_rcpf(rbf(__builtin_amdgcn_sqrtf(rbf(rbf(var) + 1e-5f))));
      rs2 = r * r;
      asm volatile("" : "+v"(rs2));
    }
    if (k == F2_WIN + 4) {
      const u32 a = aux + (4 * (lane >> 5) + ((lane & 15) >> 2)) * 64 + ((lane >> 4) & 1) * 32 + (lane & 3) * 8;
#pragma unroll
      for (int g = 0; g < 4; ++g) {
        const u32x2 w = __builtin_bit_cast(u32x2, __builtin_amdgcn_ds_read_tr16_b64_v4i16((LDS3 s16x4*)(uintptr_t)(a + g * 512)));
        hres[2 * g] = f32x2{bf_lo(w[0]), bf_hi(w[0])};
        hres[2 * g + 1] = f32x2{bf_lo(w[1]), bf_hi(w[1])};
        asm volatile("" : "+v"(hres[2 * g]), "+v"(hres[2 * g + 1]));
      }
    }
    SCHED0();
  }
  STAMP(2, 1);
  f32x16 mine;
#pragma unroll
  for (int a = 0; a < 4; ++a) {
    if (a == wave) { mine = acc[a]; }
    else {
      const int rank = wave > a ? wave - 1 : wave;
      f32x4* pp = (f32x4*)(lds + (a * 3 + rank) * 4096) + lane;
#pragma unroll
      for (int g = 0; g < 4; ++g) pp[g * 64] = f32x4{acc[a][4 * g], acc[a][4 * g + 1], acc[a][4 * g + 2], acc[a][4 * g + 3]};
    }
  }
  __syncthreads();
  u32 pk[8];
#pragma unroll
  for (int g = 0; g < 4; ++g) {
    const f32x4* pp = (const f32x4*)(lds + wave * 3 * 4096) + g * 64 + lane;
    const f32x4 s = (pp[0] + pp[256]) + (pp[512] + f32x4{mine[4 * g], mine[4 * g + 1], mine[4 * g + 2], mine[4 * g + 3]});
    const f32x2 o0 = __builtin_elementwise_fma(f32x2{s[0], s[1]}, f32x2{rs2, rs2}, hres[2 * g]);
    const f32x2 o1 = __builtin_elementwise_fma(f32x2{s[2], s[3]}, f32x2{rs2, rs2}, hres[2 * g + 1]);
    pk[2 * g] = pack_bf(o0.x, o0.y);
    pk[2 * g + 1] = pack_bf(o1.x, o1.y);
  }
  char* ob = (char*)out + tok0 * (D * 2) + d0 * 2;
#pragma unroll
  for (int k = 0; k < 2; ++k) {
    const auto r0 = __builtin_amdgcn_permlane32_swap(pk[4 * k], pk[4 * k + 2], false, false);
    const auto r1 = __builtin_amdgcn_permlane32_swap(pk[4 * k + 1], pk[4 * k + 3], false, false);
    *(u32x4*)(ob + (u32)((lane & 31) * (D * 2) + (lane >> 5) * 16 + 32 * k)) = u32x4{r0[0], r1[0], r0[1], r1[1]};
  }
  if (blockIdx.x == 0 && tid == 0) { u32* e = (u32*)(ws + WS_EPOCH); *e = next_tag(*e) ^ 0x5bd1e995u; }
  STAMP(2, 9);
  asm volatile(".Lffn2_code_end:");
}

extern "C" void kernel_launch(void* const* d_in, const int* in_sizes, int n_in,
                              void* d_out, int out_size, void* d_ws, size_t ws_size,
                              hipStream_t stream) {
  const u16* x = (const u16*)d_in[0];
  char* ws = (char*)d_ws;
  k_ssm<<<256, 512, 0, stream>>>(x, (const u16*)d_in[1], (const u16*)d_in[2], (const u16*)d_in[3], (const u16*)d_in[4],
                                 (const u16*)d_in[5], (const u16*)d_in[6], (const u16*)d_in[7], (const u16*)d_in[8],
                                 (const u16*)d_in[10], (const u16*)d_in[11], (const u16*)d_in[12], ws, (const char*)d_out);
  k_ffn1<<<256, 512, 0, stream>>>(ws);
  k_ffn2<<<224, 256, 0, stream>>>(ws, (u16*)d_out);
}
```

```cpp
#include <hip/hip_runtime.h>
#include <hip/hip_bf16.h>
#include <stdint.h>

typedef unsigned short u16;
typedef unsigned int u32;
typedef unsigned long long u64;
typedef short s16x4 __attribute__((ext_vector_type(4)));
typedef short s16x8 __attribute__((ext_vector_type(8)));
typedef _Float16 h16x8 __attribute__((ext_vector_type(8)));
typedef _Float16 h16x2 __attribute__((ext_vector_type(2)));
typedef __bf16 bf16x2 __attribute__((ext_vector_type(2)));
typedef float f32x2 __attribute__((ext_vector_type(2)));
typedef float f32x4 __attribute__((ext_vector_type(4)));
typedef float f32x16 __attribute__((ext_vector_type(16)));
typedef u32 u32x2 __attribute__((ext_vector_type(2)));
typedef u32 u32x4 __attribute__((ext_vector_type(4)));
#define LDS3 __attribute__((address_space(3)))
#define GAS __attribute__((address_space(1)))
#define DI __device__ __forceinline__
#define SCHED0() __builtin_amdgcn_sched_barrier(0)

#define WT_AUX 16
#define POLL_SLEEP 4
#define POLL_AUX 16
constexpr int L = 2048, D = 448, NM = 256, F = 1792;

constexpr u32 WS_SG = 0;
constexpr u32 WS_EPOCH = 16384;
constexpr u32 WS_CNT = 20480;
constexpr u32 WS_HT = 65536;
constexpr u32 WS_WD = WS_HT + D * L * 2;
constexpr u32 WS_HF = WS_WD + D * F * 2;
constexpr u32 WS_XG = WS_HF + L * F * 2;
constexpr u32 WS_WT = WS_XG + (D / 2) * L * 8;
constexpr u32 WS_CS = WS_WT + 2 * D * F * 2;
constexpr u32 WS_PS = WS_CS + 16 * 14 * 896;
constexpr u32 WS_END = WS_PS + 16 * 14 * 1024;

#define STAMP(kid, i) do {} while (0)
#define STAMP_DECL do {} while (0)

#define FST(i) do {} while (0)
#define FST_OUT() do {} while (0)
DI float bf_lo(u32 w) { return __builtin_bit_cast(float, w << 16); }
DI float bf_hi(u32 w) { return __builtin_bit_cast(float, w & 0xffff0000u); }
DI float bf1(u16 w) { return __builtin_bit_cast(float, (u32)w << 16); }
DI float rbf(float a) { return (float)(__bf16)a; }
DI u32 pack_bf(float a, float b) { bf16x2 v; v.x = (__bf16)a; v.y = (__bf16)b; return __builtin_bit_cast(u32, v); }
DI u32 pack_h(f32x2 a) { h16x2 v; v.x = (_Float16)a.x; v.y = (_Float16)a.y; return __builtin_bit_cast(u32, v); }
DI u32 lds_addr(const void* p) { return (u32)(uintptr_t)(const LDS3 void*)p; }
DI float relu(float v) { return __builtin_bit_cast(float, max(__builtin_bit_cast(int, v), 0)); }
DI u32 next_tag(u32 e) { return e * 2654435761u + 0x9E3779B9u; }

DI int wave_imax(int c) {
#define RED1(ctl) "s_nop 1\n\tv_max_i32_dpp %0, %0, %0 " ctl "\n\t"
  asm(RED1("row_shr:1 row_mask:0xf bank_mask:0xf bound_ctrl:1") RED1("row_shr:2 row_mask:0xf bank_mask:0xf bound_ctrl:1")
      RED1("row_shr:4 row_mask:0xf bank_mask:0xf bound_ctrl:1") RED1("row_shr:8 row_mask:0xf bank_mask:0xf bound_ctrl:1")
      RED1("row_bcast:15 row_mask:0xa bank_mask:0xf") RED1("row_bcast:31 row_mask:0xc bank_mask:0xf")
      "s_nop 0"
      : "+v"(c));
#undef RED1
  return __builtin_amdgcn_readlane(c, 63);
}
DI void oct_sum2(float& a, float& b) {
#define RED2(ctl) "s_nop 0\n\tv_add_f32_dpp %0, %0, %0 " ctl " row_mask:0xf bank_mask:0xf\n\tv_add_f32_dpp %1, %1, %1 " ctl " row_mask:0xf bank_mask:0xf\n\t"
  asm("s_nop 0\n\t" RED2("quad_perm:[1,0,3,2]") RED2("quad_perm:[2,3,0,1]") RED2("row_half_mirror") "s_nop 0" : "+v"(a), "+v"(b));
#undef RED2
}
DI void st128_dev(const char* base, u32 off, u32x4 v) {
  asm volatile("global_store_dwordx4 %0, %1, %2 sc1\n\ts_nop 1" :: "v"(off), "v"(v), "s"(base) : "memory");
}
DI float lane63(float v) { return __builtin_bit_cast(float, __builtin_amdgcn_readlane(__builtin_bit_cast(int, v), 63)); }
DI f32x2 cmul(f32x2 a, f32x2 b) {
  f32x2 t, o;
  asm("v_pk_mul_f32 %0, %1, %2 op_sel_hi:[0,1]" : "=v"(t) : "v"(a), "v"(b));
  asm("v_pk_fma_f32 %0, %1, %2, %3 op_sel:[1,1,0] op_sel_hi:[1,0,1] neg_lo:[0,1,0]" : "=v"(o) : "v"(a), "v"(b), "v"(t));
  return o;
}
DI u32 finish_stats(float mean, float var) { return pack_bf(mean, __builtin_amdgcn_sqrtf(rbf(rbf(fmaxf(var, 0.f)) + 1e-5f))); }
DI float dot_bf(u32 a, u32 b, float c) {
  return __builtin_amdgcn_fdot2_f32_bf16(__builtin_bit_cast(bf16x2, a), __builtin_bit_cast(bf16x2, b), c, false);
}

constexpr int KG_ROW = 1040;
constexpr int KG_CH = 53 * KG_ROW;
constexpr int KC_STRIDE = 4160;
constexpr int KC_CH = KG_CH;
constexpr int SL_RED = 4 * KC_STRIDE;
constexpr int SL_U = 2 * KG_CH;
constexpr int U_CH = (256 + L) * 2;
constexpr int SL_X = SL_U + 2 * U_CH;
constexpr int XR_ROW = 144;
constexpr int SL_XX = SL_X + 64 * XR_ROW;
constexpr int SL_T = SL_XX + 2 * L * 2;
constexpr int SL_FLAG = SL_T + 769 * 4;
constexpr int SL_CNT = SL_T + 770 * 4;
constexpr int SL_W = SL_T + 772 * 4;
constexpr int SL_TOTAL = SL_W + 48 * 448;
static_assert(SL_RED + 4 * 2048 <= KG_CH && KG_CH % 16 == 0 && SL_W % 16 == 0 && SL_TOTAL <= 163840 && KG_CH + 768 < 65536, "lds map");

struct GatherTab { u32 a[256 * 12]; };
constexpr GatherTab make_gather_tab() {
  GatherTab t = {};
  for (int tc = 0; tc < 256; ++tc)
    for (int j = 0; j < 12; ++j) {
      const int i = 2047 - 8 * tc - j;
      int slot = 768;
      if (i >= 0) {
        const int s = i < 256 ? 0 : i < 512 ? 1 : i < 1024 ? 2 : 3;
        const int lag = s ? ((i + (1 << (s - 1)) - 1 + ((i >> s) & 1)) >> s) << s : i;
        const int lo = (lag & 255) >> 3, b = lag >> 8, r = lag & 7;
        const int row = b == 0 ? r : b == 1 ? 8 + (r >> 1) : b < 4 ? 12 + 2 * (b - 2) + (r >> 2) : 12 + b;
        slot = row * 32 + lo;
      }
      t.a[(j >> 2) * 1024 + tc * 4 + (j & 3)] = SL_T + 4 * slot;
    }
  return t;
}
static __device__ const GatherTab gather_tab = make_gather_tab();

DI s16x8 tr2(u32 a, int o0, int o1) {
  const s16x4 lo = __builtin_amdgcn_ds_read_tr16_b64_v4i16((LDS3 s16x4*)(uintptr_t)(a + o0));
  const s16x4 hi = __builtin_amdgcn_ds_read_tr16_b64_v4i16((LDS3 s16x4*)(uintptr_t)(a + o1));
  return __builtin_shufflevector(lo, hi, 0, 1, 2, 3, 4, 5, 6, 7);
}
template <int N> DI void wait_vm() { asm volatile("s_waitcnt vmcnt(%c0)" :: "i"(N) : "memory"); }
DI float dot_ones(s16x8 f, float s) {
  const u32x4 w4 = __builtin_bit_cast(u32x4, f);
#pragma unroll
  for (int i = 0; i < 4; ++i) s = dot_bf(w4[i], 0x3f803f80u, s);
  return s;
}
DI float half_sum(float v) {
  const u32 w = __builtin_bit_cast(u32, v);
  const auto r = __builtin_amdgcn_permlane32_swap(w, w, false, false);
  const u32 r0 = r[0], r1 = r[1];
  return __builtin_bit_cast(float, r0) + __builtin_bit_cast(float, r1);
}
DI void set_m0(u32 v) { asm volatile("s_mov_b32 m0, %0\n\ts_nop 0" :: "s"(v) : "memory"); }
template <int R0, int R1, int R2, int R3, int R4, int R5>
DI void put6(u32 v0, u32 v1, u32 v2, u32 v3, u32 v4, u32 v5) {
  asm volatile("ds_write_addtid_b32 %0 offset:%c6\n\tds_write_addtid_b32 %1 offset:%c7\n\tds_write_addtid_b32 %2 offset:%c8\n\t"
               "ds_write_addtid_b32 %3 offset:%c9\n\tds_write_addtid_b32 %4 offset:%c10\n\tds_write_addtid_b32 %5 offset:%c11"
               :: "v"(v0), "v"(v1), "v"(v2), "v"(v3), "v"(v4), "v"(v5),
                  "i"(R0 * KG_ROW), "i"(R1 * KG_ROW), "i"(R2 * KG_ROW), "i"(R3 * KG_ROW), "i"(R4 * KG_ROW), "i"(R5 * KG_ROW)
               : "memory");
}
template <int K, int RB>
DI void kg_step(f32x2& fa, f32x2& fb, f32x2& e0, f32x2& e1, f32x2& e2, f32x2& e3, f32x2 z, f32x2 w, f32x2 c) {
  f32x2 t0, t1, t2, t3, t4, t5;
  u32 h0, h1, h2, h3, h4, h5;
#define KG_M(t, a, b) "v_pk_mul_f32 " t ", " a ", " b " op_sel_hi:[0,1]\n\t"
#define KG_F(a, b, t) "v_pk_fma_f32 " a ", " a ", " b ", " t " op_sel:[1,1,0] op_sel_hi:[1,0,1] neg_lo:[0,1,0]\n\t"
  asm volatile(KG_M("%6", "%0", "%12") KG_M("%7", "%1", "%13") KG_M("%8", "%2", "%14") KG_M("%9", "%3", "%14") KG_M("%10", "%4", "%14") KG_M("%11", "%5", "%14")
               KG_F("%0", "%12", "%6") KG_F("%1", "%13", "%7") KG_F("%2", "%14", "%8") KG_F("%3", "%14", "%9") KG_F("%4", "%14", "%10") KG_F("%5", "%14", "%11")
               : "+v"(fa), "+v"(fb), "+v"(e0), "+v"(e1), "+v"(e2), "+v"(e3),
                 "=&v"(t0), "=&v"(t1), "=&v"(t2), "=&v"(t3), "=&v"(t4), "=&v"(t5)
               : "v"(z), "v"(w), "v"(c));
#undef KG_M
#undef KG_F
  h0 = pack_h(fa); h1 = pack_h(fb); h2 = pack_h(e0); h3 = pack_h(e1); h4 = pack_h(e2); h5 = pack_h(e3);
  put6<K, RB, 21 + 4 * K, 22 + 4 * K, 23 + 4 * K, 24 + 4 * K>(h0, h1, h2, h3, h4, h5);
}
DI void group_barrier(u32 cnt_addr, u32 target) {
  u32 one = 1, v, sv;
  asm volatile("s_waitcnt lgkmcnt(0)\n\t"
               "s_mov_b64 exec, 1\n\t"
               "ds_add_u32 %2, %3\n\t"
               "s_mov_b64 exec, -1\n"
               "1:\n\t"
               "ds_read_b32 %0, %2\n\t"
               "s_waitcnt lgkmcnt(0)\n\t"
               "v_readfirstlane_b32 %1, %0\n\t"
               "s_cmp_lt_u32 %1, %4\n\t"
               "s_cbranch_scc1 1b"
               : "=&v"(v), "=&s"(sv) : "v"(cnt_addr), "v"(one), "s"(target) : "memory", "scc");
}

#define MFMA16(a, b, c) __builtin_amdgcn_mfma_f32_16x16x32_bf16((a), (b), (c), 0, 0, 0)
#define MFMA32(a, b, c) __builtin_amdgcn_mfma_f32_32x32x16_bf16((a), (b), (c), 0, 0, 0)

template <int TG> DI constexpr int conv_tile(int i) { return TG == 0 ? (i == 0 ? 7 : i == 1 ? 5 : i == 2 ? 2 : 0) : (i == 0 ? 6 : i == 1 ? 4 : i == 2 ? 3 : 1); }

struct ConvSet { u32x2 al[8], ah[8]; s16x8 b[8]; };
template <int RR, int P> DI void conv_rd(ConvSet& s, u32 ka, u32 ub) {
  asm volatile("ds_read_b64 %0, %3 offset:%c5\n\tds_read_b64 %1, %3 offset:%c6\n\tds_read_b128 %2, %4 offset:%c7"
               : "=&v"(s.al[P]), "=&v"(s.ah[P]), "=&v"(s.b[P])
               : "v"(ka), "v"(ub), "i"(512 * (7 - P) + 64 * (3 - RR)), "i"(512 * (7 - P) + 64 * (3 - RR) + 8), "i"(512 * P + 64 * (3 - RR)));
}
DI s16x8 conv_a(const ConvSet& s, int p) {
  return __builtin_bit_cast(s16x8, __builtin_shufflevector(s.al[p], s.ah[p], 0, 1, 2, 3));
}
template <int TG, int RR, bool PRE> DI void conv_step(const ConvSet& c, ConvSet& n, u32 ka, u32 ub, f32x4 (&acc)[4]) {
  constexpr int NP = TG == 0 ? 8 : 7;
  asm volatile("s_waitcnt lgkmcnt(0)" ::: "memory");
  SCHED0();
#define CV_MM(i, p) acc[i] = MFMA16(conv_a(c, p), c.b[conv_tile<TG>(i) - (p)], acc[i])
#define CV_RD(p) do { if (PRE && (p) < NP) conv_rd<PRE ? RR + 1 : RR, (p)>(n, ka, ub); SCHED0(); } while (0)
  CV_MM(0, 0); CV_RD(0); CV_MM(1, 0); CV_RD(1);
  CV_MM(0, 1); CV_RD(2); CV_MM(1, 1); CV_RD(3);
  CV_MM(0, 2); CV_RD(4); CV_MM(1, 2); CV_RD(5);
  CV_MM(0, 3); CV_RD(6); CV_MM(1, 3); CV_RD(7);
  CV_MM(0, 4); CV_MM(1, 4); CV_MM(0, 5); CV_MM(2, 0); CV_MM(0, 6); CV_MM(2, 1); CV_MM(3, 0); CV_MM(2, 2);
  if (TG == 0) { CV_MM(0, 7); CV_MM(1, 5); } else { CV_MM(2, 3); CV_MM(3, 1); }
  SCHED0();
#undef CV_MM
#undef CV_RD
}
template <int TG> DI void conv_body(u32 ka, u32 ub, f32x4 (&acc)[4]) {
  ConvSet s0, s1;
  conv_rd<0, 0>(s0, ka, ub); conv_rd<0, 1>(s0, ka, ub); conv_rd<0, 2>(s0, ka, ub); conv_rd<0, 3>(s0, ka, ub);
  conv_rd<0, 4>(s0, ka, ub); conv_rd<0, 5>(s0, ka, ub); conv_rd<0, 6>(s0, ka, ub);
  if (TG == 0) conv_rd<0, 7>(s0, ka, ub);
  conv_step<TG, 0, true>(s0, s1, ka, ub, acc);
  conv_step<TG, 1, true>(s1, s0, ka, ub, acc);
  conv_step<TG, 2, true>(s0, s1, ka, ub, acc);
  conv_step<TG, 3, false>(s1, s0, ka, ub, acc);
}

__global__ __launch_bounds__(512, 2) void k_ssm(
    const u16* x, const u16* A_re, const u16* A_im, const u16* B_re, const u16* B_im, const u16* C_re,
    const u16* C_im, const u16* log_dt, const u16* Dskip, const u16* Wg, const u16* Wu, const u16* Wd, char* ws) {
  __shared__ __attribute__((aligned(16))) char lds[SL_TOTAL];
  STAMP_DECL;
  STAMP(0, 0);
  const u32 tid = threadIdx.x;
  const u32 xcd = blockIdx.x & 7, idx = blockIdx.x >> 3;
  const bool chore = idx >= 28;
  const u32 cp = xcd * 28 + min(idx, 27u);
  const u32 xrow = (tid >> 3) * (D * 2), xb_off = xrow + min(tid & 7, 6u) * 16 + idx * (64 * D * 2) + xcd * 112;
  const u32 lane = tid & 63, tc = tid & 255;
  const u32 wave = __builtin_amdgcn_readfirstlane(tid >> 6);
  const u32 ch = wave >> 2, wq = wave & 3;
  u32 xb_lds = lds_addr(lds) + SL_X + (tid >> 3) * XR_ROW + (tid & 7) * 16;
  u32 xg_lds = lds_addr(lds) + SL_X + (tid & 31) * 2 * XR_ROW + (tid >> 5) * 4;
  u32 xg_off = WS_XG + xcd * 28 * (L * 8) + idx * 512 + (tid >> 5) * (L * 8) + (tid & 31) * 16;
  auto publish = [&](u32 tag, u32 a0, u32 a1, u32 a2, u32 a3) {
    st128_dev(ws, xg_off, u32x4{a0, tag, a1, tag});
    st128_dev(ws, xg_off + 14 * (L * 8), u32x4{a2, tag, a3, tag});
  };
  u32 xb_o = xb_off, cp4 = cp * 4;
  asm volatile("" : "+v"(xb_o), "+v"(xb_lds), "+v"(xg_lds), "+v"(xg_off));
  asm volatile("" : "+s"(cp4));
  SCHED0();
  asm volatile("" :: "s"(x), "s"(A_re), "s"(A_im), "s"(B_re), "s"(B_im), "s"(C_re), "s"(C_im), "s"(log_dt), "s"(Dskip),
               "s"(Wg), "s"(Wu), "s"(Wd), "s"(ws));
  u32x4 xr[7];
  if (wave == 7) {
    asm volatile("s_setprio 2\n\t"
                 "global_load_dwordx4 %0, %7, %8\n\tglobal_load_dwordx4 %1, %7, %8 offset:128\n\tglobal_load_dwordx4 %2, %7, %8 offset:256\n\t"
                 "global_load_dwordx4 %3, %7, %8 offset:384\n\tglobal_load_dwordx4 %4, %7, %8 offset:512\n\tglobal_load_dwordx4 %5, %7, %8 offset:640\n\t"
                 "global_load_dwordx4 %6, %7, %8 offset:768"
                 : "=&v"(xr[0]), "=&v"(xr[1]), "=&v"(xr[2]), "=&v"(xr[3]), "=&v"(xr[4]), "=&v"(xr[5]), "=&v"(xr[6])
                 : "v"(xrow + (tid & 7) * 16 + (blockIdx.x * 8 - 56) * (D * 2)), "s"(x) : "memory");
  }
  auto stats = [&](u32 tag) {
    asm volatile("" : "+v"(xr[0]), "+v"(xr[1]), "+v"(xr[2]), "+v"(xr[3]), "+v"(xr[4]), "+v"(xr[5]), "+v"(xr[6]));
    float s[4] = {0.f, 0.f, 0.f, 0.f}, q[4] = {0.f, 0.f, 0.f, 0.f};
#pragma unroll
    for (int j = 0; j < 7; ++j)
#pragma unroll
      for (int i = 0; i < 4; ++i) { s[i] = dot_bf(xr[j][i], 0x3f803f80u, s[i]); q[i] = dot_bf(xr[j][i], xr[j][i], q[i]); }
    float st = (s[0] + s[1]) + (s[2] + s[3]), qt = (q[0] + q[1]) + (q[2] + q[3]);
    oct_sum2(st, qt);
    const float mean = st * (1.0f / D);
    const u32x2 gr = u32x2{finish_stats(mean, qt * (1.0f / D) - mean * mean), tag};
    asm volatile("s_mov_b64 exec, %3\n\tglobal_store_dwordx2 %0, %1, %2 sc1\n\ts_mov_b64 exec, -1\n\ts_setprio 0"
                 :: "v"(WS_SG + blockIdx.x * 64 + lane), "v"(gr), "s"(ws), "s"(0x0101010101010101ull) : "memory");
  };
  u32x4 xb, ga0, ga1, ga2, cpf;
  u32 epoch, pa, pi_, pbr, pbi, pcr, pci, sl, sd;
  asm volatile("global_load_dwordx4 %0, %4, %5\n\t"
               "s_load_dword %1, %6, %c7\n\ts_load_dword %2, %8, %10\n\ts_load_dword %3, %9, %10"
               : "=&v"(xb), "=&s"(epoch), "=&s"(sl), "=&s"(sd)
               : "v"(xb_o), "s"(x), "s"(ws), "i"(WS_EPOCH), "s"(log_dt), "s"(Dskip), "s"(cp4) : "memory");
  SCHED0();
  if (idx < 4) {
    const GAS char* pc; u32 n1;
    asm volatile("s_getpc_b64 %0\n.Lssm_code_pc:\n\ts_mov_b32 %1, .Lffn2_code_end-.Lssm_code_pc-16" : "=s"(pc), "=s"(n1));
    asm volatile("global_load_dwordx4 %0, %1, %2" : "=&v"(cpf) : "v"(min(idx * 8192u + tid * 16u, n1)), "s"(pc) : "memory");
  }
  const bool tiler = wave < 4 && !chore;
  const u32 wslice = 4 * (xcd >> 1) + (idx & 3), wchunk = (xcd & 1) * 7 + (idx >> 2), wv = cp;
  u32 gu0 = 0, gu1 = 0, dn0 = 0, dn1 = 0, ga_off = tc * 16;
  if (tiler) {
    gu0 = lane * 16 + ((lane * 37) >> 9) * (F * 2 - 224) + (wchunk * 32 + wq * 8) * (F * 2) + wslice * 224;
    dn0 = lane * 16 + (lane >= 28 ? 448 : 0) + ((wv >> 1) * 16 + wq * 4) * (D * 2) + (wv & 1) * 448;
    gu1 = gu0 + 4 * (F * 2) - 1792; dn1 = dn0 + 2 * (D * 2) - 896;
    asm volatile("" : "+v"(gu0), "+v"(gu1), "+v"(dn0), "+v"(dn1));
    __builtin_amdgcn_s_setprio(1);
  }
  const char* gtab = (const char*)gather_tab.a + 4096;
  const char* gtab2 = gtab + 4096;
  asm volatile("" : "+s"(gtab), "+s"(gtab2));
  asm volatile("" : "+v"(ga_off));
  if (tid == 0) *(u32x4*)(lds + SL_T + 768 * 4) = u32x4{0u, 0u, 0u, 0u};
  if (wq == 3) *(u32x2*)(lds + SL_U + ch * U_CH + lane * 8) = u32x2{0u, 0u};
  SCHED0();
  asm volatile("global_load_ushort %0, %6, %7\n\tglobal_load_ushort %1, %6, %8\n\tglobal_load_ushort %2, %6, %9\n\t"
               "global_load_ushort %3, %6, %10\n\tglobal_load_ushort %4, %6, %11\n\tglobal_load_ushort %5, %6, %12"
               : "=&v"(pa), "=&v"(pi_), "=&v"(pbr), "=&v"(pbi), "=&v"(pcr), "=&v"(pci)
               : "v"(cp * 1024 + tid * 2), "s"(A_re), "s"(A_im), "s"(B_re), "s"(B_im), "s"(C_re), "s"(C_im) : "memory");
  asm volatile("global_load_dwordx4 %0, %3, %4 offset:-4096\n\tglobal_load_dwordx4 %1, %3, %4\n\tglobal_load_dwordx4 %2, %3, %5"
               : "=&v"(ga0), "=&v"(ga1), "=&v"(ga2) : "v"(ga_off), "s"(gtab), "s"(gtab2) : "memory");

  asm volatile("s_waitcnt vmcnt(9) lgkmcnt(0)" : "+v"(xb), "+v"(cpf) :: "memory");
  asm volatile("" : "+s"(epoch), "+s"(sl), "+s"(sd));
  const u32 pl = ch ? sl >> 16 : sl, pd = ch ? sd >> 16 : sd;
  *(LDS3 u32x4*)(uintptr_t)xb_lds = xb;
  const u32 tag = next_tag(epoch);
  asm volatile("s_waitcnt lgkmcnt(0)" ::: "memory");
  __builtin_amdgcn_s_barrier();
  asm volatile("" ::: "memory");
  {
    const LDS3 u32* xr_l = (const LDS3 u32*)(uintptr_t)xg_lds;
    const u32 a0 = xr_l[0], a1 = xr_l[XR_ROW / 4], a2 = xr_l[14], a3 = xr_l[XR_ROW / 4 + 14];
    asm volatile("s_waitcnt vmcnt(0)" : "+v"(pa), "+v"(pi_), "+v"(pbr), "+v"(pbi), "+v"(pcr), "+v"(pci), "+v"(ga0), "+v"(ga1), "+v"(ga2) :: "memory");
    if (tiler) {
      const u32 lw = lds_addr(lds) + SL_W + wq * 3584;
      asm volatile("s_mov_b32 m0, %0\n\ts_mov_b64 exec, %10\n\t"
                   "global_load_lds_dwordx4 %3, %7\n\tglobal_load_lds_dwordx4 %4, %7 offset:1792\n\t"
                   "s_mov_b32 m0, %1\n\ts_nop 0\n\t"
                   "global_load_lds_dwordx4 %3, %8\n\tglobal_load_lds_dwordx4 %4, %8 offset:1792\n\t"
                   "s_mov_b32 m0, %2\n\ts_nop 0\n\t"
                   "global_load_lds_dwordx4 %5, %9\n\tglobal_load_lds_dwordx4 %6, %9 offset:896\n\t"
                   "s_mov_b64 exec, -1"
                   :: "s"(lw), "s"(lw + 896), "s"(lds_addr(lds) + SL_W + 32 * 448 + wq * 1792),
                      "v"(gu0), "v"(gu1), "v"(dn0), "v"(dn1), "s"(Wg), "s"(Wu), "s"(Wd), "s"(0x00ffffffffffffffull)
                   : "memory");
    }
    if (wave == 7) stats(tag); else publish(tag, a0, a1, a2, a3);
    if (chore) {
      if (blockIdx.x == 255 && tid < 16) *(u32*)(ws + WS_CNT + tid * 256) = 0u;
      STAMP(0, 9);
      return;
    }
  }
  STAMP(0, 1);
  s16x8 tf[2][3];
  float tcs[2];
  auto tile_read = [&]() {
    const u32 hh = lane >> 5, blk = (lane >> 4) & 1, q = (lane & 15) >> 2, p = lane & 3;
    const u32 a = lds_addr(lds) + SL_W + wq * 64 + hh * 3584 + q * 224 + blk * 896 + 8 * p;
    const u32 b = lds_addr(lds) + SL_W + 32 * 448 + wq * 128 + (4 * hh + q) * 448 + blk * 32 + 8 * p;
#pragma unroll
    for (int i = 0; i < 2; ++i) {
      tf[i][0] = tr2(a + 32 * i, 0, 1792); tf[i][1] = tr2(a + 32 * i, 4 * 1792, 5 * 1792); tf[i][2] = tr2(b + 64 * i, 0, 8 * 448);
    }
#pragma unroll
    for (int i = 0; i < 2; ++i) tcs[i] = half_sum(dot_ones(tf[i][1], dot_ones(tf[i][0], 0.f)));
  };
  auto tile_store = [&]() {
    const u32 un = wslice * 14 + wchunk;
    const u32 wt = WS_WT + (un * 14 + 2 * wq) * 1024 + lane * 16;
    const u32 wd = WS_WD + (((wv & 1) * 7 + 2 * wq) * 112 + (wv >> 1)) * 1024 + lane * 16;
    const u32 cs = WS_CS + un * 896 + wq * 256 + lane * 4;
#pragma unroll
    for (int i = 0; i < 2; ++i) {
      if (i == 0 || wq < 3) {
        st128_dev(ws, wt + 1024 * i, __builtin_bit_cast(u32x4, tf[i][0]));
        st128_dev(ws, wt + 1024 * i + 7168, __builtin_bit_cast(u32x4, tf[i][1]));
        st128_dev(ws, wd + i * 112 * 1024, __builtin_bit_cast(u32x4, tf[i][2]));
        asm volatile("s_mov_b64 exec, %3\n\tglobal_store_dword %0, %1, %2 sc1\n\ts_mov_b64 exec, -1"
                     :: "v"(cs + 128 * i), "v"(tcs[i]), "s"(ws), "s"(0xffffffffull) : "memory");
      }
    }
  };
  const __amdgpu_buffer_rsrc_t rs = __builtin_amdgcn_make_buffer_rsrc((void*)ws, 0, (int)WS_END, 0x00020000);
  const u32 cnt = lds_addr(lds) + SL_CNT + ch * 4;
  f32x2 cb;
  float inv_sc, p2, rv;
  {
    const float dt = rbf(__builtin_amdgcn_exp2f(bf_lo(pl) * 1.4426950408889634f));
    p2 = (dt * bf_lo(pa)) * 1.4426950408889634f;
    rv = (dt * bf_lo(pi_)) * 0.15915494309189535f;
    cb = cmul(f32x2{bf_lo(pcr), bf_lo(pci)}, f32x2{bf_lo(pbr), bf_lo(pbi)} * dt);
    const float cbx = cb.x, cby = cb.y;
    u32 eb = (u32)wave_imax(max(__builtin_bit_cast(int, cbx) & 0x7fffffff, __builtin_bit_cast(int, cby) & 0x7fffffff)) >> 23;
    asm("s_max_u32 %0, %0, 27\n\ts_min_u32 %0, %0, 227" : "+s"(eb) :: "scc");
    inv_sc = __builtin_bit_cast(float, (eb - 9) << 23);
    cb *= __builtin_bit_cast(float, (263 - eb) << 23);
  }
  STAMP(0, 2);

  {
    set_m0(lds_addr(lds) + ch * KG_CH + wq * 256);
    auto cpow = [&](float mp, float mr) {
      const float mag = __builtin_amdgcn_exp2f(p2 * mp);
      const float t = __builtin_amdgcn_fractf(rv * mr);
      return f32x2{__builtin_amdgcn_cosf(t), __builtin_amdgcn_sinf(t)} * mag;
    };
    const f32x2 z1 = cpow(1.f, 1.f), c8 = cpow(8.f, -8.f), w256 = cpow(256.f, 256.f);
    const f32x2 z2 = cmul(z1, z1), c16 = cmul(c8, c8);
    const f32x2 z4 = cmul(z2, z2), c32 = cmul(c16, c16);
    f32x2 e0 = f32x2{1.f, 0.f}, e1 = c8, e2 = c16, e3 = cmul(c16, c8);
    f32x2 fa = cb, fb = cmul(cb, w256);
    put6<0, 8, 21, 22, 23, 24>(pack_h(fa), pack_h(fb), 0x3c00u, pack_h(e1), pack_h(e2), pack_h(e3));
    const f32x2 g1 = cmul(fb, z2), g2 = cmul(fb, z4);
    kg_step<1, 12>(fa, fb, e0, e1, e2, e3, z1, w256, c32);
    const f32x2 g3 = cmul(g1, z4), g4 = cmul(fb, z4);
    kg_step<2, 14>(fa, fb, e0, e1, e2, e3, z1, w256, c32);
    const u32 h772 = pack_h(cmul(fb, z4));
    put6<9, 10, 11, 13, 15, 15>(pack_h(g1), pack_h(g2), pack_h(g3), pack_h(g4), h772, h772);
    kg_step<3, 16>(fa, fb, e0, e1, e2, e3, z1, w256, c32);
    kg_step<4, 17>(fa, fb, e0, e1, e2, e3, z1, w256, c32);
    kg_step<5, 18>(fa, fb, e0, e1, e2, e3, z1, w256, c32);
    kg_step<6, 19>(fa, fb, e0, e1, e2, e3, z1, w256, c32);
    kg_step<7, 20>(fa, fb, e0, e1, e2, e3, z1, w256, c32);
  }
  STAMP(0, 3);
  {
    char* kg = lds + ch * KG_CH + wq * 256;
    const char* fa = kg + (lane & 31) * KG_ROW + (lane >> 5) * 16;
    h16x8 a[8], b[8];
    asm volatile("s_waitcnt lgkmcnt(0)" ::: "memory");
#pragma unroll
    for (int s = 0; s < 8; ++s) { a[s] = *(const h16x8*)(fa + 32 * s); b[s] = *(const h16x8*)(fa + 21 * KG_ROW + 32 * s); }
    SCHED0();
    f32x16 acc;
#pragma unroll
    for (int i = 0; i < 16; ++i) acc[i] = 0.f;
#pragma unroll
    for (int s = 0; s < 8; ++s) acc = __builtin_amdgcn_mfma_f32_32x32x16_f16(a[s], b[s], acc, 0, 0, 0);
    char* pt = kg + (lane >> 4) * KG_ROW + (lane & 15) * 16;
#pragma unroll
    for (int q = 0; q < 3; ++q)
      *(f32x4*)(pt + 4 * q * KG_ROW) = f32x4{acc[4 * q], acc[4 * q + 1], acc[4 * q + 2], acc[4 * q + 3]} * inv_sc;
  }
  const u32 xo = WS_XG + cp * (L * 8);
  u32x4 gx[4], gs[4];
  auto repoll = [&]() {
#pragma unroll
    for (int i = 0; i < 4; ++i) {
      gx[i] = __builtin_amdgcn_raw_buffer_load_b128(rs, tc * 16, xo + 4096 * i, 16);
      gs[i] = __builtin_amdgcn_raw_buffer_load_b128(rs, tc * 16, WS_SG + 4096 * i, 16);
    }
  };
  auto poll = [&]() {
#pragma unroll
    for (int i = 0; i < 4; ++i) {
      gx[i] = __builtin_amdgcn_raw_buffer_load_b128(rs, tc * 16, xo + 4096 * i, POLL_AUX);
      gs[i] = __builtin_amdgcn_raw_buffer_load_b128(rs, tc * 16, WS_SG + 4096 * i, POLL_AUX);
    }
  };
  STAMP(0, 4);
  group_barrier(cnt, 4);
  if (wq < 3) {
    const f32x4* pt = (const f32x4*)(lds + ch * KG_CH + 4 * wq * KG_ROW + (lane >> 4) * KG_ROW + (lane & 15) * 16);
    const f32x4 s = (pt[0] + pt[16]) + (pt[32] + pt[48]);
    const u32 p01 = pack_bf(s[0], s[1]), p23 = pack_bf(s[2], s[3]);
    u16* T = (u16*)(lds + SL_T + wq * 1024 + ch * 2 + (lane >> 5) * 512 + (lane & 31) * 4);
    T[0] = (u16)p01; T[64] = (u16)(p01 >> 16); T[128] = (u16)p23; T[192] = (u16)(p23 >> 16);
  }
  if (wave == 7) asm volatile("s_waitcnt vmcnt(1)" ::: "memory"); else asm volatile("s_waitcnt vmcnt(2)" ::: "memory");
  group_barrier(cnt, 8);
  poll();
  const u32 sel = ch ? 0x07060302u : 0x05040100u;
  {
    u32 e[11];
#pragma unroll
    for (int j = 0; j < 11; ++j) {
      const u32 a = j < 4 ? ga0[j & 3] : j < 8 ? ga1[j & 3] : ga2[j & 3];
      e[j] = *(const LDS3 u32*)(uintptr_t)(a + lds_addr(lds));
    }
    char* kc = lds + ch * KC_CH + tc * 16;
#pragma unroll
    for (int s = 0; s < 4; ++s)
      *(u32x4*)(kc + s * KC_STRIDE) = u32x4{__builtin_amdgcn_perm(e[s + 1], e[s], sel), __builtin_amdgcn_perm(e[s + 3], e[s + 2], sel),
                                            __builtin_amdgcn_perm(e[s + 5], e[s + 4], sel), __builtin_amdgcn_perm(e[s + 7], e[s + 6], sel)};
    if (wq == 0 && lane < 8) *(u32x4*)(lds + ch * KC_CH + (lane >> 1) * KC_STRIDE + (256 + (lane & 1)) * 16) = u32x4{0u, 0u, 0u, 0u};
  }
  STAMP(0, 5);
  if (tiler) tile_read();
  u32 cv_ka, cv_ub, ep_lo;
  {
    const u32 b = lane & 15, g = lane >> 4, r3 = 8 * (wq >> 1) + 6;
    const u32 gg = 16 * (g & 1) - 32 * (g >> 1);
    cv_ka = lds_addr(lds) + ch * KC_CH + 3 * KC_STRIDE + (2044 - 16 * r3 - 256 * 7) * 2 - (b & 3) * KC_STRIDE - 8 * (b >> 2) + gg;
    cv_ub = lds_addr(lds) + SL_U + ch * U_CH + 512 - 32 * r3 + 32 * b + gg;
    ep_lo = 32 * b + 8 * g;
    asm volatile("" : "+v"(cv_ka), "+v"(cv_ub), "+v"(ep_lo));
  }
  {
    bool clean = true;
    for (u32 spins = 0;; ++spins) {
      u32 bad = 0;
#pragma unroll
      for (int i = 0; i < 4; ++i) bad |= ((gx[i][1] ^ tag) | (gx[i][3] ^ tag)) | ((gs[i][1] ^ tag) | (gs[i][3] ^ tag));
      if (__all(bad == 0u)) break;
      if (spins >= 4096u) { clean = false; break; }
      __builtin_amdgcn_s_sleep(POLL_SLEEP);
      asm volatile("" ::: "memory");
      repoll();
    }
    u32 xs[8], st[8];
#pragma unroll
    for (int i = 0; i < 8; ++i) { xs[i] = gx[i >> 1][2 * (i & 1)]; st[i] = gs[i >> 1][2 * (i & 1)]; }
    if (!clean) {
      *(u32*)(lds + SL_FLAG) = 1u;
#pragma unroll 1
      for (int i = 0; i < 8; ++i) {
        const u32* xrow = (const u32*)(x + (512 * (i >> 1) + 2 * tc + (i & 1)) * D);
        float s = 0.f, q = 0.f;
#pragma unroll 1
        for (int j = 0; j < D / 2; ++j) { const u32 w = xrow[j]; s = dot_bf(w, 0x3f803f80u, s); q = dot_bf(w, w, q); }
        const float mean = s * (1.0f / D);
#pragma unroll
        for (int k = 0; k < 7; ++k) { st[k] = st[k + 1]; xs[k] = xs[k + 1]; }
        st[7] = finish_stats(mean, q * (1.0f / D) - mean * mean);
        xs[7] = xrow[cp];
      }
    }
    u32x4 xw, uw;
#pragma unroll
    for (int i = 0; i < 4; ++i) {
      xw[i] = __builtin_amdgcn_perm(xs[2 * i + 1], xs[2 * i], sel);
      const f32x2 u2 = (f32x2{bf_lo(xw[i]), bf_hi(xw[i])} - f32x2{bf_lo(st[2 * i]), bf_lo(st[2 * i + 1])}) *
                       f32x2{__builtin_amdgcn_rcpf(bf_hi(st[2 * i])), __builtin_amdgcn_rcpf(bf_hi(st[2 * i + 1]))};
      uw[i] = pack_bf(u2.x, u2.y);
    }
    u32* up = (u32*)(lds + SL_U + ch * U_CH + 512) + tc;
    u32* xp = (u32*)(lds + SL_XX + ch * L * 2) + tc;
#pragma unroll
    for (int i = 0; i < 4; ++i) { up[256 * i] = uw[i]; xp[256 * i] = xw[i]; }
  }
  STAMP(0, 6);
  if (tiler) tile_store();
  group_barrier(cnt, 12);
  if (blockIdx.x == 0 && tid == 0 && *(const u32*)(lds + SL_FLAG) == 0u) *(u32*)(ws + WS_EPOCH) = tag;
  STAMP(0, 7);
  const u32 tg = wq & 1, rh = wq >> 1;
  f32x4 acc[4];
#pragma unroll
  for (int i = 0; i < 4; ++i) acc[i] = f32x4{0.f, 0.f, 0.f, 0.f};
  if (tg == 0) conv_body<0>(cv_ka, cv_ub, acc);
  else conv_body<1>(cv_ka, cv_ub, acc);
  STAMP(0, 8);
  {
    f32x4* red = (f32x4*)(lds + ch * KG_CH + SL_RED) + lane;
    f32x4 y[2];
    if (rh) { red[wq * 128] = acc[0]; red[wq * 128 + 64] = acc[3]; y[0] = acc[1]; y[1] = acc[2]; }
    else { red[wq * 128] = acc[1]; red[wq * 128 + 64] = acc[2]; y[0] = acc[0]; y[1] = acc[3]; }
    const int t0 = rh ? 5 - tg : 7 - tg, t1 = rh ? 2 + tg : tg;
    const char* up = lds + SL_U + ch * U_CH + 512 + ep_lo;
    const char* xp = lds + SL_XX + ch * L * 2 + ep_lo;
    const u32x2 uu0 = *(const u32x2*)(up + 512 * t0), uu1 = *(const u32x2*)(up + 512 * t1);
    const u32x2 xx0 = *(const u32x2*)(xp + 512 * t0), xx1 = *(const u32x2*)(xp + 512 * t1);
    const float dsk = bf_lo(pd);
    f32x2 b0[2], b1[2];
#pragma unroll
    for (int j = 0; j < 2; ++j) {
      b0[j] = __builtin_elementwise_fma(f32x2{bf_lo(uu0[j]), bf_hi(uu0[j])}, f32x2{dsk, dsk}, f32x2{bf_lo(xx0[j]), bf_hi(xx0[j])}) + f32x2{y[0][2 * j], y[0][2 * j + 1]};
      b1[j] = __builtin_elementwise_fma(f32x2{bf_lo(uu1[j]), bf_hi(uu1[j])}, f32x2{dsk, dsk}, f32x2{bf_lo(xx1[j]), bf_hi(xx1[j])}) + f32x2{y[1][2 * j], y[1][2 * j + 1]};
    }
    asm volatile("" : "+v"(b0[0]), "+v"(b0[1]), "+v"(b1[0]), "+v"(b1[1]));
    group_barrier(cnt, 16);
    const f32x4 o0 = red[(wq ^ 2) * 128], o1 = red[(wq ^ 2) * 128 + 64];
    const u32 hb = WS_HT + (2 * cp + ch) * 256, ho = (ep_lo & 255) + (ep_lo >> 8) * (D * 256);
    __builtin_amdgcn_raw_buffer_store_b64(u32x2{pack_bf(b0[0].x + o0[0], b0[0].y + o0[1]), pack_bf(b0[1].x + o0[2], b0[1].y + o0[3])}, rs, ho, hb + 2 * D * 256 * t0, 0);
    __builtin_amdgcn_raw_buffer_store_b64(u32x2{pack_bf(b1[0].x + o1[0], b1[0].y + o1[1]), pack_bf(b1[1].x + o1[2], b1[1].y + o1[3])}, rs, ho, hb + 2 * D * 256 * t1, 0);
  }
  STAMP(0, 9);
}

constexpr int F1_H = 32 * 256;
constexpr int F1_STAGE = F1_H + 14 * 1024;
constexpr int F1_NST = 6;
constexpr int F1_CS = F1_NST * F1_STAGE;
constexpr int F1_ST = F1_CS + 7 * 256;
constexpr int F1_FL = F1_ST + 512;
constexpr int F2_RES = F1_FL + 32;
constexpr int F1_DUMP = F2_RES + 4 * 2048;
constexpr int F1_TOTAL = F1_DUMP + 2048;
constexpr int F1_PRE = 3;
#define F1_PF0 20
#define F1_UNR 2
#define F2_WIN 8
constexpr int F2_REG = 12;
static_assert(4 * (28 - F2_REG) * 2048 <= F1_NST * F1_STAGE, "W_down part in LDS");

DI void f1_publish(char* ws, u32 arr_addr, int panel) {
  u32 old, one = 1;
  asm volatile("s_mov_b64 exec, 1\n\tds_add_rtn_u32 %0, %1, %2\n\ts_mov_b64 exec, -1\n\ts_waitcnt lgkmcnt(0)" : "=&v"(old) : "v"(arr_addr), "v"(one) : "memory");
  if (__builtin_amdgcn_readfirstlane(old) == 7u && (threadIdx.x & 63) == 0)
    __hip_atomic_fetch_add((GAS u32*)(ws + WS_CNT + panel * 256), 1u, __ATOMIC_RELAXED, __HIP_MEMORY_SCOPE_AGENT);
}

struct F1Frag { s16x8 a[4], b[2]; };

__global__ __launch_bounds__(512, 2) void k_ffn(char* __restrict__ ws, u16* __restrict__ out) {
  __shared__ __attribute__((aligned(16))) char lds[F1_TOTAL];
  STAMP_DECL;
  STAMP(1, 0);
  const int tid = threadIdx.x, lane = tid & 63;
  const int wave = __builtin_amdgcn_readfirstlane(tid >> 6);
  const int xcd = blockIdx.x & 7, bi = blockIdx.x >> 3;
  const int panel = (xcd & 1) * 8 + (bi & 7), slice = (xcd >> 1) * 4 + (bi >> 3);

  if (wave >= 4) {
    const int lw = wave - 4;
    const char* hb = ws + WS_HT + panel * (D * 256);
    const char* wb = ws + WS_WT + slice * (14 * 14 * 1024);
    const int r = lane >> 4;
    const u32 ho = (u32)((lw * 8 + r) * 256 + (((lane & 15) ^ (r << 2)) * 16));
    const u32 wo = lw * 3584 + lane * 16;
    const u32 lbase = lds_addr(lds) + lw * 2048;
    auto stage = [&](int c, int slot) {
      asm volatile(
          "s_mov_b32 m0, %0\n\t"
          "s_nop 0\n\t"
          "global_load_lds_dwordx4 %2, %4\n\t"
          "global_load_lds_dwordx4 %2, %4 offset:1024\n\t"
          "s_mov_b32 m0, %1\n\t"
          "s_nop 0\n\t"
          "global_load_lds_dwordx4 %3, %5\n\t"
          "global_load_lds_dwordx4 %3, %5 offset:1024\n\t"
          "global_load_lds_dwordx4 %3, %5 offset:2048\n\t"
          "s_mov_b64 exec, %6\n\t"
          "global_load_lds_dwordx4 %3, %5 offset:3072\n\t"
          "s_mov_b64 exec, -1"
          :: "s"(lbase + slot * F1_STAGE), "s"(lbase + slot * F1_STAGE + F1_H + lw * (3584 - 2048)),
             "v"(ho), "v"(wo), "s"(hb + c * 32 * 256), "s"(wb + c * 14 * 1024), "s"(0xffffffffull)
          : "memory");
    };
    stage(0, 0);
    SCHED0();
    auto pf = [&](int i) {
      asm volatile("s_mov_b32 m0, %0\n\ts_mov_b64 exec, %3\n\t"
                   "global_load_lds_dwordx4 %1, %2\n\tglobal_load_lds_dwordx4 %1, %2 offset:896\n\t"
                   "s_mov_b64 exec, -1"
                   :: "s"(lds_addr(lds) + F1_DUMP), "v"(lane * 16), "s"(ws + WS_WD + bi * 50176 + lw * 12544 + i * 1792), "s"(0x00ffffffffffffffull)
                   : "memory");
    };
    f32x2 psum = {0.f, 0.f};
    float v[14];
    auto part = [&]() {
      {
        const int q = (lane & 15) >> 2;
        const u32 a = lds_addr(lds) + (slice % F1_NST) * F1_STAGE + (8 * (lane >> 5) + q) * 256 + ((lw ^ q) * 64) + ((lane >> 4) & 1) * 32 + 8 * (lane & 3);
        const u32x4 n0 = __builtin_bit_cast(u32x4, tr2(a, 0, 1024)), n1 = __builtin_bit_cast(u32x4, tr2(a, 4096, 5120));
        float s0 = 0.f, s1 = 0.f, q0 = 0.f, q1 = 0.f;
#pragma unroll
        for (int i = 0; i < 8; ++i) {
          const u32 w = i < 4 ? n0[i & 3] : n1[i & 3];
          const float lo = bf_lo(w), hi = bf_hi(w);
          s0 += lo; q0 = __builtin_fmaf(lo, lo, q0);
          s1 += hi; q1 = __builtin_fmaf(hi, hi, q1);
          asm("" : "+v"(s0)); asm("" : "+v"(q0)); asm("" : "+v"(s1)); asm("" : "+v"(q1));
        }
        psum = f32x2{s0 + s1, q0 + q1};
        asm volatile("" : "+v"(psum));
      }
    };
    {
      const u32 co = min(tid - 256, 223) * 4;
      const char* cp = ws + WS_CS + slice * (14 * 896);
      asm volatile("global_load_dword %0, %14, %15\n\tglobal_load_dword %1, %14, %15 offset:896\n\tglobal_load_dword %2, %14, %15 offset:1792\n\t"
                   "global_load_dword %3, %14, %15 offset:2688\n\tglobal_load_dword %4, %14, %15 offset:3584\n\t"
                   "global_load_dword %5, %14, %16\n\tglobal_load_dword %6, %14, %16 offset:896\n\tglobal_load_dword %7, %14, %16 offset:1792\n\t"
                   "global_load_dword %8, %14, %16 offset:2688\n\tglobal_load_dword %9, %14, %16 offset:3584\n\t"
                   "global_load_dword %10, %14, %17\n\tglobal_load_dword %11, %14, %17 offset:896\n\tglobal_load_dword %12, %14, %17 offset:1792\n\t"
                   "global_load_dword %13, %14, %17 offset:2688"
                   : "=&v"(v[0]), "=&v"(v[1]), "=&v"(v[2]), "=&v"(v[3]), "=&v"(v[4]), "=&v"(v[5]), "=&v"(v[6]),
                     "=&v"(v[7]), "=&v"(v[8]), "=&v"(v[9]), "=&v"(v[10]), "=&v"(v[11]), "=&v"(v[12]), "=&v"(v[13])
                   : "v"(co), "s"(cp), "s"(cp + 4480), "s"(cp + 8960) : "memory");
    }
    stage(1, 1); stage(2, 2);
    wait_vm<2 * 6 + 14>();
    __builtin_amdgcn_s_barrier();
    asm volatile("" ::: "memory");
    int slot = F1_PRE;
#pragma unroll 1
    for (int j = 0; j < 11; ++j) {
      stage(j + F1_PRE, slot);
      slot = slot == F1_NST - 1 ? 0 : slot + 1;
      if (j >= F1_PF0 && j < F1_PF0 + 7) pf(j - F1_PF0);
      if (j == 1) {
        asm volatile("" : "+v"(v[0]), "+v"(v[1]), "+v"(v[2]), "+v"(v[3]), "+v"(v[4]), "+v"(v[5]), "+v"(v[6]),
                     "+v"(v[7]), "+v"(v[8]), "+v"(v[9]), "+v"(v[10]), "+v"(v[11]), "+v"(v[12]), "+v"(v[13]));
        const float csum = (((v[0] + v[1]) + (v[2] + v[3])) + ((v[4] + v[5]) + (v[6] + v[7]))) + (((v[8] + v[9]) + (v[10] + v[11])) + (v[12] + v[13]));
        const int i = tid - 256;
        u32* ca = (u32*)(lds + F1_CS) + (i >> 5) * 64 + (i & 31);
        if (i < 224) { ca[0] = pack_bf(csum, csum - rbf(csum)); ca[32] = 0u; }
      }
      if (j == slice) part();
      if (j >= F1_PF0 + 2 && j < F1_PF0 + 7) asm volatile("s_waitcnt vmcnt(18) lgkmcnt(0)" ::: "memory");
      else asm volatile("s_waitcnt vmcnt(12) lgkmcnt(0)" ::: "memory");
      __builtin_amdgcn_s_barrier();
      asm volatile("" ::: "memory");
    }
    wait_vm<0>();
    __builtin_amdgcn_s_barrier();
    asm volatile("" ::: "memory");
    const __amdgpu_buffer_rsrc_t rs = __builtin_amdgcn_make_buffer_rsrc((void*)ws, 0, (int)WS_END, 0x00020000);
    if (slice < 14) {
      if (slice >= 11) part();
      const float a = half_sum(psum.x), b = half_sum(psum.y);
      if (lane < 32) __builtin_amdgcn_raw_buffer_store_b64(__builtin_bit_cast(u32x2, f32x2{a, b}), rs, lane * 8, WS_PS + ((panel * 14 + slice) * 128 + lw * 32) * 8, 16);
    }
    if (bi >= 28) {
      wait_vm<0>();
      f1_publish(ws, lds_addr(lds) + F1_FL + 16, panel);
      STAMP(1, 9);
      return;
    }
    const int tgp = (xcd & 1) * 16 + (xcd >> 1) * 4 + (bi & 3), dp = bi >> 2;
    const u32 sa = WS_WD + (dp * 224 + lw * 28) * 1024, sb = WS_HF + (tgp * 224 + lw * 28) * 1024, vo = lane * 16;
    s16x8 ra[F2_REG][2];
#pragma unroll
    for (int k = 0; k < F2_REG; ++k) {
      ra[k][0] = __builtin_bit_cast(s16x8, __builtin_amdgcn_raw_buffer_load_b128(rs, vo + (k & 3) * 1024, sa + (k >> 2) * 4096, 0));
      ra[k][1] = __builtin_bit_cast(s16x8, __builtin_amdgcn_raw_buffer_load_b128(rs, vo + (k & 3) * 1024, sa + (k >> 2) * 4096 + 112 * 1024, 0));
      SCHED0();
    }
    wait_vm<2 * F2_REG>();
    f1_publish(ws, lds_addr(lds) + F1_FL + 16, panel);
    __builtin_amdgcn_s_barrier();
    asm volatile("" ::: "memory");
    STAMP(1, 4);
    const int tok0 = tgp * 64 + (lw & 1) * 32, d0 = dp * 64 + (lw >> 1) * 32;
    const u32 alds = lds_addr(lds) + lw * 32768, aux = lds_addr(lds) + F2_RES + lw * 2048;
#pragma unroll
    for (int i = 0; i < 8; ++i) {
      asm volatile("s_mov_b32 m0, %0\n\ts_nop 0\n\t"
                   "global_load_lds_dwordx4 %1, %2\n\tglobal_load_lds_dwordx4 %1, %2 offset:1024\n\t"
                   "global_load_lds_dwordx4 %1, %2 offset:2048\n\tglobal_load_lds_dwordx4 %1, %2 offset:3072"
                   :: "s"(alds + i * 4096), "v"(vo), "s"(ws + sa + (i >> 2) * 112 * 1024 + (F2_REG + (i & 3) * 4) * 1024) : "memory");
    }
    {
      const char* hr = ws + WS_HT + (tok0 >> 7) * (D * 256) + d0 * 256 + (tok0 & 127) * 2;
      asm volatile("s_mov_b32 m0, %0\n\ts_nop 0\n\tglobal_load_lds_dwordx4 %1, %2\n\tglobal_load_lds_dwordx4 %1, %3 offset:1024"
                   :: "s"(aux), "v"((lane >> 2) * 256 + (lane & 3) * 16), "s"(hr), "s"(hr + 4096 - 1024) : "memory");
    }
    if (lw == 0) {
      for (u32 spins = 0; spins < (1u << 16); ++spins) {
        const u32 c = __builtin_amdgcn_raw_buffer_load_b32(rs, 0, WS_CNT + (tgp >> 1) * 256, 16);
        if (__builtin_amdgcn_readfirstlane(c) >= 16u) break;
        __builtin_amdgcn_s_sleep(2);
        asm volatile("" ::: "memory");
      }
    }
    __builtin_amdgcn_s_barrier();
    asm volatile("" ::: "memory");
    STAMP(1, 5);
    s16x8 rb[F2_WIN][2];
    auto ldb = [&](int k) {
      rb[k % F2_WIN][0] = __builtin_bit_cast(s16x8, __builtin_amdgcn_raw_buffer_load_b128(rs, vo + (k & 3) * 1024, sb + (k >> 2) * 4096, 16));
      rb[k % F2_WIN][1] = __builtin_bit_cast(s16x8, __builtin_amdgcn_raw_buffer_load_b128(rs, vo + (k & 3) * 1024, sb + (k >> 2) * 4096 + 112 * 1024, 16));
    };
    ldb(0); ldb(1);
    SCHED0();
    f32x2 ps[7];
#pragma unroll
    for (int i = 0; i < 7; ++i)
      ps[i] = __builtin_bit_cast(f32x2, __builtin_amdgcn_raw_buffer_load_b64(rs, (lane & 31) * 8 + (lane >> 5) * 7168 + (i & 3) * 1024,
                                                                             WS_PS + (tok0 >> 7) * (14 * 1024) + (tok0 & 127) * 8 + (i >> 2) * 4096, 16));
    SCHED0();
#pragma unroll
    for (int k = 2; k < F2_WIN; ++k) { ldb(k); SCHED0(); }
    f32x16 acc[4];
#pragma unroll
    for (int a = 0; a < 4; ++a)
#pragma unroll
      for (int i = 0; i < 16; ++i) acc[a][i] = 0.f;
    float rs2 = 0.f;
    u32x2 hres[4];
    s16x8 la[2][2];
#pragma unroll
    for (int k = 0; k < 28; ++k) {
      if (k + 1 >= F2_REG && k + 1 < 28) {
        la[(k + 1) & 1][0] = *(const LDS3 s16x8*)(uintptr_t)(alds + vo + (k + 1 - F2_REG) * 1024);
        la[(k + 1) & 1][1] = *(const LDS3 s16x8*)(uintptr_t)(alds + vo + (k + 1 - F2_REG) * 1024 + 16384);
      }
#pragma unroll
      for (int a = 0; a < 4; ++a) acc[a] = MFMA32(k < F2_REG ? ra[k < F2_REG ? k : 0][a >> 1] : la[k & 1][a >> 1], rb[k % F2_WIN][a & 1], acc[a]);
      SCHED0();
      if (k + F2_WIN < 28) ldb(k + F2_WIN);
      if (k == 2) {
        const f32x2 t = ((ps[0] + ps[1]) + (ps[2] + ps[3])) + ((ps[4] + ps[5]) + ps[6]);
        const float mean = half_sum(t.x) * (1.0f / D);
        const float var = fmaxf(half_sum(t.y) * (1.0f / D) - mean * mean, 0.f);
        const float r = __builtin_amdgcn_rcpf(rbf(__builtin_amdgcn_sqrtf(rbf(rbf(var) + 1e-5f))));
        rs2 = r * r;
        asm volatile("" : "+v"(rs2));
      }
      if (k == 20) {
        const u32 a = aux + (4 * (lane >> 5) + ((lane & 15) >> 2)) * 64 + ((lane >> 4) & 1) * 32 + (lane & 3) * 8;
#pragma unroll
        for (int g = 0; g < 4; ++g) hres[g] = __builtin_bit_cast(u32x2, __builtin_amdgcn_ds_read_tr16_b64_v4i16((LDS3 s16x4*)(uintptr_t)(a + g * 512)));
      }
      SCHED0();
    }
    STAMP(1, 6);
    f32x16 mine;
#pragma unroll
    for (int a = 0; a < 4; ++a) {
      if (a == lw) { mine = acc[a]; }
      else {
        LDS3 f32x4* pp = (LDS3 f32x4*)(uintptr_t)(alds + a * 4096 + vo);
#pragma unroll
        for (int g = 0; g < 4; ++g) pp[g * 64] = f32x4{acc[a][4 * g], acc[a][4 * g + 1], acc[a][4 * g + 2], acc[a][4 * g + 3]};
      }
    }
    __syncthreads();
    u32 pk[8];
    const u32 rdb = lds_addr(lds) + lw * 4096 + vo;
#pragma unroll
    for (int g = 0; g < 4; ++g) {
      const f32x4 p0 = *(const LDS3 f32x4*)(uintptr_t)(rdb + ((lw + 1) & 3) * 32768 + g * 1024);
      const f32x4 p1 = *(const LDS3 f32x4*)(uintptr_t)(rdb + ((lw + 2) & 3) * 32768 + g * 1024);
      const f32x4 p2 = *(const LDS3 f32x4*)(uintptr_t)(rdb + ((lw + 3) & 3) * 32768 + g * 1024);
      const f32x4 sv = (p0 + p1) + (p2 + f32x4{mine[4 * g], mine[4 * g + 1], mine[4 * g + 2], mine[4 * g + 3]});
      const f32x2 o0 = __builtin_elementwise_fma(f32x2{sv[0], sv[1]}, f32x2{rs2, rs2}, f32x2{bf_lo(hres[g][0]), bf_hi(hres[g][0])});
      const f32x2 o1 = __builtin_elementwise_fma(f32x2{sv[2], sv[3]}, f32x2{rs2, rs2}, f32x2{bf_lo(hres[g][1]), bf_hi(hres[g][1])});
      pk[2 * g] = pack_bf(o0.x, o0.y);
      pk[2 * g + 1] = pack_bf(o1.x, o1.y);
    }
    char* ob = (char*)out + tok0 * (D * 2) + d0 * 2;
#pragma unroll
    for (int k = 0; k < 2; ++k) {
      const auto r0 = __builtin_amdgcn_permlane32_swap(pk[4 * k], pk[4 * k + 2], false, false);
      const auto r1 = __builtin_amdgcn_permlane32_swap(pk[4 * k + 1], pk[4 * k + 3], false, false);
      *(u32x4*)(ob + (u32)((lane & 31) * (D * 2) + (lane >> 5) * 16 + 32 * k)) = u32x4{r0[0], r1[0], r0[1], r1[1]};
    }
    if (blockIdx.x == 0 && tid == 256) { u32* e = (u32*)(ws + WS_EPOCH); *e = next_tag(*e) ^ 0x5bd1e995u; }
    STAMP(1, 9);
    asm volatile(".Lffn2_code_end:");
    return;
  }

  const int th = wave >> 1, g = wave & 1;
  const int hh = lane >> 5, blk = (lane >> 4) & 1, q = (lane & 15) >> 2, p = lane & 3;
  const int town = 2 * th + g, toth = 2 * th + 1 - g;
  u32 bO = lds_addr(lds) + (8 * hh + q) * 256 + ((town ^ q) * 64) + blk * 32 + 8 * p;
  u32 bX = bO ^ 64;
  u32 bA = lds_addr(lds) + F1_H + lane * 16;
  u32 bG = bA + g * 3072;
  f32x16 acc[7];
#pragma unroll
  for (int t = 0; t < 7; ++t)
#pragma unroll
    for (int i = 0; i < 16; ++i) acc[t][i] = 0.f;
  float ss0 = 0.f, ss1 = 0.f;
  auto rd = [&](int s, F1Frag& f) {
    f.b[0] = tr2(bO, s * 4096, s * 4096 + 1024);
    f.a[0] = *(const LDS3 s16x8*)(uintptr_t)(bG + s * 7168);
    f.b[1] = tr2(bX, s * 4096, s * 4096 + 1024);
    f.a[1] = *(const LDS3 s16x8*)(uintptr_t)(bG + s * 7168 + 1024);
    f.a[2] = *(const LDS3 s16x8*)(uintptr_t)(bG + s * 7168 + 2048);
    f.a[3] = *(const LDS3 s16x8*)(uintptr_t)(bA + s * 7168 + 6144);
  };
  auto msum = [&](const F1Frag& f) {
    const u32x4 bw = __builtin_bit_cast(u32x4, f.b[0]);
#pragma unroll
    for (int i = 0; i < 4; ++i) {
      ss0 += bf_lo(bw[i]); ss1 += bf_hi(bw[i]);
      asm("" : "+v"(ss0)); asm("" : "+v"(ss1));
    }
  };
  auto mm = [&](const F1Frag& f) {
    acc[0] = MFMA32(f.a[0], f.b[0], acc[0]);
    acc[3] = MFMA32(f.a[0], f.b[1], acc[3]);
    acc[1] = MFMA32(f.a[1], f.b[0], acc[1]);
    acc[4] = MFMA32(f.a[1], f.b[1], acc[4]);
    acc[2] = MFMA32(f.a[2], f.b[0], acc[2]);
    acc[5] = MFMA32(f.a[2], f.b[1], acc[5]);
    acc[6] = MFMA32(f.a[3], f.b[0], acc[6]);
    msum(f);
  };
  auto interleave = [&](int extra) {
    __builtin_amdgcn_sched_group_barrier(0x8, 1, 0);
    __builtin_amdgcn_sched_group_barrier(0x100, 3, 0);
    __builtin_amdgcn_sched_group_barrier(0x2, 1, 0);
    __builtin_amdgcn_sched_group_barrier(0x8, 1, 0);
    __builtin_amdgcn_sched_group_barrier(0x100, 3, 0);
    __builtin_amdgcn_sched_group_barrier(0x2, 1, 0);
    __builtin_amdgcn_sched_group_barrier(0x8, 1, 0);
    __builtin_amdgcn_sched_group_barrier(0x100, 2, 0);
    __builtin_amdgcn_sched_group_barrier(0x2, 2, 0);
#pragma unroll
    for (int i = 0; i < 3; ++i) {
      __builtin_amdgcn_sched_group_barrier(0x8, 1, 0);
      if (extra) __builtin_amdgcn_sched_group_barrier(0x2, 5, 0); else __builtin_amdgcn_sched_group_barrier(0x2, 4, 0);
    }
    __builtin_amdgcn_sched_group_barrier(0x8, 1, 0);
    SCHED0();
  };
  F1Frag f0, f1;
  if (lane == 0) { *(u32*)(lds + F1_FL + wave * 4) = 0u; *(u32*)(lds + F1_FL + 16) = 0u; }
  asm volatile("s_waitcnt lgkmcnt(0)" ::: "memory");
  __builtin_amdgcn_s_barrier();
  asm volatile("" ::: "memory");
  STAMP(1, 1);
  rd(0, f0);
  SCHED0();
  static_assert(F1_NST == 6, "the loop below walks the ring twice, then two more chunks");
  auto chunk = [&](int step, bool bar, bool next) {
    rd(1, f1);
    mm(f0);
    bO += step; bX += step; bA += step; bG += step;
    interleave(1);
    asm volatile("" : "+v"(bO), "+v"(bX), "+v"(bA), "+v"(bG));
    if (bar) {
      asm volatile("s_waitcnt lgkmcnt(0)" ::: "memory");
      __builtin_amdgcn_s_barrier();
      asm volatile("" ::: "memory");
    }
    SCHED0();
    if (next) rd(0, f0);
    mm(f1);
    interleave(0);
  };
#pragma unroll F1_UNR
  for (int it = 0; it < 2; ++it) {
#pragma unroll
    for (int j = 0; j < 6; ++j) chunk(j == 5 ? -5 * F1_STAGE : F1_STAGE, true, true);
  }
  chunk(F1_STAGE, false, true);
  STAMP(1, 3);
  rd(1, f1);
  const u32* ca = (const u32*)(lds + F1_CS) + lane;
  const u32 c0 = ca[g * 192], c1 = ca[g * 192 + 64], c2 = ca[g * 192 + 128], c6 = ca[384];
  msum(f0); msum(f1);
  const float nmean = half_sum(ss0 + ss1) * (-1.0f / D);
  const u32 mo = pack_bf(nmean, nmean);
  if (lane < 32) *(u32*)(lds + F1_ST + (town * 32 + lane) * 4) = mo;
  asm volatile("" ::: "memory");
  *(u32*)(lds + F1_FL + town * 4) = 1u;
  char* dsto = ws + WS_HF + ((panel * 4 + town) * 112 + slice * 7) * 1024;
  char* dstx = ws + WS_HF + ((panel * 4 + toth) * 112 + slice * 7) * 1024;
  auto fin = [&](int t, u32 mw) {
    const int ai = t == 6 ? 3 : t % 3, bi2 = (t >= 3 && t < 6) ? 1 : 0;
    const u32 cw = t == 6 ? c6 : t % 3 == 0 ? c0 : t % 3 == 1 ? c1 : c2;
    acc[t] = MFMA32(f0.a[ai], f0.b[bi2], acc[t]);
    acc[t] = MFMA32(f1.a[ai], f1.b[bi2], acc[t]);
    acc[t] = MFMA32(__builtin_bit_cast(s16x8, u32x4{cw, 0u, 0u, 0u}), __builtin_bit_cast(s16x8, u32x4{mw, 0u, 0u, 0u}), acc[t]);
  };
  auto put = [&](int t) {
    u32x4 o;
#pragma unroll
    for (int j = 0; j < 4; ++j) {
      const float h0 = relu(acc[t][2 * j]) * acc[t][8 + 2 * j], h1 = relu(acc[t][2 * j + 1]) * acc[t][9 + 2 * j];
      float a0 = h0, a1 = h1;
      asm("" : "+v"(a0)); asm("" : "+v"(a1));
      o[j] = pack_bf(a0, a1);
    }
    char* dst = (t >= 3 && t < 6) ? dstx : dsto;
    st128_dev(dst, (u32)((t == 6 ? 6144 : g * 3072 + (t % 3) * 1024) + lane * 16), o);
  };
  auto mix = [&]() {
#pragma unroll
    for (int i = 0; i < 3; ++i) {
      __builtin_amdgcn_sched_group_barrier(0x8, 1, 0);
      __builtin_amdgcn_sched_group_barrier(0x2, 7, 0);
    }
    SCHED0();
  };
  SCHED0();
  fin(0, mo); fin(1, mo);
  SCHED0();
  fin(2, mo); put(0); mix();
  u32 mx;
  {
    u32 fl, sv;
    asm volatile("1:\n\tds_read_b32 %1, %3\n\tds_read_b32 %0, %4\n\ts_waitcnt lgkmcnt(0)\n\t"
                 "v_readfirstlane_b32 %2, %1\n\ts_cmp_eq_u32 %2, 0\n\ts_cbranch_scc1 1b"
                 : "=&v"(mx), "=&v"(fl), "=&s"(sv)
                 : "v"(lds_addr(lds) + F1_FL + toth * 4), "v"(lds_addr(lds) + F1_ST + (toth * 32 + (lane & 31)) * 4) : "memory", "scc");
  }
  SCHED0();
  fin(6, mo); put(1); mix();
  fin(3, mx); put(2); mix();
  fin(4, mx); put(6); mix();
  fin(5, mx); put(3); mix();
  put(4); put(5);
  __builtin_amdgcn_s_barrier();
  STAMP(1, 4);
  wait_vm<0>();
  f1_publish(ws, lds_addr(lds) + F1_FL + 16, panel);
  STAMP(1, 9);
}

extern "C" void kernel_launch(void* const* d_in, const int* in_sizes, int n_in,
                              void* d_out, int out_size, void* d_ws, size_t ws_size,
                              hipStream_t stream) {
  const u16* x = (const u16*)d_in[0];
  char* ws = (char*)d_ws;
  k_ssm<<<256, 512, 0, stream>>>(x, (const u16*)d_in[1], (const u16*)d_in[2], (const u16*)d_in[3], (const u16*)d_in[4],
                                 (const u16*)d_in[5], (const u16*)d_in[6], (const u16*)d_in[7], (const u16*)d_in[8],
                                 (const u16*)d_in[10], (const u16*)d_in[11], (const u16*)d_in[12], ws);
  k_ffn<<<256, 512, 0, stream>>>(ws, (u16*)d_out);
}
```

```cpp
#include <hip/hip_runtime.h>
#include <hip/hip_bf16.h>
#include <stdint.h>

typedef unsigned short u16;
typedef unsigned int u32;
typedef unsigned long long u64;
typedef short s16x4 __attribute__((ext_vector_type(4)));
typedef short s16x8 __attribute__((ext_vector_type(8)));
typedef _Float16 h16x8 __attribute__((ext_vector_type(8)));
typedef _Float16 h16x2 __attribute__((ext_vector_type(2)));
typedef __bf16 bf16x2 __attribute__((ext_vector_type(2)));
typedef float f32x2 __attribute__((ext_vector_type(2)));
typedef float f32x4 __attribute__((ext_vector_type(4)));
typedef float f32x16 __attribute__((ext_vector_type(16)));
typedef u32 u32x2 __attribute__((ext_vector_type(2)));
typedef u32 u32x4 __attribute__((ext_vector_type(4)));
#define LDS3 __attribute__((address_space(3)))
#define GAS __attribute__((address_space(1)))
#define DI __device__ __forceinline__
#define SCHED0() __builtin_amdgcn_sched_barrier(0)

#define WT_AUX 16
#define POLL_SLEEP 4
#define POLL_AUX 16
constexpr int L = 2048, D = 448, NM = 256, F = 1792;

constexpr u32 WS_SG = 0;
constexpr u32 WS_EPOCH = 16384;
constexpr u32 WS_CNT = 32768;
constexpr u32 WS_HT = 65536;
constexpr u32 WS_HF = WS_HT + D * L * 2 + D * F * 2;
constexpr u32 WS_XG = WS_HF + L * F * 2;
constexpr u32 WS_WT = WS_XG + (D / 2) * L * 8;
constexpr u32 WS_CS = WS_WT + 2 * D * F * 2;
constexpr u32 WS_PS = WS_CS + 16 * 14 * 896;
constexpr u32 WS_END = WS_PS + 16 * 14 * 1024;

#define STAMP(kid, i) do {} while (0)
#define STAMP_DECL do {} while (0)

#define FST(i) do {} while (0)
#define FST_OUT() do {} while (0)
DI float bf_lo(u32 w) { return __builtin_bit_cast(float, w << 16); }
DI float bf_hi(u32 w) { return __builtin_bit_cast(float, w & 0xffff0000u); }
DI float bf1(u16 w) { return __builtin_bit_cast(float, (u32)w << 16); }
DI float rbf(float a) { return (float)(__bf16)a; }
DI u32 pack_bf(float a, float b) { bf16x2 v; v.x = (__bf16)a; v.y = (__bf16)b; return __builtin_bit_cast(u32, v); }
DI u32 pack_h(f32x2 a) { h16x2 v; v.x = (_Float16)a.x; v.y = (_Float16)a.y; return __builtin_bit_cast(u32, v); }
DI u32 lds_addr(const void* p) { return (u32)(uintptr_t)(const LDS3 void*)p; }
DI float relu(float v) { return __builtin_bit_cast(float, max(__builtin_bit_cast(int, v), 0)); }
DI u32 next_tag(u32 e) { return e * 2654435761u + 0x9E3779B9u; }

DI int wave_imax(int c) {
#define RED1(ctl) "s_nop 1\n\tv_max_i32_dpp %0, %0, %0 " ctl "\n\t"
  asm(RED1("row_shr:1 row_mask:0xf bank_mask:0xf bound_ctrl:1") RED1("row_shr:2 row_mask:0xf bank_mask:0xf bound_ctrl:1")
      RED1("row_shr:4 row_mask:0xf bank_mask:0xf bound_ctrl:1") RED1("row_shr:8 row_mask:0xf bank_mask:0xf bound_ctrl:1")
      RED1("row_bcast:15 row_mask:0xa bank_mask:0xf") RED1("row_bcast:31 row_mask:0xc bank_mask:0xf")
      "s_nop 0"
      : "+v"(c));
#undef RED1
  return __builtin_amdgcn_readlane(c, 63);
}
DI void oct_sum2(float& a, float& b) {
#define RED2(ctl) "s_nop 0\n\tv_add_f32_dpp %0, %0, %0 " ctl " row_mask:0xf bank_mask:0xf\n\tv_add_f32_dpp %1, %1, %1 " ctl " row_mask:0xf bank_mask:0xf\n\t"
  asm("s_nop 0\n\t" RED2("quad_perm:[1,0,3,2]") RED2("quad_perm:[2,3,0,1]") RED2("row_half_mirror") "s_nop 0" : "+v"(a), "+v"(b));
#undef RED2
}
DI void st128_dev(const char* base, u32 off, u32x4 v) {
  asm volatile("global_store_dwordx4 %0, %1, %2 sc1\n\ts_nop 1" :: "v"(off), "v"(v), "s"(base) : "memory");
}
DI float lane63(float v) { return __builtin_bit_cast(float, __builtin_amdgcn_readlane(__builtin_bit_cast(int, v), 63)); }
DI f32x2 cmul(f32x2 a, f32x2 b) {
  f32x2 t, o;
  asm("v_pk_mul_f32 %0, %1, %2 op_sel_hi:[0,1]" : "=v"(t) : "v"(a), "v"(b));
  asm("v_pk_fma_f32 %0, %1, %2, %3 op_sel:[1,1,0] op_sel_hi:[1,0,1] neg_lo:[0,1,0]" : "=v"(o) : "v"(a), "v"(b), "v"(t));
  return o;
}
DI u32 finish_stats(float mean, float var) { return pack_bf(mean, __builtin_amdgcn_sqrtf(rbf(rbf(fmaxf(var, 0.f)) + 1e-5f))); }
DI float dot_bf(u32 a, u32 b, float c) {
  return __builtin_amdgcn_fdot2_f32_bf16(__builtin_bit_cast(bf16x2, a), __builtin_bit_cast(bf16x2, b), c, false);
}

constexpr int KG_ROW = 1040;
constexpr int KG_CH = 53 * KG_ROW;
constexpr int KC_STRIDE = 4160;
constexpr int KC_CH = KG_CH;
constexpr int SL_RED = 4 * KC_STRIDE;
constexpr int SL_U = 2 * KG_CH;
constexpr int U_CH = (256 + L) * 2;
constexpr int SL_X = SL_U + 2 * U_CH;
constexpr int XR_ROW = 144;
constexpr int SL_XX = SL_X + 64 * XR_ROW;
constexpr int SL_T = SL_XX + 2 * L * 2;
constexpr int SL_FLAG = SL_T + 769 * 4;
constexpr int SL_CNT = SL_T + 770 * 4;
constexpr int SL_W = SL_T + 772 * 4;
constexpr int SL_TOTAL = SL_W + 32 * 448 + 64;
static_assert(SL_RED + 4 * 2048 <= KG_CH && KG_CH % 16 == 0 && SL_W % 16 == 0 && SL_TOTAL <= 163840 && KG_CH + 768 < 65536, "lds map");

struct GatherTab { u32 a[256 * 12]; };
constexpr GatherTab make_gather_tab() {
  GatherTab t = {};
  for (int tc = 0; tc < 256; ++tc)
    for (int j = 0; j < 12; ++j) {
      const int i = 2047 - 8 * tc - j;
      int slot = 768;
      if (i >= 0) {
        const int s = i < 256 ? 0 : i < 512 ? 1 : i < 1024 ? 2 : 3;
        const int lag = s ? ((i + (1 << (s - 1)) - 1 + ((i >> s) & 1)) >> s) << s : i;
        const int lo = (lag & 255) >> 3, b = lag >> 8, r = lag & 7;
        const int row = b == 0 ? r : b == 1 ? 8 + (r >> 1) : b < 4 ? 12 + 2 * (b - 2) + (r >> 2) : 12 + b;
        slot = row * 32 + lo;
      }
      t.a[(j >> 2) * 1024 + tc * 4 + (j & 3)] = SL_T + 4 * slot;
    }
  return t;
}
static __device__ const GatherTab gather_tab = make_gather_tab();

DI s16x8 tr2(u32 a, int o0, int o1) {
  const s16x4 lo = __builtin_amdgcn_ds_read_tr16_b64_v4i16((LDS3 s16x4*)(uintptr_t)(a + o0));
  const s16x4 hi = __builtin_amdgcn_ds_read_tr16_b64_v4i16((LDS3 s16x4*)(uintptr_t)(a + o1));
  return __builtin_shufflevector(lo, hi, 0, 1, 2, 3, 4, 5, 6, 7);
}
template <int N> DI void wait_vm() { asm volatile("s_waitcnt vmcnt(%c0)" :: "i"(N) : "memory"); }
DI float dot_ones(s16x8 f, float s) {
  const u32x4 w4 = __builtin_bit_cast(u32x4, f);
#pragma unroll
  for (int i = 0; i < 4; ++i) s = dot_bf(w4[i], 0x3f803f80u, s);
  return s;
}
DI float half_sum(float v) {
  const u32 w = __builtin_bit_cast(u32, v);
  const auto r = __builtin_amdgcn_permlane32_swap(w, w, false, false);
  const u32 r0 = r[0], r1 = r[1];
  return __builtin_bit_cast(float, r0) + __builtin_bit_cast(float, r1);
}
DI void set_m0(u32 v) { asm volatile("s_mov_b32 m0, %0\n\ts_nop 0" :: "s"(v) : "memory"); }
template <int R0, int R1, int R2, int R3, int R4, int R5>
DI void put6(u32 v0, u32 v1, u32 v2, u32 v3, u32 v4, u32 v5) {
  asm volatile("ds_write_addtid_b32 %0 offset:%c6\n\tds_write_addtid_b32 %1 offset:%c7\n\tds_write_addtid_b32 %2 offset:%c8\n\t"
               "ds_write_addtid_b32 %3 offset:%c9\n\tds_write_addtid_b32 %4 offset:%c10\n\tds_write_addtid_b32 %5 offset:%c11"
               :: "v"(v0), "v"(v1), "v"(v2), "v"(v3), "v"(v4), "v"(v5),
                  "i"(R0 * KG_ROW), "i"(R1 * KG_ROW), "i"(R2 * KG_ROW), "i"(R3 * KG_ROW), "i"(R4 * KG_ROW), "i"(R5 * KG_ROW)
               : "memory");
}
template <int K, int RB>
DI void kg_step(f32x2& fa, f32x2& fb, f32x2& e0, f32x2& e1, f32x2& e2, f32x2& e3, f32x2 z, f32x2 w, f32x2 c) {
  f32x2 t0, t1, t2, t3, t4, t5;
  u32 h0, h1, h2, h3, h4, h5;
#define KG_M(t, a, b) "v_pk_mul_f32 " t ", " a ", " b " op_sel_hi:[0,1]\n\t"
#define KG_F(a, b, t) "v_pk_fma_f32 " a ", " a ", " b ", " t " op_sel:[1,1,0] op_sel_hi:[1,0,1] neg_lo:[0,1,0]\n\t"
  asm volatile(KG_M("%6", "%0", "%12") KG_M("%7", "%1", "%13") KG_M("%8", "%2", "%14") KG_M("%9", "%3", "%14") KG_M("%10", "%4", "%14") KG_M("%11", "%5", "%14")
               KG_F("%0", "%12", "%6") KG_F("%1", "%13", "%7") KG_F("%2", "%14", "%8") KG_F("%3", "%14", "%9") KG_F("%4", "%14", "%10") KG_F("%5", "%14", "%11")
               : "+v"(fa), "+v"(fb), "+v"(e0), "+v"(e1), "+v"(e2), "+v"(e3),
                 "=&v"(t0), "=&v"(t1), "=&v"(t2), "=&v"(t3), "=&v"(t4), "=&v"(t5)
               : "v"(z), "v"(w), "v"(c));
#undef KG_M
#undef KG_F
  h0 = pack_h(fa); h1 = pack_h(fb); h2 = pack_h(e0); h3 = pack_h(e1); h4 = pack_h(e2); h5 = pack_h(e3);
  put6<K, RB, 21 + 4 * K, 22 + 4 * K, 23 + 4 * K, 24 + 4 * K>(h0, h1, h2, h3, h4, h5);
}
DI void group_barrier(u32 cnt_addr, u32 target) {
  u32 one = 1, v, sv;
  asm volatile("s_waitcnt lgkmcnt(0)\n\t"
               "s_mov_b64 exec, 1\n\t"
               "ds_add_u32 %2, %3\n\t"
               "s_mov_b64 exec, -1\n"
               "1:\n\t"
               "ds_read_b32 %0, %2\n\t"
               "s_waitcnt lgkmcnt(0)\n\t"
               "v_readfirstlane_b32 %1, %0\n\t"
               "s_cmp_lt_u32 %1, %4\n\t"
               "s_cbranch_scc1 1b"
               : "=&v"(v), "=&s"(sv) : "v"(cnt_addr), "v"(one), "s"(target) : "memory", "scc");
}

#define MFMA16(a, b, c) __builtin_amdgcn_mfma_f32_16x16x32_bf16((a), (b), (c), 0, 0, 0)
#define MFMA32(a, b, c) __builtin_amdgcn_mfma_f32_32x32x16_bf16((a), (b), (c), 0, 0, 0)

template <int TG> DI constexpr int conv_tile(int i) { return TG == 0 ? (i == 0 ? 7 : i == 1 ? 5 : i == 2 ? 2 : 0) : (i == 0 ? 6 : i == 1 ? 4 : i == 2 ? 3 : 1); }

struct ConvSet { u32x2 al[8], ah[8]; s16x8 b[8]; };
template <int RR, int P> DI void conv_rd(ConvSet& s, u32 ka, u32 ub) {
  asm volatile("ds_read_b64 %0, %3 offset:%c5\n\tds_read_b64 %1, %3 offset:%c6\n\tds_read_b128 %2, %4 offset:%c7"
               : "=&v"(s.al[P]), "=&v"(s.ah[P]), "=&v"(s.b[P])
               : "v"(ka), "v"(ub), "i"(512 * (7 - P) + 64 * (3 - RR)), "i"(512 * (7 - P) + 64 * (3 - RR) + 8), "i"(512 * P + 64 * (3 - RR)));
}
DI s16x8 conv_a(const ConvSet& s, int p) {
  return __builtin_bit_cast(s16x8, __builtin_shufflevector(s.al[p], s.ah[p], 0, 1, 2, 3));
}
template <int TG, int RR, bool PRE> DI void conv_step(const ConvSet& c, ConvSet& n, u32 ka, u32 ub, f32x4 (&acc)[4]) {
  constexpr int NP = TG == 0 ? 8 : 7;
  asm volatile("s_waitcnt lgkmcnt(0)" ::: "memory");
  SCHED0();
#define CV_MM(i, p) acc[i] = MFMA16(conv_a(c, p), c.b[conv_tile<TG>(i) - (p)], acc[i])
#define CV_RD(p) do { if (PRE && (p) < NP) conv_rd<PRE ? RR + 1 : RR, (p)>(n, ka, ub); SCHED0(); } while (0)
  CV_MM(0, 0); CV_RD(0); CV_MM(1, 0); CV_RD(1);
  CV_MM(0, 1); CV_RD(2); CV_MM(1, 1); CV_RD(3);
  CV_MM(0, 2); CV_RD(4); CV_MM(1, 2); CV_RD(5);
  CV_MM(0, 3); CV_RD(6); CV_MM(1, 3); CV_RD(7);
  CV_MM(0, 4); CV_MM(1, 4); CV_MM(0, 5); CV_MM(2, 0); CV_MM(0, 6); CV_MM(2, 1); CV_MM(3, 0); CV_MM(2, 2);
  if (TG == 0) { CV_MM(0, 7); CV_MM(1, 5); } else { CV_MM(2, 3); CV_MM(3, 1); }
  SCHED0();
#undef CV_MM
#undef CV_RD
}
template <int TG> DI void conv_body(u32 ka, u32 ub, f32x4 (&acc)[4]) {
  ConvSet s0, s1;
  conv_rd<0, 0>(s0, ka, ub); conv_rd<0, 1>(s0, ka, ub); conv_rd<0, 2>(s0, ka, ub); conv_rd<0, 3>(s0, ka, ub);
  conv_rd<0, 4>(s0, ka, ub); conv_rd<0, 5>(s0, ka, ub); conv_rd<0, 6>(s0, ka, ub);
  if (TG == 0) conv_rd<0, 7>(s0, ka, ub);
  conv_step<TG, 0, true>(s0, s1, ka, ub, acc);
  conv_step<TG, 1, true>(s1, s0, ka, ub, acc);
  conv_step<TG, 2, true>(s0, s1, ka, ub, acc);
  conv_step<TG, 3, false>(s1, s0, ka, ub, acc);
}

__global__ __launch_bounds__(512, 2) void k_ssm(
    const u16* x, const u16* A_re, const u16* A_im, const u16* B_re, const u16* B_im, const u16* C_re,
    const u16* C_im, const u16* log_dt, const u16* Dskip, const u16* Wg, const u16* Wu, const u16* Wd, char* ws) {
  __shared__ __attribute__((aligned(16))) char lds[SL_TOTAL];
  STAMP_DECL;
  STAMP(0, 0);
  const u32 tid = threadIdx.x;
  const u32 xcd = blockIdx.x & 7, idx = blockIdx.x >> 3;
  const bool chore = idx >= 28;
  const u32 cp = xcd * 28 + min(idx, 27u);
  const u32 xrow = (tid >> 3) * (D * 2), xb_off = xrow + min(tid & 7, 6u) * 16 + idx * (64 * D * 2) + xcd * 112;
  const u32 lane = tid & 63, tc = tid & 255;
  const u32 wave = __builtin_amdgcn_readfirstlane(tid >> 6);
  const u32 ch = wave >> 2, wq = wave & 3;
  u32 xb_lds = lds_addr(lds) + SL_X + (tid >> 3) * XR_ROW + (tid & 7) * 16;
  u32 xg_lds = lds_addr(lds) + SL_X + (tid & 31) * 2 * XR_ROW + (tid >> 5) * 4;
  u32 xg_off = WS_XG + xcd * 28 * (L * 8) + idx * 512 + (tid >> 5) * (L * 8) + (tid & 31) * 16;
  auto publish = [&](u32 tag, u32 a0, u32 a1, u32 a2, u32 a3) {
    st128_dev(ws, xg_off, u32x4{a0, tag, a1, tag});
    st128_dev(ws, xg_off + 14 * (L * 8), u32x4{a2, tag, a3, tag});
  };
  u32 xb_o = xb_off, cp4 = cp * 4;
  asm volatile("" : "+v"(xb_o), "+v"(xb_lds), "+v"(xg_lds), "+v"(xg_off));
  asm volatile("" : "+s"(cp4));
  SCHED0();
  asm volatile("" :: "s"(x), "s"(A_re), "s"(A_im), "s"(B_re), "s"(B_im), "s"(C_re), "s"(C_im), "s"(log_dt), "s"(Dskip),
               "s"(Wg), "s"(Wu), "s"(Wd), "s"(ws));
  u32x4 xr[7];
  if (wave == 7) {
    asm volatile("s_setprio 2\n\t"
                 "global_load_dwordx4 %0, %7, %8\n\tglobal_load_dwordx4 %1, %7, %8 offset:128\n\tglobal_load_dwordx4 %2, %7, %8 offset:256\n\t"
                 "global_load_dwordx4 %3, %7, %8 offset:384\n\tglobal_load_dwordx4 %4, %7, %8 offset:512\n\tglobal_load_dwordx4 %5, %7, %8 offset:640\n\t"
                 "global_load_dwordx4 %6, %7, %8 offset:768"
                 : "=&v"(xr[0]), "=&v"(xr[1]), "=&v"(xr[2]), "=&v"(xr[3]), "=&v"(xr[4]), "=&v"(xr[5]), "=&v"(xr[6])
                 : "v"(xrow + (tid & 7) * 16 + (blockIdx.x * 8 - 56) * (D * 2)), "s"(x) : "memory");
  }
  auto stats = [&](u32 tag) {
    asm volatile("" : "+v"(xr[0]), "+v"(xr[1]), "+v"(xr[2]), "+v"(xr[3]), "+v"(xr[4]), "+v"(xr[5]), "+v"(xr[6]));
    float s[4] = {0.f, 0.f, 0.f, 0.f}, q[4] = {0.f, 0.f, 0.f, 0.f};
#pragma unroll
    for (int j = 0; j < 7; ++j)
#pragma unroll
      for (int i = 0; i < 4; ++i) { s[i] = dot_bf(xr[j][i], 0x3f803f80u, s[i]); q[i] = dot_bf(xr[j][i], xr[j][i], q[i]); }
    float st = (s[0] + s[1]) + (s[2] + s[3]), qt = (q[0] + q[1]) + (q[2] + q[3]);
    oct_sum2(st, qt);
    const float mean = st * (1.0f / D);
    const u32x2 gr = u32x2{finish_stats(mean, qt * (1.0f / D) - mean * mean), tag};
    asm volatile("s_mov_b64 exec, %3\n\tglobal_store_dwordx2 %0, %1, %2 sc1\n\ts_mov_b64 exec, -1\n\ts_setprio 0"
                 :: "v"(WS_SG + blockIdx.x * 64 + lane), "v"(gr), "s"(ws), "s"(0x0101010101010101ull) : "memory");
  };
  u32x4 xb, ga0, ga1, ga2, cpf;
  u32 epoch, pa, pi_, pbr, pbi, pcr, pci, sl, sd;
  asm volatile("global_load_dwordx4 %0, %4, %5\n\t"
               "s_load_dword %1, %6, %c7\n\ts_load_dword %2, %8, %10\n\ts_load_dword %3, %9, %10"
               : "=&v"(xb), "=&s"(epoch), "=&s"(sl), "=&s"(sd)
               : "v"(xb_o), "s"(x), "s"(ws), "i"(WS_EPOCH), "s"(log_dt), "s"(Dskip), "s"(cp4) : "memory");
  SCHED0();
  if (idx < 4) {
    const GAS char* pc; u32 n1;
    asm volatile("s_getpc_b64 %0\n.Lssm_code_pc:\n\ts_mov_b32 %1, .Lffn2_code_end-.Lssm_code_pc-16" : "=s"(pc), "=s"(n1));
    asm volatile("global_load_dwordx4 %0, %1, %2" : "=&v"(cpf) : "v"(min(idx * 8192u + tid * 16u, n1)), "s"(pc) : "memory");
  }
  const bool tiler = wave < 4 && !chore;
  const u32 wslice = 4 * (xcd >> 1) + (idx & 3), wchunk = (xcd & 1) * 7 + (idx >> 2);
  u32 gu0 = 0, gu1 = 0, ga_off = tc * 16;
  if (tiler) {
    gu0 = lane * 16 + ((lane * 37) >> 9) * (F * 2 - 224) + (wchunk * 32 + wq * 8) * (F * 2) + wslice * 224;
    gu1 = gu0 + 4 * (F * 2) - 1792;
    asm volatile("" : "+v"(gu0), "+v"(gu1));
    __builtin_amdgcn_s_setprio(1);
  }
  const char* gtab = (const char*)gather_tab.a + 4096;
  const char* gtab2 = gtab + 4096;
  asm volatile("" : "+s"(gtab), "+s"(gtab2));
  asm volatile("" : "+v"(ga_off));
  if (tid == 0) *(u32x4*)(lds + SL_T + 768 * 4) = u32x4{0u, 0u, 0u, 0u};
  if (wq == 3) *(u32x2*)(lds + SL_U + ch * U_CH + lane * 8) = u32x2{0u, 0u};
  SCHED0();
  asm volatile("global_load_ushort %0, %6, %7\n\tglobal_load_ushort %1, %6, %8\n\tglobal_load_ushort %2, %6, %9\n\t"
               "global_load_ushort %3, %6, %10\n\tglobal_load_ushort %4, %6, %11\n\tglobal_load_ushort %5, %6, %12"
               : "=&v"(pa), "=&v"(pi_), "=&v"(pbr), "=&v"(pbi), "=&v"(pcr), "=&v"(pci)
               : "v"(cp * 1024 + tid * 2), "s"(A_re), "s"(A_im), "s"(B_re), "s"(B_im), "s"(C_re), "s"(C_im) : "memory");
  asm volatile("global_load_dwordx4 %0, %3, %4 offset:-4096\n\tglobal_load_dwordx4 %1, %3, %4\n\tglobal_load_dwordx4 %2, %3, %5"
               : "=&v"(ga0), "=&v"(ga1), "=&v"(ga2) : "v"(ga_off), "s"(gtab), "s"(gtab2) : "memory");

  asm volatile("s_waitcnt vmcnt(9) lgkmcnt(0)" : "+v"(xb), "+v"(cpf) :: "memory");
  asm volatile("" : "+s"(epoch), "+s"(sl), "+s"(sd));
  const u32 pl = ch ? sl >> 16 : sl, pd = ch ? sd >> 16 : sd;
  *(LDS3 u32x4*)(uintptr_t)xb_lds = xb;
  const u32 tag = next_tag(epoch);
  asm volatile("s_waitcnt lgkmcnt(0)" ::: "memory");
  __builtin_amdgcn_s_barrier();
  asm volatile("" ::: "memory");
  {
    const LDS3 u32* xr_l = (const LDS3 u32*)(uintptr_t)xg_lds;
    const u32 a0 = xr_l[0], a1 = xr_l[XR_ROW / 4], a2 = xr_l[14], a3 = xr_l[XR_ROW / 4 + 14];
    asm volatile("s_waitcnt vmcnt(0)" : "+v"(pa), "+v"(pi_), "+v"(pbr), "+v"(pbi), "+v"(pcr), "+v"(pci), "+v"(ga0), "+v"(ga1), "+v"(ga2) :: "memory");
    if (tiler) {
      const u32 lw = lds_addr(lds) + SL_W + wq * 3584;
      asm volatile("s_mov_b32 m0, %0\n\ts_mov_b64 exec, %6\n\t"
                   "global_load_lds_dwordx4 %2, %4\n\tglobal_load_lds_dwordx4 %3, %4 offset:1792\n\t"
                   "s_mov_b32 m0, %1\n\ts_nop 0\n\t"
                   "global_load_lds_dwordx4 %2, %5\n\tglobal_load_lds_dwordx4 %3, %5 offset:1792\n\t"
                   "s_mov_b64 exec, -1"
                   :: "s"(lw), "s"(lw + 896), "v"(gu0), "v"(gu1), "s"(Wg), "s"(Wu), "s"(0x00ffffffffffffffull)
                   : "memory");
    }
    if (wave == 7) stats(tag); else publish(tag, a0, a1, a2, a3);
    if (chore) {
      if (blockIdx.x == 255 && tid < 64) *(u32*)(ws + WS_CNT + tid * 256) = 0u;
      STAMP(0, 9);
      return;
    }
  }
  STAMP(0, 1);
  s16x8 tf[2][2];
  float tcs[2];
  auto tile_read = [&]() {
    const u32 hh = lane >> 5, blk = (lane >> 4) & 1, q = (lane & 15) >> 2, p = lane & 3;
    const u32 a = lds_addr(lds) + SL_W + wq * 64 + hh * 3584 + q * 224 + blk * 896 + 8 * p;
#pragma unroll
    for (int i = 0; i < 2; ++i) {
      tf[i][0] = tr2(a + 32 * i, 0, 1792); tf[i][1] = tr2(a + 32 * i, 4 * 1792, 5 * 1792);
    }
#pragma unroll
    for (int i = 0; i < 2; ++i) tcs[i] = half_sum(dot_ones(tf[i][1], dot_ones(tf[i][0], 0.f)));
  };
  auto tile_store = [&]() {
    const u32 un = wslice * 14 + wchunk;
    const u32 wt = WS_WT + (un * 14 + 2 * wq) * 1024 + lane * 16;
    const u32 cs = WS_CS + un * 896 + wq * 256 + lane * 4;
#pragma unroll
    for (int i = 0; i < 2; ++i) {
      if (i == 0 || wq < 3) {
        st128_dev(ws, wt + 1024 * i, __builtin_bit_cast(u32x4, tf[i][0]));
        st128_dev(ws, wt + 1024 * i + 7168, __builtin_bit_cast(u32x4, tf[i][1]));
        asm volatile("s_mov_b64 exec, %3\n\tglobal_store_dword %0, %1, %2 sc1\n\ts_mov_b64 exec, -1"
                     :: "v"(cs + 128 * i), "v"(tcs[i]), "s"(ws), "s"(0xffffffffull) : "memory");
      }
    }
  };
  const __amdgpu_buffer_rsrc_t rs = __builtin_amdgcn_make_buffer_rsrc((void*)ws, 0, (int)WS_END, 0x00020000);
  const u32 cnt = lds_addr(lds) + SL_CNT + ch * 4;
  f32x2 cb;
  float inv_sc, p2, rv;
  {
    const float dt = rbf(__builtin_amdgcn_exp2f(bf_lo(pl) * 1.4426950408889634f));
    p2 = (dt * bf_lo(pa)) * 1.4426950408889634f;
    rv = (dt * bf_lo(pi_)) * 0.15915494309189535f;
    cb = cmul(f32x2{bf_lo(pcr), bf_lo(pci)}, f32x2{bf_lo(pbr), bf_lo(pbi)} * dt);
    const float cbx = cb.x, cby = cb.y;
    u32 eb = (u32)wave_imax(max(__builtin_bit_cast(int, cbx) & 0x7fffffff, __builtin_bit_cast(int, cby) & 0x7fffffff)) >> 23;
    asm("s_max_u32 %0, %0, 27\n\ts_min_u32 %0, %0, 227" : "+s"(eb) :: "scc");
    inv_sc = __builtin_bit_cast(float, (eb - 9) << 23);
    cb *= __builtin_bit_cast(float, (263 - eb) << 23);
  }
  STAMP(0, 2);

  {
    set_m0(lds_addr(lds) + ch * KG_CH + wq * 256);
    auto cpow = [&](float mp, float mr) {
      const float mag = __builtin_amdgcn_exp2f(p2 * mp);
      const float t = __builtin_amdgcn_fractf(rv * mr);
      return f32x2{__builtin_amdgcn_cosf(t), __builtin_amdgcn_sinf(t)} * mag;
    };
    const f32x2 z1 = cpow(1.f, 1.f), c8 = cpow(8.f, -8.f), w256 = cpow(256.f, 256.f);
    const f32x2 z2 = cmul(z1, z1), c16 = cmul(c8, c8);
    const f32x2 z4 = cmul(z2, z2), c32 = cmul(c16, c16);
    f32x2 e0 = f32x2{1.f, 0.f}, e1 = c8, e2 = c16, e3 = cmul(c16, c8);
    f32x2 fa = cb, fb = cmul(cb, w256);
    put6<0, 8, 21, 22, 23, 24>(pack_h(fa), pack_h(fb), 0x3c00u, pack_h(e1), pack_h(e2), pack_h(e3));
    const f32x2 g1 = cmul(fb, z2), g2 = cmul(fb, z4);
    kg_step<1, 12>(fa, fb, e0, e1, e2, e3, z1, w256, c32);
    const f32x2 g3 = cmul(g1, z4), g4 = cmul(fb, z4);
    kg_step<2, 14>(fa, fb, e0, e1, e2, e3, z1, w256, c32);
    const u32 h772 = pack_h(cmul(fb, z4));
    put6<9, 10, 11, 13, 15, 15>(pack_h(g1), pack_h(g2), pack_h(g3), pack_h(g4), h772, h772);
    kg_step<3, 16>(fa, fb, e0, e1, e2, e3, z1, w256, c32);
    kg_step<4, 17>(fa, fb, e0, e1, e2, e3, z1, w256, c32);
    kg_step<5, 18>(fa, fb, e0, e1, e2, e3, z1, w256, c32);
    kg_step<6, 19>(fa, fb, e0, e1, e2, e3, z1, w256, c32);
    kg_step<7, 20>(fa, fb, e0, e1, e2, e3, z1, w256, c32);
  }
  STAMP(0, 3);
  {
    char* kg = lds + ch * KG_CH + wq * 256;
    const char* fa = kg + (lane & 31) * KG_ROW + (lane >> 5) * 16;
    h16x8 a[8], b[8];
    asm volatile("s_waitcnt lgkmcnt(0)" ::: "memory");
#pragma unroll
    for (int s = 0; s < 8; ++s) { a[s] = *(const h16x8*)(fa + 32 * s); b[s] = *(const h16x8*)(fa + 21 * KG_ROW + 32 * s); }
    SCHED0();
    f32x16 acc;
#pragma unroll
    for (int i = 0; i < 16; ++i) acc[i] = 0.f;
#pragma unroll
    for (int s = 0; s < 8; ++s) acc = __builtin_amdgcn_mfma_f32_32x32x16_f16(a[s], b[s], acc, 0, 0, 0);
    char* pt = kg + (lane >> 4) * KG_ROW + (lane & 15) * 16;
#pragma unroll
    for (int q = 0; q < 3; ++q)
      *(f32x4*)(pt + 4 * q * KG_ROW) = f32x4{acc[4 * q], acc[4 * q + 1], acc[4 * q + 2], acc[4 * q + 3]} * inv_sc;
  }
  const u32 xo = WS_XG + cp * (L * 8);
  u32x4 gx[4], gs[4];
  auto repoll = [&]() {
#pragma unroll
    for (int i = 0; i < 4; ++i) {
      gx[i] = __builtin_amdgcn_raw_buffer_load_b128(rs, tc * 16, xo + 4096 * i, 16);
      gs[i] = __builtin_amdgcn_raw_buffer_load_b128(rs, tc * 16, WS_SG + 4096 * i, 16);
    }
  };
  auto poll = [&]() {
#pragma unroll
    for (int i = 0; i < 4; ++i) {
      gx[i] = __builtin_amdgcn_raw_buffer_load_b128(rs, tc * 16, xo + 4096 * i, POLL_AUX);
      gs[i] = __builtin_amdgcn_raw_buffer_load_b128(rs, tc * 16, WS_SG + 4096 * i, POLL_AUX);
    }
  };
  STAMP(0, 4);
  group_barrier(cnt, 4);
  if (wq < 3) {
    const f32x4* pt = (const f32x4*)(lds + ch * KG_CH + 4 * wq * KG_ROW + (lane >> 4) * KG_ROW + (lane & 15) * 16);
    const f32x4 s = (pt[0] + pt[16]) + (pt[32] + pt[48]);
    const u32 p01 = pack_bf(s[0], s[1]), p23 = pack_bf(s[2], s[3]);
    u16* T = (u16*)(lds + SL_T + wq * 1024 + ch * 2 + (lane >> 5) * 512 + (lane & 31) * 4);
    T[0] = (u16)p01; T[64] = (u16)(p01 >> 16); T[128] = (u16)p23; T[192] = (u16)(p23 >> 16);
  }
  if (wave == 7) asm volatile("s_waitcnt vmcnt(1)" ::: "memory"); else asm volatile("s_waitcnt vmcnt(2)" ::: "memory");
  group_barrier(cnt, 8);
  poll();
  const u32 sel = ch ? 0x07060302u : 0x05040100u;
  {
    u32 e[11];
#pragma unroll
    for (int j = 0; j < 11; ++j) {
      const u32 a = j < 4 ? ga0[j & 3] : j < 8 ? ga1[j & 3] : ga2[j & 3];
      e[j] = *(const LDS3 u32*)(uintptr_t)(a + lds_addr(lds));
    }
    char* kc = lds + ch * KC_CH + tc * 16;
#pragma unroll
    for (int s = 0; s < 4; ++s)
      *(u32x4*)(kc + s * KC_STRIDE) = u32x4{__builtin_amdgcn_perm(e[s + 1], e[s], sel), __builtin_amdgcn_perm(e[s + 3], e[s + 2], sel),
                                            __builtin_amdgcn_perm(e[s + 5], e[s + 4], sel), __builtin_amdgcn_perm(e[s + 7], e[s + 6], sel)};
    if (wq == 0 && lane < 8) *(u32x4*)(lds + ch * KC_CH + (lane >> 1) * KC_STRIDE + (256 + (lane & 1)) * 16) = u32x4{0u, 0u, 0u, 0u};
  }
  STAMP(0, 5);
  if (tiler) tile_read();
  u32 cv_ka, cv_ub, ep_lo;
  {
    const u32 b = lane & 15, g = lane >> 4, r3 = 8 * (wq >> 1) + 6;
    const u32 gg = 16 * (g & 1) - 32 * (g >> 1);
    cv_ka = lds_addr(lds) + ch * KC_CH + 3 * KC_STRIDE + (2044 - 16 * r3 - 256 * 7) * 2 - (b & 3) * KC_STRIDE - 8 * (b >> 2) + gg;
    cv_ub = lds_addr(lds) + SL_U + ch * U_CH + 512 - 32 * r3 + 32 * b + gg;
    ep_lo = 32 * b + 8 * g;
    asm volatile("" : "+v"(cv_ka), "+v"(cv_ub), "+v"(ep_lo));
  }
  {
    bool clean = true;
    for (u32 spins = 0;; ++spins) {
      u32 bad = 0;
#pragma unroll
      for (int i = 0; i < 4; ++i) bad |= ((gx[i][1] ^ tag) | (gx[i][3] ^ tag)) | ((gs[i][1] ^ tag) | (gs[i][3] ^ tag));
      if (__all(bad == 0u)) break;
      if (spins >= 4096u) { clean = false; break; }
      __builtin_amdgcn_s_sleep(POLL_SLEEP);
      asm volatile("" ::: "memory");
      repoll();
    }
    u32 xs[8], st[8];
#pragma unroll
    for (int i = 0; i < 8; ++i) { xs[i] = gx[i >> 1][2 * (i & 1)]; st[i] = gs[i >> 1][2 * (i & 1)]; }
    if (!clean) {
      *(u32*)(lds + SL_FLAG) = 1u;
#pragma unroll 1
      for (int i = 0; i < 8; ++i) {
        const u32* xrow = (const u32*)(x + (512 * (i >> 1) + 2 * tc + (i & 1)) * D);
        float s = 0.f, q = 0.f;
#pragma unroll 1
        for (int j = 0; j < D / 2; ++j) { const u32 w = xrow[j]; s = dot_bf(w, 0x3f803f80u, s); q = dot_bf(w, w, q); }
        const float mean = s * (1.0f / D);
#pragma unroll
        for (int k = 0; k < 7; ++k) { st[k] = st[k + 1]; xs[k] = xs[k + 1]; }
        st[7] = finish_stats(mean, q * (1.0f / D) - mean * mean);
        xs[7] = xrow[cp];
      }
    }
    u32x4 xw, uw;
#pragma unroll
    for (int i = 0; i < 4; ++i) {
      xw[i] = __builtin_amdgcn_perm(xs[2 * i + 1], xs[2 * i], sel);
      const f32x2 u2 = (f32x2{bf_lo(xw[i]), bf_hi(xw[i])} - f32x2{bf_lo(st[2 * i]), bf_lo(st[2 * i + 1])}) *
                       f32x2{__builtin_amdgcn_rcpf(bf_hi(st[2 * i])), __builtin_amdgcn_rcpf(bf_hi(st[2 * i + 1]))};
      uw[i] = pack_bf(u2.x, u2.y);
    }
    u32* up = (u32*)(lds + SL_U + ch * U_CH + 512) + tc;
    u32* xp = (u32*)(lds + SL_XX + ch * L * 2) + tc;
#pragma unroll
    for (int i = 0; i < 4; ++i) { up[256 * i] = uw[i]; xp[256 * i] = xw[i]; }
  }
  STAMP(0, 6);
  if (tiler) tile_store();
  group_barrier(cnt, 12);
  if (blockIdx.x == 0 && tid == 0 && *(const u32*)(lds + SL_FLAG) == 0u) *(u32*)(ws + WS_EPOCH) = tag;
  if (wave < 7)
    asm volatile("s_mov_b32 m0, %0\n\ts_nop 0\n\tglobal_load_lds_dword %1, %2"
                 :: "s"(lds_addr(lds) + SL_X), "v"(idx * 57344 + tid * 128), "s"(Wd) : "memory");
  STAMP(0, 7);
  const u32 tg = wq & 1, rh = wq >> 1;
  f32x4 acc[4];
#pragma unroll
  for (int i = 0; i < 4; ++i) acc[i] = f32x4{0.f, 0.f, 0.f, 0.f};
  if (tg == 0) conv_body<0>(cv_ka, cv_ub, acc);
  else conv_body<1>(cv_ka, cv_ub, acc);
  STAMP(0, 8);
  {
    f32x4* red = (f32x4*)(lds + ch * KG_CH + SL_RED) + lane;
    f32x4 y[2];
    if (rh) { red[wq * 128] = acc[0]; red[wq * 128 + 64] = acc[3]; y[0] = acc[1]; y[1] = acc[2]; }
    else { red[wq * 128] = acc[1]; red[wq * 128 + 64] = acc[2]; y[0] = acc[0]; y[1] = acc[3]; }
    const int t0 = rh ? 5 - tg : 7 - tg, t1 = rh ? 2 + tg : tg;
    const char* up = lds + SL_U + ch * U_CH + 512 + ep_lo;
    const char* xp = lds + SL_XX + ch * L * 2 + ep_lo;
    const u32x2 uu0 = *(const u32x2*)(up + 512 * t0), uu1 = *(const u32x2*)(up + 512 * t1);
    const u32x2 xx0 = *(const u32x2*)(xp + 512 * t0), xx1 = *(const u32x2*)(xp + 512 * t1);
    const float dsk = bf_lo(pd);
    f32x2 b0[2], b1[2];
#pragma unroll
    for (int j = 0; j < 2; ++j) {
      b0[j] = __builtin_elementwise_fma(f32x2{bf_lo(uu0[j]), bf_hi(uu0[j])}, f32x2{dsk, dsk}, f32x2{bf_lo(xx0[j]), bf_hi(xx0[j])}) + f32x2{y[0][2 * j], y[0][2 * j + 1]};
      b1[j] = __builtin_elementwise_fma(f32x2{bf_lo(uu1[j]), bf_hi(uu1[j])}, f32x2{dsk, dsk}, f32x2{bf_lo(xx1[j]), bf_hi(xx1[j])}) + f32x2{y[1][2 * j], y[1][2 * j + 1]};
    }
    asm volatile("" : "+v"(b0[0]), "+v"(b0[1]), "+v"(b1[0]), "+v"(b1[1]));
    group_barrier(cnt, 16);
    const f32x4 o0 = red[(wq ^ 2) * 128], o1 = red[(wq ^ 2) * 128 + 64];
    const u32 hb = WS_HT + (2 * cp + ch) * 256, ho = (ep_lo & 255) + (ep_lo >> 8) * (D * 256);
    __builtin_amdgcn_raw_buffer_store_b64(u32x2{pack_bf(b0[0].x + o0[0], b0[0].y + o0[1]), pack_bf(b0[1].x + o0[2], b0[1].y + o0[3])}, rs, ho, hb + 2 * D * 256 * t0, 0);
    __builtin_amdgcn_raw_buffer_store_b64(u32x2{pack_bf(b1[0].x + o1[0], b1[0].y + o1[1]), pack_bf(b1[1].x + o1[2], b1[1].y + o1[3])}, rs, ho, hb + 2 * D * 256 * t1, 0);
  }
  STAMP(0, 9);
}

constexpr int F1_H = 32 * 256;
constexpr int F1_STAGE = F1_H + 14 * 1024;
constexpr int F1_NST = 6;
constexpr int F1_CS = F1_NST * F1_STAGE;
constexpr int F1_ST = F1_CS + 7 * 256;
constexpr int F1_FL = F1_ST + 512;
constexpr int F2_RES = F1_FL + 32;
constexpr int F2_PS = F2_RES + 4 * 2048;
constexpr int F1_TOTAL = F2_PS + 2048;
constexpr int F1_PRE = 3;
#define RING_AT 0
#define CNT_SLEEP 2
#define F1_UNR 2
#define F2_WIN 8
constexpr int F2_REG = 12;
static_assert(4 * (28 - F2_REG) * 2048 <= F1_NST * F1_STAGE, "W_down part in LDS");

DI void f1_publish(char* ws, u32 arr_addr, int cnt) {
  u32 old, one = 1;
  asm volatile("s_mov_b64 exec, 1\n\tds_add_rtn_u32 %0, %1, %2\n\ts_mov_b64 exec, -1\n\ts_waitcnt lgkmcnt(0)" : "=&v"(old) : "v"(arr_addr), "v"(one) : "memory");
  if (__builtin_amdgcn_readfirstlane(old) == 7u && (threadIdx.x & 63) == 0)
    __hip_atomic_fetch_add((GAS u32*)(ws + WS_CNT + cnt * 256), 1u, __ATOMIC_RELAXED, __HIP_MEMORY_SCOPE_AGENT);
}

struct F1Frag { s16x8 a[4], b[2]; };

__global__ __launch_bounds__(512, 2) void k_ffn(char* __restrict__ ws, u16* __restrict__ out, const char* __restrict__ Wd) {
  __shared__ __attribute__((aligned(16))) char lds[F1_TOTAL];
  STAMP_DECL;
  STAMP(1, 0);
  const int tid = threadIdx.x, lane = tid & 63;
  const int wave = __builtin_amdgcn_readfirstlane(tid >> 6);
  const int xcd = blockIdx.x & 7, bi = blockIdx.x >> 3;
  const int panel = (xcd & 1) * 8 + (bi & 7), slice = (xcd >> 1) * 4 + (bi >> 3);

  if (wave >= 4) {
    const int lw = wave - 4;
    const char* hb = ws + WS_HT + panel * (D * 256);
    const char* wb = ws + WS_WT + slice * (14 * 14 * 1024);
    const int r = lane >> 4;
    const u32 ho = (u32)((lw * 8 + r) * 256 + (((lane & 15) ^ (r << 2)) * 16));
    const u32 wo = lw * 3584 + lane * 16;
    const u32 lbase = lds_addr(lds) + lw * 2048;
    auto stage = [&](int c, int slot) {
      asm volatile(
          "s_mov_b32 m0, %0\n\t"
          "s_nop 0\n\t"
          "global_load_lds_dwordx4 %2, %4\n\t"
          "global_load_lds_dwordx4 %2, %4 offset:1024\n\t"
          "s_mov_b32 m0, %1\n\t"
          "s_nop 0\n\t"
          "global_load_lds_dwordx4 %3, %5\n\t"
          "global_load_lds_dwordx4 %3, %5 offset:1024\n\t"
          "global_load_lds_dwordx4 %3, %5 offset:2048\n\t"
          "s_mov_b64 exec, %6\n\t"
          "global_load_lds_dwordx4 %3, %5 offset:3072\n\t"
          "s_mov_b64 exec, -1"
          :: "s"(lbase + slot * F1_STAGE), "s"(lbase + slot * F1_STAGE + F1_H + lw * (3584 - 2048)),
             "v"(ho), "v"(wo), "s"(hb + c * 32 * 256), "s"(wb + c * 14 * 1024), "s"(0xffffffffull)
          : "memory");
    };
    stage(0, 0);
    SCHED0();
    f32x2 psum = {0.f, 0.f};
    float v[14];
    auto part = [&]() {
      {
        const int q = (lane & 15) >> 2;
        const u32 a = lds_addr(lds) + (slice % F1_NST) * F1_STAGE + (8 * (lane >> 5) + q) * 256 + ((lw ^ q) * 64) + ((lane >> 4) & 1) * 32 + 8 * (lane & 3);
        const u32x4 n0 = __builtin_bit_cast(u32x4, tr2(a, 0, 1024)), n1 = __builtin_bit_cast(u32x4, tr2(a, 4096, 5120));
        float s0 = 0.f, s1 = 0.f, q0 = 0.f, q1 = 0.f;
#pragma unroll
        for (int i = 0; i < 8; ++i) {
          const u32 w = i < 4 ? n0[i & 3] : n1[i & 3];
          const float lo = bf_lo(w), hi = bf_hi(w);
          s0 += lo; q0 = __builtin_fmaf(lo, lo, q0);
          s1 += hi; q1 = __builtin_fmaf(hi, hi, q1);
          asm("" : "+v"(s0)); asm("" : "+v"(q0)); asm("" : "+v"(s1)); asm("" : "+v"(q1));
        }
        psum = f32x2{s0 + s1, q0 + q1};
        asm volatile("" : "+v"(psum));
      }
    };
    {
      const u32 co = min(tid - 256, 223) * 4;
      const char* cp = ws + WS_CS + slice * (14 * 896);
      asm volatile("global_load_dword %0, %14, %15\n\tglobal_load_dword %1, %14, %15 offset:896\n\tglobal_load_dword %2, %14, %15 offset:1792\n\t"
                   "global_load_dword %3, %14, %15 offset:2688\n\tglobal_load_dword %4, %14, %15 offset:3584\n\t"
                   "global_load_dword %5, %14, %16\n\tglobal_load_dword %6, %14, %16 offset:896\n\tglobal_load_dword %7, %14, %16 offset:1792\n\t"
                   "global_load_dword %8, %14, %16 offset:2688\n\tglobal_load_dword %9, %14, %16 offset:3584\n\t"
                   "global_load_dword %10, %14, %17\n\tglobal_load_dword %11, %14, %17 offset:896\n\tglobal_load_dword %12, %14, %17 offset:1792\n\t"
                   "global_load_dword %13, %14, %17 offset:2688"
                   : "=&v"(v[0]), "=&v"(v[1]), "=&v"(v[2]), "=&v"(v[3]), "=&v"(v[4]), "=&v"(v[5]), "=&v"(v[6]),
                     "=&v"(v[7]), "=&v"(v[8]), "=&v"(v[9]), "=&v"(v[10]), "=&v"(v[11]), "=&v"(v[12]), "=&v"(v[13])
                   : "v"(co), "s"(cp), "s"(cp + 4480), "s"(cp + 8960) : "memory");
    }
    stage(1, 1); stage(2, 2);
    wait_vm<2 * 6 + 14>();
    __builtin_amdgcn_s_barrier();
    asm volatile("" ::: "memory");
    int slot = F1_PRE;
#pragma unroll 1
    for (int j = 0; j < 11; ++j) {
      stage(j + F1_PRE, slot);
      slot = slot == F1_NST - 1 ? 0 : slot + 1;
      if (j == 1) {
        asm volatile("" : "+v"(v[0]), "+v"(v[1]), "+v"(v[2]), "+v"(v[3]), "+v"(v[4]), "+v"(v[5]), "+v"(v[6]),
                     "+v"(v[7]), "+v"(v[8]), "+v"(v[9]), "+v"(v[10]), "+v"(v[11]), "+v"(v[12]), "+v"(v[13]));
        const float csum = (((v[0] + v[1]) + (v[2] + v[3])) + ((v[4] + v[5]) + (v[6] + v[7]))) + (((v[8] + v[9]) + (v[10] + v[11])) + (v[12] + v[13]));
        const int i = tid - 256;
        u32* ca = (u32*)(lds + F1_CS) + (i >> 5) * 64 + (i & 31);
        if (i < 224) { ca[0] = pack_bf(csum, csum - rbf(csum)); ca[32] = 0u; }
      }
      if (j == slice) part();
      asm volatile("s_waitcnt vmcnt(12) lgkmcnt(0)" ::: "memory");
      __builtin_amdgcn_s_barrier();
      asm volatile("" ::: "memory");
    }
    wait_vm<0>();
    __builtin_amdgcn_s_barrier();
    asm volatile("" ::: "memory");
    const __amdgpu_buffer_rsrc_t rs = __builtin_amdgcn_make_buffer_rsrc((void*)ws, 0, (int)WS_END, 0x00020000);
    if (slice < 14) {
      if (slice >= 11) part();
      const float a = half_sum(psum.x), b = half_sum(psum.y);
      if (lane < 32) __builtin_amdgcn_raw_buffer_store_b64(__builtin_bit_cast(u32x2, f32x2{a, b}), rs, lane * 8, WS_PS + ((panel * 14 + slice) * 128 + lw * 32) * 8, 16);
    }
    if (bi >= 28) {
      wait_vm<0>();
      f1_publish(ws, lds_addr(lds) + F1_FL + 16, panel * 4 + (slice >> 2));
      STAMP(1, 9);
      return;
    }
    const int tgp = (xcd & 1) * 16 + (xcd >> 1) * 4 + (bi & 3), dp = bi >> 2;
    const u32 sb = WS_HF + (tgp * 224 + lw * 28) * 1024, vo = lane * 16;
    wait_vm<0>();
    f1_publish(ws, lds_addr(lds) + F1_FL + 16, panel * 4 + (slice >> 2));
    const u32 alds = lds_addr(lds) + lw * 32768, aux = lds_addr(lds) + F2_RES + lw * 2048;
    const u32 wsrc = (lane >> 3) * (D * 2) + (((lane & 7) ^ ((lane >> 2) & 4)) * 16);
    const char* wbase = Wd + lw * 448 * (D * 2) + dp * 128;
    auto wd_load = [&](int row0, int n4) {
#pragma unroll
      for (int i = 0; i < n4; ++i) {
        const char* b0 = wbase + (row0 + 32 * i) * (D * 2);
        asm volatile("s_mov_b32 m0, %0\n\ts_nop 0\n\t"
                     "global_load_lds_dwordx4 %1, %2\n\tglobal_load_lds_dwordx4 %1, %3 offset:1024\n\t"
                     "global_load_lds_dwordx4 %1, %4 offset:2048\n\tglobal_load_lds_dwordx4 %1, %5 offset:3072"
                     :: "s"(alds + i * 4096), "v"(wsrc), "s"(b0), "s"(b0 + 8 * (D * 2) - 1024), "s"(b0 + 16 * (D * 2) - 2048), "s"(b0 + 24 * (D * 2) - 3072)
                     : "memory");
      }
    };
    const u32 q4 = (lane & 15) >> 2;
    const u32 ta0 = alds + (4 * (lane >> 5) + q4) * 128 + (q4 >> 1) * 64 + ((lane >> 4) & 1) * 32 + (lane & 3) * 8, ta1 = ta0 ^ 64;
    __builtin_amdgcn_s_barrier();
    asm volatile("" ::: "memory");
    STAMP(1, 4);
    wd_load(0, F2_REG / 2);
    wait_vm<0>();
    s16x8 ra[F2_REG][2];
#pragma unroll
    for (int k = 0; k < F2_REG; ++k) { ra[k][0] = tr2(ta0, k * 2048, k * 2048 + 1024); ra[k][1] = tr2(ta1, k * 2048, k * 2048 + 1024); }
#pragma unroll
    for (int k = 0; k < F2_REG; ++k) asm volatile("" : "+v"(ra[k][0]), "+v"(ra[k][1]));
    wd_load(16 * F2_REG, (28 - F2_REG) / 2);
    const int tok0 = tgp * 64 + (lw & 1) * 32, d0 = dp * 64 + (lw >> 1) * 32;
    {
      const char* hr = ws + WS_HT + (tok0 >> 7) * (D * 256) + d0 * 256 + (tok0 & 127) * 2;
      asm volatile("s_mov_b32 m0, %0\n\ts_nop 0\n\tglobal_load_lds_dwordx4 %1, %2\n\tglobal_load_lds_dwordx4 %1, %3 offset:1024"
                   :: "s"(aux), "v"((lane >> 2) * 256 + (lane & 3) * 16), "s"(hr), "s"(hr + 4096 - 1024) : "memory");
    }
    for (u32 spins = 0; spins < (1u << 16); ++spins) {
      const u32 c = __builtin_amdgcn_raw_buffer_load_b32(rs, 0, WS_CNT + ((tgp >> 1) * 4 + lw) * 256, 16);
      if (__builtin_amdgcn_readfirstlane(c) >= 4u) break;
      __builtin_amdgcn_s_sleep(CNT_SLEEP);
      asm volatile("" ::: "memory");
    }
    asm volatile("" ::: "memory");
    STAMP(1, 5);
    s16x8 rb[F2_WIN][2];
    auto ldb = [&](int k) {
      rb[k % F2_WIN][0] = __builtin_bit_cast(s16x8, __builtin_amdgcn_raw_buffer_load_b128(rs, vo + (k & 3) * 1024, sb + (k >> 2) * 4096, 16));
      rb[k % F2_WIN][1] = __builtin_bit_cast(s16x8, __builtin_amdgcn_raw_buffer_load_b128(rs, vo + (k & 3) * 1024, sb + (k >> 2) * 4096 + 112 * 1024, 16));
    };
    ldb(0); ldb(1);
    SCHED0();
    f32x2 ps[4];
#pragma unroll
    for (int i = 0; i < 4; ++i)
      ps[i] = __builtin_bit_cast(f32x2, __builtin_amdgcn_raw_buffer_load_b64(rs, lane * 8 + (i < 2 || lw < 3 ? i : i - 2) * 1024,
                                                                             WS_PS + (tgp >> 1) * (14 * 1024) + (tgp & 1) * 512 + lw * 4096, 16));
    SCHED0();
#pragma unroll
    for (int k = 2; k < F2_WIN; ++k) { ldb(k); SCHED0(); }
    f32x16 acc[4];
#pragma unroll
    for (int a = 0; a < 4; ++a)
#pragma unroll
      for (int i = 0; i < 16; ++i) acc[a][i] = 0.f;
    u32x2 hres[4];
    s16x8 la[2][2];
#pragma unroll
    for (int k = 0; k < 28; ++k) {
      if (k + 1 >= F2_REG && k + 1 < 28) {
        la[(k + 1) & 1][0] = tr2(ta0, (k + 1 - F2_REG) * 2048, (k + 1 - F2_REG) * 2048 + 1024);
        la[(k + 1) & 1][1] = tr2(ta1, (k + 1 - F2_REG) * 2048, (k + 1 - F2_REG) * 2048 + 1024);
      }
#pragma unroll
      for (int a = 0; a < 4; ++a) acc[a] = MFMA32(k < F2_REG ? ra[k < F2_REG ? k : 0][a >> 1] : la[k & 1][a >> 1], rb[k % F2_WIN][a & 1], acc[a]);
      SCHED0();
      if (k + F2_WIN < 28) ldb(k + F2_WIN);
      if (k == 2) {
        const f32x2 t = lw < 3 ? (ps[0] + ps[1]) + (ps[2] + ps[3]) : ps[0] + ps[1];
        *(LDS3 f32x2*)(uintptr_t)(lds_addr(lds) + F2_PS + lw * 512 + lane * 8) = t;
      }
      if (k == 20) {
        const u32 a = aux + (4 * (lane >> 5) + ((lane & 15) >> 2)) * 64 + ((lane >> 4) & 1) * 32 + (lane & 3) * 8;
#pragma unroll
        for (int g = 0; g < 4; ++g) hres[g] = __builtin_bit_cast(u32x2, __builtin_amdgcn_ds_read_tr16_b64_v4i16((LDS3 s16x4*)(uintptr_t)(a + g * 512)));
      }
      SCHED0();
    }
    STAMP(1, 6);
    f32x16 mine;
#pragma unroll
    for (int a = 0; a < 4; ++a) {
      if (a == lw) { mine = acc[a]; }
      else {
        LDS3 f32x4* pp = (LDS3 f32x4*)(uintptr_t)(alds + a * 4096 + vo);
#pragma unroll
        for (int g = 0; g < 4; ++g) pp[g * 64] = f32x4{acc[a][4 * g], acc[a][4 * g + 1], acc[a][4 * g + 2], acc[a][4 * g + 3]};
      }
    }
    __syncthreads();
    float rs2;
    {
      const LDS3 f32x2* pq = (const LDS3 f32x2*)(uintptr_t)(lds_addr(lds) + F2_PS + ((lw & 1) * 32 + (lane & 31)) * 8);
      const f32x2 t = (pq[0] + pq[64]) + (pq[128] + pq[192]);
      const float mean = t.x * (1.0f / D);
      const float var = fmaxf(t.y * (1.0f / D) - mean * mean, 0.f);
      const float r = __builtin_amdgcn_rcpf(rbf(__builtin_amdgcn_sqrtf(rbf(rbf(var) + 1e-5f))));
      rs2 = r * r;
    }
    u32 pk[8];
    const u32 rdb = lds_addr(lds) + lw * 4096 + vo;
#pragma unroll
    for (int g = 0; g < 4; ++g) {
      const f32x4 p0 = *(const LDS3 f32x4*)(uintptr_t)(rdb + ((lw + 1) & 3) * 32768 + g * 1024);
      const f32x4 p1 = *(const LDS3 f32x4*)(uintptr_t)(rdb + ((lw + 2) & 3) * 32768 + g * 1024);
      const f32x4 p2 = *(const LDS3 f32x4*)(uintptr_t)(rdb + ((lw + 3) & 3) * 32768 + g * 1024);
      const f32x4 sv = (p0 + p1) + (p2 + f32x4{mine[4 * g], mine[4 * g + 1], mine[4 * g + 2], mine[4 * g + 3]});
      const f32x2 o0 = __builtin_elementwise_fma(f32x2{sv[0], sv[1]}, f32x2{rs2, rs2}, f32x2{bf_lo(hres[g][0]), bf_hi(hres[g][0])});
      const f32x2 o1 = __builtin_elementwise_fma(f32x2{sv[2], sv[3]}, f32x2{rs2, rs2}, f32x2{bf_lo(hres[g][1]), bf_hi(hres[g][1])});
      pk[2 * g] = pack_bf(o0.x, o0.y);
      pk[2 * g + 1] = pack_bf(o1.x, o1.y);
    }
    char* ob = (char*)out + tok0 * (D * 2) + d0 * 2;
#pragma unroll
    for (int k = 0; k < 2; ++k) {
      const auto r0 = __builtin_amdgcn_permlane32_swap(pk[4 * k], pk[4 * k + 2], false, false);
      const auto r1 = __builtin_amdgcn_permlane32_swap(pk[4 * k + 1], pk[4 * k + 3], false, false);
      *(u32x4*)(ob + (u32)((lane & 31) * (D * 2) + (lane >> 5) * 16 + 32 * k)) = u32x4{r0[0], r1[0], r0[1], r1[1]};
    }
    if (blockIdx.x == 0 && tid == 256) { u32* e = (u32*)(ws + WS_EPOCH); *e = next_tag(*e) ^ 0x5bd1e995u; }
    STAMP(1, 9);
    asm volatile(".Lffn2_code_end:");
    return;
  }

  const int th = wave >> 1, g = wave & 1;
  const int hh = lane >> 5, blk = (lane >> 4) & 1, q = (lane & 15) >> 2, p = lane & 3;
  const int town = 2 * th + g, toth = 2 * th + 1 - g;
  u32 bO = lds_addr(lds) + (8 * hh + q) * 256 + ((town ^ q) * 64) + blk * 32 + 8 * p;
  u32 bX = bO ^ 64;
  u32 bA = lds_addr(lds) + F1_H + lane * 16;
  u32 bG = bA + g * 3072;
  f32x16 acc[7];
#pragma unroll
  for (int t = 0; t < 7; ++t)
#pragma unroll
    for (int i = 0; i < 16; ++i) acc[t][i] = 0.f;
  float ss0 = 0.f, ss1 = 0.f;
  auto rd = [&](int s, F1Frag& f) {
    f.b[0] = tr2(bO, s * 4096, s * 4096 + 1024);
    f.a[0] = *(const LDS3 s16x8*)(uintptr_t)(bG + s * 7168);
    f.b[1] = tr2(bX, s * 4096, s * 4096 + 1024);
    f.a[1] = *(const LDS3 s16x8*)(uintptr_t)(bG + s * 7168 + 1024);
    f.a[2] = *(const LDS3 s16x8*)(uintptr_t)(bG + s * 7168 + 2048);
    f.a[3] = *(const LDS3 s16x8*)(uintptr_t)(bA + s * 7168 + 6144);
  };
  auto msum = [&](const F1Frag& f) {
    const u32x4 bw = __builtin_bit_cast(u32x4, f.b[0]);
#pragma unroll
    for (int i = 0; i < 4; ++i) {
      ss0 += bf_lo(bw[i]); ss1 += bf_hi(bw[i]);
      asm("" : "+v"(ss0)); asm("" : "+v"(ss1));
    }
  };
  auto mm = [&](const F1Frag& f) {
    acc[0] = MFMA32(f.a[0], f.b[0], acc[0]);
    acc[3] = MFMA32(f.a[0], f.b[1], acc[3]);
    acc[1] = MFMA32(f.a[1], f.b[0], acc[1]);
    acc[4] = MFMA32(f.a[1], f.b[1], acc[4]);
    acc[2] = MFMA32(f.a[2], f.b[0], acc[2]);
    acc[5] = MFMA32(f.a[2], f.b[1], acc[5]);
    acc[6] = MFMA32(f.a[3], f.b[0], acc[6]);
    msum(f);
  };
  auto interleave = [&](int extra) {
    __builtin_amdgcn_sched_group_barrier(0x8, 1, 0);
    __builtin_amdgcn_sched_group_barrier(0x100, 3, 0);
    __builtin_amdgcn_sched_group_barrier(0x2, 1, 0);
    __builtin_amdgcn_sched_group_barrier(0x8, 1, 0);
    __builtin_amdgcn_sched_group_barrier(0x100, 3, 0);
    __builtin_amdgcn_sched_group_barrier(0x2, 1, 0);
    __builtin_amdgcn_sched_group_barrier(0x8, 1, 0);
    __builtin_amdgcn_sched_group_barrier(0x100, 2, 0);
    __builtin_amdgcn_sched_group_barrier(0x2, 2, 0);
#pragma unroll
    for (int i = 0; i < 3; ++i) {
      __builtin_amdgcn_sched_group_barrier(0x8, 1, 0);
      if (extra) __builtin_amdgcn_sched_group_barrier(0x2, 5, 0); else __builtin_amdgcn_sched_group_barrier(0x2, 4, 0);
    }
    __builtin_amdgcn_sched_group_barrier(0x8, 1, 0);
    SCHED0();
  };
  char* dsto = ws + WS_HF + ((panel * 4 + town) * 112 + slice * 7) * 1024;
  char* dstx = ws + WS_HF + ((panel * 4 + toth) * 112 + slice * 7) * 1024;
  const u32 dsl = lane * 16, dsg = g * 3072 + lane * 16;
  auto claim = [&](int t) {
  };
  F1Frag f0, f1;
  if (lane == 0) { *(u32*)(lds + F1_FL + wave * 4) = 0u; *(u32*)(lds + F1_FL + 16) = 0u; }
  asm volatile("s_waitcnt lgkmcnt(0)" ::: "memory");
  __builtin_amdgcn_s_barrier();
  asm volatile("" ::: "memory");
  STAMP(1, 1);
  rd(0, f0);
  SCHED0();
  static_assert(F1_NST == 6, "the loop below walks the ring twice, then two more chunks");
  auto chunk = [&](int step, bool bar, bool next) {
    rd(1, f1);
    mm(f0);
    bO += step; bX += step; bA += step; bG += step;
    interleave(1);
    asm volatile("" : "+v"(bO), "+v"(bX), "+v"(bA), "+v"(bG));
    if (bar) {
      asm volatile("s_waitcnt lgkmcnt(0)" ::: "memory");
      __builtin_amdgcn_s_barrier();
      asm volatile("" ::: "memory");
    }
    SCHED0();
    if (next) rd(0, f0);
    mm(f1);
    interleave(0);
  };
#pragma unroll F1_UNR
  for (int it = 0; it < 2; ++it) {
#pragma unroll
    for (int j = 0; j < 6; ++j) {
      if (it == 0) claim(j); else if (j == 0) claim(6);
      chunk(j == 5 ? -5 * F1_STAGE : F1_STAGE, true, true);
    }
  }
  chunk(F1_STAGE, false, true);
  STAMP(1, 3);
  rd(1, f1);
  const u32* ca = (const u32*)(lds + F1_CS) + lane;
  const u32 c0 = ca[g * 192], c1 = ca[g * 192 + 64], c2 = ca[g * 192 + 128], c6 = ca[384];
  if (RING_AT == 0) {
    asm volatile("s_waitcnt lgkmcnt(0)" ::: "memory");
    __builtin_amdgcn_s_barrier();
    asm volatile("" ::: "memory");
  }
  msum(f0); msum(f1);
  const float nmean = half_sum(ss0 + ss1) * (-1.0f / D);
  const u32 mo = pack_bf(nmean, nmean);
  if (lane < 32) *(u32*)(lds + F1_ST + (town * 32 + lane) * 4) = mo;
  asm volatile("" ::: "memory");
  *(u32*)(lds + F1_FL + town * 4) = 1u;
  auto fin = [&](int t, u32 mw) {
    const int ai = t == 6 ? 3 : t % 3, bi2 = (t >= 3 && t < 6) ? 1 : 0;
    const u32 cw = t == 6 ? c6 : t % 3 == 0 ? c0 : t % 3 == 1 ? c1 : c2;
    acc[t] = MFMA32(f0.a[ai], f0.b[bi2], acc[t]);
    acc[t] = MFMA32(f1.a[ai], f1.b[bi2], acc[t]);
    acc[t] = MFMA32(__builtin_bit_cast(s16x8, u32x4{cw, 0u, 0u, 0u}), __builtin_bit_cast(s16x8, u32x4{mw, 0u, 0u, 0u}), acc[t]);
  };
  auto put = [&](int t) {
    u32x4 o;
#pragma unroll
    for (int j = 0; j < 4; ++j) {
      const float h0 = relu(acc[t][2 * j]) * acc[t][8 + 2 * j], h1 = relu(acc[t][2 * j + 1]) * acc[t][9 + 2 * j];
      float a0 = h0, a1 = h1;
      asm("" : "+v"(a0)); asm("" : "+v"(a1));
      o[j] = pack_bf(a0, a1);
    }
    char* dst = (t >= 3 && t < 6) ? dstx : dsto;
    st128_dev(dst, t == 6 ? dsl + 6144 : dsg + (t % 3) * 1024, o);
  };
  auto mix = [&]() {
#pragma unroll
    for (int i = 0; i < 3; ++i) {
      __builtin_amdgcn_sched_group_barrier(0x8, 1, 0);
      __builtin_amdgcn_sched_group_barrier(0x2, 7, 0);
    }
    SCHED0();
  };
  SCHED0();
  fin(0, mo); fin(1, mo);
  SCHED0();
  fin(2, mo); put(0); mix();
  u32 mx;
  {
    u32 fl, sv;
    asm volatile("1:\n\tds_read_b32 %1, %3\n\tds_read_b32 %0, %4\n\ts_waitcnt lgkmcnt(0)\n\t"
                 "v_readfirstlane_b32 %2, %1\n\ts_cmp_eq_u32 %2, 0\n\ts_cbranch_scc1 1b"
                 : "=&v"(mx), "=&v"(fl), "=&s"(sv)
                 : "v"(lds_addr(lds) + F1_FL + toth * 4), "v"(lds_addr(lds) + F1_ST + (toth * 32 + (lane & 31)) * 4) : "memory", "scc");
  }
  SCHED0();
  fin(6, mo); put(1); mix();
  if (RING_AT == 2) __builtin_amdgcn_s_barrier();
  fin(3, mx); put(2); mix();
  fin(4, mx); put(6); mix();
  if (RING_AT == 4) __builtin_amdgcn_s_barrier();
  fin(5, mx); put(3); mix();
  put(4); put(5);
  if (RING_AT == 7) __builtin_amdgcn_s_barrier();
  STAMP(1, 4);
  wait_vm<0>();
  f1_publish(ws, lds_addr(lds) + F1_FL + 16, panel * 4 + (slice >> 2));
  STAMP(1, 9);
}

extern "C" void kernel_launch(void* const* d_in, const int* in_sizes, int n_in,
                              void* d_out, int out_size, void* d_ws, size_t ws_size,
                              hipStream_t stream) {
  const u16* x = (const u16*)d_in[0];
  char* ws = (char*)d_ws;
  k_ssm<<<256, 512, 0, stream>>>(x, (const u16*)d_in[1], (const u16*)d_in[2], (const u16*)d_in[3], (const u16*)d_in[4],
                                 (const u16*)d_in[5], (const u16*)d_in[6], (const u16*)d_in[7], (const u16*)d_in[8],
                                 (const u16*)d_in[10], (const u16*)d_in[11], (const u16*)d_in[12], ws);
  k_ffn<<<256, 512, 0, stream>>>(ws, (u16*)d_out, (const char*)d_in[12]);
}
```

```cpp
#include <hip/hip_runtime.h>
#include <hip/hip_bf16.h>
#include <stdint.h>

typedef unsigned short u16;
typedef unsigned int u32;
typedef unsigned long long u64;
typedef short s16x4 __attribute__((ext_vector_type(4)));
typedef short s16x8 __attribute__((ext_vector_type(8)));
typedef _Float16 h16x8 __attribute__((ext_vector_type(8)));
typedef _Float16 h16x2 __attribute__((ext_vector_type(2)));
typedef __bf16 bf16x2 __attribute__((ext_vector_type(2)));
typedef float f32x2 __attribute__((ext_vector_type(2)));
typedef float f32x4 __attribute__((ext_vector_type(4)));
typedef float f32x16 __attribute__((ext_vector_type(16)));
typedef u32 u32x2 __attribute__((ext_vector_type(2)));
typedef u32 u32x4 __attribute__((ext_vector_type(4)));
#define LDS3 __attribute__((address_space(3)))
#define GAS __attribute__((address_space(1)))
#define DI __device__ __forceinline__
#define SCHED0() __builtin_amdgcn_sched_barrier(0)

#define WT_AUX 16
#define POLL_SLEEP 4
#define POLL_AUX 16
constexpr int L = 2048, D = 448, NM = 256, F = 1792;

constexpr u32 WS_SG = 0;
constexpr u32 WS_EPOCH = 16384;
constexpr u32 WS_CNT = 32768;
constexpr u32 WS_HT = 65536;
constexpr u32 WS_HF = WS_HT + D * L * 2 + D * F * 2;
constexpr u32 WS_XG = WS_HF + L * F * 2;
constexpr u32 WS_WT = WS_XG + (D / 2) * L * 8;
constexpr u32 WS_CS = WS_WT + 2 * D * F * 2;
constexpr u32 WS_PS = WS_CS + 16 * 14 * 896;
constexpr u32 WS_END = WS_PS + 16 * 14 * 1024;

#define STAMP(kid, i) do {} while (0)
#define STAMP_DECL do {} while (0)

#define FST(i) do {} while (0)
#define FST_OUT() do {} while (0)
DI float bf_lo(u32 w) { return __builtin_bit_cast(float, w << 16); }
DI float bf_hi(u32 w) { return __builtin_bit_cast(float, w & 0xffff0000u); }
DI float bf1(u16 w) { return __builtin_bit_cast(float, (u32)w << 16); }
DI float rbf(float a) { return (float)(__bf16)a; }
DI u32 pack_bf(float a, float b) { bf16x2 v; v.x = (__bf16)a; v.y = (__bf16)b; return __builtin_bit_cast(u32, v); }
DI u32 pack_h(f32x2 a) { h16x2 v; v.x = (_Float16)a.x; v.y = (_Float16)a.y; return __builtin_bit_cast(u32, v); }
DI u32 lds_addr(const void* p) { return (u32)(uintptr_t)(const LDS3 void*)p; }
DI float relu(float v) { return __builtin_bit_cast(float, max(__builtin_bit_cast(int, v), 0)); }
DI u32 next_tag(u32 e) { return e * 2654435761u + 0x9E3779B9u; }

DI int wave_imax(int c) {
#define RED1(ctl) "s_nop 1\n\tv_max_i32_dpp %0, %0, %0 " ctl "\n\t"
  asm(RED1("row_shr:1 row_mask:0xf bank_mask:0xf bound_ctrl:1") RED1("row_shr:2 row_mask:0xf bank_mask:0xf bound_ctrl:1")
      RED1("row_shr:4 row_mask:0xf bank_mask:0xf bound_ctrl:1") RED1("row_shr:8 row_mask:0xf bank_mask:0xf bound_ctrl:1")
      RED1("row_bcast:15 row_mask:0xa bank_mask:0xf") RED1("row_bcast:31 row_mask:0xc bank_mask:0xf")
      "s_nop 0"
      : "+v"(c));
#undef RED1
  return __builtin_amdgcn_readlane(c, 63);
}
DI void oct_sum2(float& a, float& b) {
#define RED2(ctl) "s_nop 0\n\tv_add_f32_dpp %0, %0, %0 " ctl " row_mask:0xf bank_mask:0xf\n\tv_add_f32_dpp %1, %1, %1 " ctl " row_mask:0xf bank_mask:0xf\n\t"
  asm("s_nop 0\n\t" RED2("quad_perm:[1,0,3,2]") RED2("quad_perm:[2,3,0,1]") RED2("row_half_mirror") "s_nop 0" : "+v"(a), "+v"(b));
#undef RED2
}
DI void st128_dev(const char* base, u32 off, u32x4 v) {
  asm volatile("global_store_dwordx4 %0, %1, %2 sc1\n\ts_nop 1" :: "v"(off), "v"(v), "s"(base) : "memory");
}
DI float lane63(float v) { return __builtin_bit_cast(float, __builtin_amdgcn_readlane(__builtin_bit_cast(int, v), 63)); }
DI f32x2 cmul(f32x2 a, f32x2 b) {
  f32x2 t, o;
  asm("v_pk_mul_f32 %0, %1, %2 op_sel_hi:[0,1]" : "=v"(t) : "v"(a), "v"(b));
  asm("v_pk_fma_f32 %0, %1, %2, %3 op_sel:[1,1,0] op_sel_hi:[1,0,1] neg_lo:[0,1,0]" : "=v"(o) : "v"(a), "v"(b), "v"(t));
  return o;
}
DI u32 finish_stats(float mean, float var) { return pack_bf(mean, __builtin_amdgcn_sqrtf(rbf(rbf(fmaxf(var, 0.f)) + 1e-5f))); }
DI float dot_bf(u32 a, u32 b, float c) {
  return __builtin_amdgcn_fdot2_f32_bf16(__builtin_bit_cast(bf16x2, a), __builtin_bit_cast(bf16x2, b), c, false);
}

constexpr int KG_ROW = 1040;
constexpr int KG_CH = 53 * KG_ROW;
constexpr int KC_STRIDE = 4160;
constexpr int KC_CH = KG_CH;
constexpr int SL_RED = 4 * KC_STRIDE;
constexpr int SL_U = 2 * KG_CH;
constexpr int U_CH = (256 + L) * 2;
constexpr int SL_X = SL_U + 2 * U_CH;
constexpr int XR_ROW = 144;
constexpr int SL_XX = SL_X + 64 * XR_ROW;
constexpr int SL_T = SL_XX + 2 * L * 2;
constexpr int SL_FLAG = SL_T + 769 * 4;
constexpr int SL_CNT = SL_T + 770 * 4;
constexpr int SL_W = SL_T + 772 * 4;
constexpr int SL_TOTAL = SL_W + 32 * 448 + 64;
static_assert(SL_RED + 4 * 2048 <= KG_CH && KG_CH % 16 == 0 && SL_W % 16 == 0 && SL_TOTAL <= 163840 && KG_CH + 768 < 65536, "lds map");

struct GatherTab { u32 a[256 * 12]; };
constexpr GatherTab make_gather_tab() {
  GatherTab t = {};
  for (int tc = 0; tc < 256; ++tc)
    for (int j = 0; j < 12; ++j) {
      const int i = 2047 - 8 * tc - j;
      int slot = 768;
      if (i >= 0) {
        const int s = i < 256 ? 0 : i < 512 ? 1 : i < 1024 ? 2 : 3;
        const int lag = s ? ((i + (1 << (s - 1)) - 1 + ((i >> s) & 1)) >> s) << s : i;
        const int lo = (lag & 255) >> 3, b = lag >> 8, r = lag & 7;
        const int row = b == 0 ? r : b == 1 ? 8 + (r >> 1) : b < 4 ? 12 + 2 * (b - 2) + (r >> 2) : 12 + b;
        slot = row * 32 + lo;
      }
      t.a[(j >> 2) * 1024 + tc * 4 + (j & 3)] = SL_T + 4 * slot;
    }
  return t;
}
static __device__ const GatherTab gather_tab = make_gather_tab();

DI s16x8 tr2(u32 a, int o0, int o1) {
  const s16x4 lo = __builtin_amdgcn_ds_read_tr16_b64_v4i16((LDS3 s16x4*)(uintptr_t)(a + o0));
  const s16x4 hi = __builtin_amdgcn_ds_read_tr16_b64_v4i16((LDS3 s16x4*)(uintptr_t)(a + o1));
  return __builtin_shufflevector(lo, hi, 0, 1, 2, 3, 4, 5, 6, 7);
}
template <int N> DI void wait_vm() { asm volatile("s_waitcnt vmcnt(%c0)" :: "i"(N) : "memory"); }
DI float dot_ones(s16x8 f, float s) {
  const u32x4 w4 = __builtin_bit_cast(u32x4, f);
#pragma unroll
  for (int i = 0; i < 4; ++i) s = dot_bf(w4[i], 0x3f803f80u, s);
  return s;
}
DI float half_sum(float v) {
  const u32 w = __builtin_bit_cast(u32, v);
  const auto r = __builtin_amdgcn_permlane32_swap(w, w, false, false);
  const u32 r0 = r[0], r1 = r[1];
  return __builtin_bit_cast(float, r0) + __builtin_bit_cast(float, r1);
}
DI void set_m0(u32 v) { asm volatile("s_mov_b32 m0, %0\n\ts_nop 0" :: "s"(v) : "memory"); }
template <int R0, int R1, int R2, int R3, int R4, int R5>
DI void put6(u32 v0, u32 v1, u32 v2, u32 v3, u32 v4, u32 v5) {
  asm volatile("ds_write_addtid_b32 %0 offset:%c6\n\tds_write_addtid_b32 %1 offset:%c7\n\tds_write_addtid_b32 %2 offset:%c8\n\t"
               "ds_write_addtid_b32 %3 offset:%c9\n\tds_write_addtid_b32 %4 offset:%c10\n\tds_write_addtid_b32 %5 offset:%c11"
               :: "v"(v0), "v"(v1), "v"(v2), "v"(v3), "v"(v4), "v"(v5),
                  "i"(R0 * KG_ROW), "i"(R1 * KG_ROW), "i"(R2 * KG_ROW), "i"(R3 * KG_ROW), "i"(R4 * KG_ROW), "i"(R5 * KG_ROW)
               : "memory");
}
template <int K, int RB>
DI void kg_step(f32x2& fa, f32x2& fb, f32x2& e0, f32x2& e1, f32x2& e2, f32x2& e3, f32x2 z, f32x2 w, f32x2 c) {
  f32x2 t0, t1, t2, t3, t4, t5;
  u32 h0, h1, h2, h3, h4, h5;
#define KG_M(t, a, b) "v_pk_mul_f32 " t ", " a ", " b " op_sel_hi:[0,1]\n\t"
#define KG_F(a, b, t) "v_pk_fma_f32 " a ", " a ", " b ", " t " op_sel:[1,1,0] op_sel_hi:[1,0,1] neg_lo:[0,1,0]\n\t"
  asm volatile(KG_M("%6", "%0", "%12") KG_M("%7", "%1", "%13") KG_M("%8", "%2", "%14") KG_M("%9", "%3", "%14") KG_M("%10", "%4", "%14") KG_M("%11", "%5", "%14")
               KG_F("%0", "%12", "%6") KG_F("%1", "%13", "%7") KG_F("%2", "%14", "%8") KG_F("%3", "%14", "%9") KG_F("%4", "%14", "%10") KG_F("%5", "%14", "%11")
               : "+v"(fa), "+v"(fb), "+v"(e0), "+v"(e1), "+v"(e2), "+v"(e3),
                 "=&v"(t0), "=&v"(t1), "=&v"(t2), "=&v"(t3), "=&v"(t4), "=&v"(t5)
               : "v"(z), "v"(w), "v"(c));
#undef KG_M
#undef KG_F
  h0 = pack_h(fa); h1 = pack_h(fb); h2 = pack_h(e0); h3 = pack_h(e1); h4 = pack_h(e2); h5 = pack_h(e3);
  put6<K, RB, 21 + 4 * K, 22 + 4 * K, 23 + 4 * K, 24 + 4 * K>(h0, h1, h2, h3, h4, h5);
}
DI void group_barrier(u32 cnt_addr, u32 target) {
  u32 one = 1, v, sv;
  asm volatile("s_waitcnt lgkmcnt(0)\n\t"
               "s_mov_b64 exec, 1\n\t"
               "ds_add_u32 %2, %3\n\t"
               "s_mov_b64 exec, -1\n"
               "1:\n\t"
               "ds_read_b32 %0, %2\n\t"
               "s_waitcnt lgkmcnt(0)\n\t"
               "v_readfirstlane_b32 %1, %0\n\t"
               "s_cmp_lt_u32 %1, %4\n\t"
               "s_cbranch_scc1 1b"
               : "=&v"(v), "=&s"(sv) : "v"(cnt_addr), "v"(one), "s"(target) : "memory", "scc");
}

#define MFMA16(a, b, c) __builtin_amdgcn_mfma_f32_16x16x32_bf16((a), (b), (c), 0, 0, 0)
#define MFMA32(a, b, c) __builtin_amdgcn_mfma_f32_32x32x16_bf16((a), (b), (c), 0, 0, 0)

template <int TG> DI constexpr int conv_tile(int i) { return TG == 0 ? (i == 0 ? 7 : i == 1 ? 5 : i == 2 ? 2 : 0) : (i == 0 ? 6 : i == 1 ? 4 : i == 2 ? 3 : 1); }

struct ConvSet { u32x2 al[8], ah[8]; s16x8 b[8]; };
template <int RR, int P> DI void conv_rd(ConvSet& s, u32 ka, u32 ub) {
  asm volatile("ds_read_b64 %0, %3 offset:%c5\n\tds_read_b64 %1, %3 offset:%c6\n\tds_read_b128 %2, %4 offset:%c7"
               : "=&v"(s.al[P]), "=&v"(s.ah[P]), "=&v"(s.b[P])
               : "v"(ka), "v"(ub), "i"(512 * (7 - P) + 64 * (3 - RR)), "i"(512 * (7 - P) + 64 * (3 - RR) + 8), "i"(512 * P + 64 * (3 - RR)));
}
DI s16x8 conv_a(const ConvSet& s, int p) {
  return __builtin_bit_cast(s16x8, __builtin_shufflevector(s.al[p], s.ah[p], 0, 1, 2, 3));
}
template <int TG, int RR, bool PRE> DI void conv_step(const ConvSet& c, ConvSet& n, u32 ka, u32 ub, f32x4 (&acc)[4]) {
  constexpr int NP = TG == 0 ? 8 : 7;
  asm volatile("s_waitcnt lgkmcnt(0)" ::: "memory");
  SCHED0();
#define CV_MM(i, p) acc[i] = MFMA16(conv_a(c, p), c.b[conv_tile<TG>(i) - (p)], acc[i])
#define CV_RD(p) do { if (PRE && (p) < NP) conv_rd<PRE ? RR + 1 : RR, (p)>(n, ka, ub); SCHED0(); } while (0)
  CV_MM(0, 0); CV_RD(0); CV_MM(1, 0); CV_RD(1);
  CV_MM(0, 1); CV_RD(2); CV_MM(1, 1); CV_RD(3);
  CV_MM(0, 2); CV_RD(4); CV_MM(1, 2); CV_RD(5);
  CV_MM(0, 3); CV_RD(6); CV_MM(1, 3); CV_RD(7);
  CV_MM(0, 4); CV_MM(1, 4); CV_MM(0, 5); CV_MM(2, 0); CV_MM(0, 6); CV_MM(2, 1); CV_MM(3, 0); CV_MM(2, 2);
  if (TG == 0) { CV_MM(0, 7); CV_MM(1, 5); } else { CV_MM(2, 3); CV_MM(3, 1); }
  SCHED0();
#undef CV_MM
#undef CV_RD
}
template <int TG> DI void conv_body(u32 ka, u32 ub, f32x4 (&acc)[4]) {
  ConvSet s0, s1;
  conv_rd<0, 0>(s0, ka, ub); conv_rd<0, 1>(s0, ka, ub); conv_rd<0, 2>(s0, ka, ub); conv_rd<0, 3>(s0, ka, ub);
  conv_rd<0, 4>(s0, ka, ub); conv_rd<0, 5>(s0, ka, ub); conv_rd<0, 6>(s0, ka, ub);
  if (TG == 0) conv_rd<0, 7>(s0, ka, ub);
  conv_step<TG, 0, true>(s0, s1, ka, ub, acc);
  conv_step<TG, 1, true>(s1, s0, ka, ub, acc);
  conv_step<TG, 2, true>(s0, s1, ka, ub, acc);
  conv_step<TG, 3, false>(s1, s0, ka, ub, acc);
}

__global__ __launch_bounds__(512, 2) void k_ssm(
    const u16* x, const u16* A_re, const u16* A_im, const u16* B_re, const u16* B_im, const u16* C_re,
    const u16* C_im, const u16* log_dt, const u16* Dskip, const u16* Wg, const u16* Wu, const u16* Wd, char* ws) {
  __shared__ __attribute__((aligned(16))) char lds[SL_TOTAL];
  STAMP_DECL;
  STAMP(0, 0);
  const u32 tid = threadIdx.x;
  const u32 xcd = blockIdx.x & 7, idx = blockIdx.x >> 3;
  const bool chore = idx >= 28;
  const u32 cp = xcd * 28 + min(idx, 27u);
  const u32 xrow = (tid >> 3) * (D * 2), xb_off = xrow + min(tid & 7, 6u) * 16 + idx * (64 * D * 2) + xcd * 112;
  const u32 lane = tid & 63, tc = tid & 255;
  const u32 wave = __builtin_amdgcn_readfirstlane(tid >> 6);
  const u32 ch = wave >> 2, wq = wave & 3;
  u32 xb_lds = lds_addr(lds) + SL_X + (tid >> 3) * XR_ROW + (tid & 7) * 16;
  u32 xg_lds = lds_addr(lds) + SL_X + (tid & 31) * 2 * XR_ROW + (tid >> 5) * 4;
  u32 xg_off = WS_XG + xcd * 28 * (L * 8) + idx * 512 + (tid >> 5) * (L * 8) + (tid & 31) * 16;
  auto publish = [&](u32 tag, u32 a0, u32 a1, u32 a2, u32 a3) {
    st128_dev(ws, xg_off, u32x4{a0, tag, a1, tag});
    st128_dev(ws, xg_off + 14 * (L * 8), u32x4{a2, tag, a3, tag});
  };
  u32 xb_o = xb_off, cp4 = cp * 4;
  asm volatile("" : "+v"(xb_o), "+v"(xb_lds), "+v"(xg_lds), "+v"(xg_off));
  asm volatile("" : "+s"(cp4));
  SCHED0();
  asm volatile("" :: "s"(x), "s"(A_re), "s"(A_im), "s"(B_re), "s"(B_im), "s"(C_re), "s"(C_im), "s"(log_dt), "s"(Dskip),
               "s"(Wg), "s"(Wu), "s"(Wd), "s"(ws));
  u32x4 xr[7];
  if (wave == 7) {
    asm volatile("s_setprio 2\n\t"
                 "global_load_dwordx4 %0, %7, %8\n\tglobal_load_dwordx4 %1, %7, %8 offset:128\n\tglobal_load_dwordx4 %2, %7, %8 offset:256\n\t"
                 "global_load_dwordx4 %3, %7, %8 offset:384\n\tglobal_load_dwordx4 %4, %7, %8 offset:512\n\tglobal_load_dwordx4 %5, %7, %8 offset:640\n\t"
                 "global_load_dwordx4 %6, %7, %8 offset:768"
                 : "=&v"(xr[0]), "=&v"(xr[1]), "=&v"(xr[2]), "=&v"(xr[3]), "=&v"(xr[4]), "=&v"(xr[5]), "=&v"(xr[6])
                 : "v"(xrow + (tid & 7) * 16 + (blockIdx.x * 8 - 56) * (D * 2)), "s"(x) : "memory");
  }
  auto stats = [&](u32 tag) {
    asm volatile("" : "+v"(xr[0]), "+v"(xr[1]), "+v"(xr[2]), "+v"(xr[3]), "+v"(xr[4]), "+v"(xr[5]), "+v"(xr[6]));
    float s[4] = {0.f, 0.f, 0.f, 0.f}, q[4] = {0.f, 0.f, 0.f, 0.f};
#pragma unroll
    for (int j = 0; j < 7; ++j)
#pragma unroll
      for (int i = 0; i < 4; ++i) { s[i] = dot_bf(xr[j][i], 0x3f803f80u, s[i]); q[i] = dot_bf(xr[j][i], xr[j][i], q[i]); }
    float st = (s[0] + s[1]) + (s[2] + s[3]), qt = (q[0] + q[1]) + (q[2] + q[3]);
    oct_sum2(st, qt);
    const float mean = st * (1.0f / D);
    const u32x2 gr = u32x2{finish_stats(mean, qt * (1.0f / D) - mean * mean), tag};
    asm volatile("s_mov_b64 exec, %3\n\tglobal_store_dwordx2 %0, %1, %2 sc1\n\ts_mov_b64 exec, -1\n\ts_setprio 0"
                 :: "v"(WS_SG + blockIdx.x * 64 + lane), "v"(gr), "s"(ws), "s"(0x0101010101010101ull) : "memory");
  };
  u32x4 xb, ga0, ga1, ga2, cpf;
  u32 epoch, pa, pi_, pbr, pbi, pcr, pci, sl, sd;
  asm volatile("global_load_dwordx4 %0, %4, %5\n\t"
               "s_load_dword %1, %6, %c7\n\ts_load_dword %2, %8, %10\n\ts_load_dword %3, %9, %10"
               : "=&v"(xb), "=&s"(epoch), "=&s"(sl), "=&s"(sd)
               : "v"(xb_o), "s"(x), "s"(ws), "i"(WS_EPOCH), "s"(log_dt), "s"(Dskip), "s"(cp4) : "memory");
  SCHED0();
  if (idx < 4) {
    const GAS char* pc; u32 n1;
    asm volatile("s_getpc_b64 %0\n.Lssm_code_pc:\n\ts_mov_b32 %1, .Lffn2_code_end-.Lssm_code_pc-16" : "=s"(pc), "=s"(n1));
    asm volatile("global_load_dwordx4 %0, %1, %2" : "=&v"(cpf) : "v"(min(idx * 8192u + tid * 16u, n1)), "s"(pc) : "memory");
  }
  const bool tiler = wave < 4 && !chore;
  const u32 wslice = 4 * (xcd >> 1) + (idx & 3), wchunk = (xcd & 1) * 7 + (idx >> 2);
  u32 gu0 = 0, gu1 = 0, ga_off = tc * 16;
  if (tiler) {
    gu0 = lane * 16 + ((lane * 37) >> 9) * (F * 2 - 224) + (wchunk * 32 + wq * 8) * (F * 2) + wslice * 224;
    gu1 = gu0 + 4 * (F * 2) - 1792;
    asm volatile("" : "+v"(gu0), "+v"(gu1));
    __builtin_amdgcn_s_setprio(1);
  }
  const char* gtab = (const char*)gather_tab.a + 4096;
  const char* gtab2 = gtab + 4096;
  asm volatile("" : "+s"(gtab), "+s"(gtab2));
  asm volatile("" : "+v"(ga_off));
  if (tid == 0) *(u32x4*)(lds + SL_T + 768 * 4) = u32x4{0u, 0u, 0u, 0u};
  if (wq == 3) *(u32x2*)(lds + SL_U + ch * U_CH + lane * 8) = u32x2{0u, 0u};
  SCHED0();
  asm volatile("global_load_ushort %0, %6, %7\n\tglobal_load_ushort %1, %6, %8\n\tglobal_load_ushort %2, %6, %9\n\t"
               "global_load_ushort %3, %6, %10\n\tglobal_load_ushort %4, %6, %11\n\tglobal_load_ushort %5, %6, %12"
               : "=&v"(pa), "=&v"(pi_), "=&v"(pbr), "=&v"(pbi), "=&v"(pcr), "=&v"(pci)
               : "v"(cp * 1024 + tid * 2), "s"(A_re), "s"(A_im), "s"(B_re), "s"(B_im), "s"(C_re), "s"(C_im) : "memory");
  asm volatile("global_load_dwordx4 %0, %3, %4 offset:-4096\n\tglobal_load_dwordx4 %1, %3, %4\n\tglobal_load_dwordx4 %2, %3, %5"
               : "=&v"(ga0), "=&v"(ga1), "=&v"(ga2) : "v"(ga_off), "s"(gtab), "s"(gtab2) : "memory");

  asm volatile("s_waitcnt vmcnt(9) lgkmcnt(0)" : "+v"(xb), "+v"(cpf) :: "memory");
  asm volatile("" : "+s"(epoch), "+s"(sl), "+s"(sd));
  const u32 pl = ch ? sl >> 16 : sl, pd = ch ? sd >> 16 : sd;
  *(LDS3 u32x4*)(uintptr_t)xb_lds = xb;
  const u32 tag = next_tag(epoch);
  asm volatile("s_waitcnt lgkmcnt(0)" ::: "memory");
  __builtin_amdgcn_s_barrier();
  asm volatile("" ::: "memory");
  {
    const LDS3 u32* xr_l = (const LDS3 u32*)(uintptr_t)xg_lds;
    const u32 a0 = xr_l[0], a1 = xr_l[XR_ROW / 4], a2 = xr_l[14], a3 = xr_l[XR_ROW / 4 + 14];
    asm volatile("s_waitcnt vmcnt(0)" : "+v"(pa), "+v"(pi_), "+v"(pbr), "+v"(pbi), "+v"(pcr), "+v"(pci), "+v"(ga0), "+v"(ga1), "+v"(ga2) :: "memory");
    if (tiler) {
      const u32 lw = lds_addr(lds) + SL_W + wq * 3584;
      asm volatile("s_mov_b32 m0, %0\n\ts_mov_b64 exec, %6\n\t"
                   "global_load_lds_dwordx4 %2, %4\n\tglobal_load_lds_dwordx4 %3, %4 offset:1792\n\t"
                   "s_mov_b32 m0, %1\n\ts_nop 0\n\t"
                   "global_load_lds_dwordx4 %2, %5\n\tglobal_load_lds_dwordx4 %3, %5 offset:1792\n\t"
                   "s_mov_b64 exec, -1"
                   :: "s"(lw), "s"(lw + 896), "v"(gu0), "v"(gu1), "s"(Wg), "s"(Wu), "s"(0x00ffffffffffffffull)
                   : "memory");
    }
    if (wave == 7) stats(tag); else publish(tag, a0, a1, a2, a3);
    if (chore) {
      if (blockIdx.x == 255 && tid < 64) *(u32*)(ws + WS_CNT + tid * 256) = 0u;
      STAMP(0, 9);
      return;
    }
  }
  STAMP(0, 1);
  s16x8 tf[2][2];
  float tcs[2];
  auto tile_read = [&]() {
    const u32 hh = lane >> 5, blk = (lane >> 4) & 1, q = (lane & 15) >> 2, p = lane & 3;
    const u32 a = lds_addr(lds) + SL_W + wq * 64 + hh * 3584 + q * 224 + blk * 896 + 8 * p;
#pragma unroll
    for (int i = 0; i < 2; ++i) {
      tf[i][0] = tr2(a + 32 * i, 0, 1792); tf[i][1] = tr2(a + 32 * i, 4 * 1792, 5 * 1792);
    }
#pragma unroll
    for (int i = 0; i < 2; ++i) tcs[i] = half_sum(dot_ones(tf[i][1], dot_ones(tf[i][0], 0.f)));
  };
  auto tile_store = [&]() {
    const u32 un = wslice * 14 + wchunk;
    const u32 wt = WS_WT + wslice * (196 * 1024) + (wq < 2 ? wchunk * 8 + 2 * wq : 112 + wchunk * 6 + 2 * wq - 4) * 1024 + lane * 16;
    const u32 ks = wq < 2 ? 4096 : 3072;
    const u32 cs = WS_CS + un * 896 + wq * 256 + lane * 4;
#pragma unroll
    for (int i = 0; i < 2; ++i) {
      if (i == 0 || wq < 3) {
        st128_dev(ws, wt + 1024 * i, __builtin_bit_cast(u32x4, tf[i][0]));
        st128_dev(ws, wt + 1024 * i + ks, __builtin_bit_cast(u32x4, tf[i][1]));
        asm volatile("s_mov_b64 exec, %3\n\tglobal_store_dword %0, %1, %2 sc1\n\ts_mov_b64 exec, -1"
                     :: "v"(cs + 128 * i), "v"(tcs[i]), "s"(ws), "s"(0xffffffffull) : "memory");
      }
    }
  };
  const __amdgpu_buffer_rsrc_t rs = __builtin_amdgcn_make_buffer_rsrc((void*)ws, 0, (int)WS_END, 0x00020000);
  const u32 cnt = lds_addr(lds) + SL_CNT + ch * 4;
  f32x2 cb;
  float inv_sc, p2, rv;
  {
    const float dt = rbf(__builtin_amdgcn_exp2f(bf_lo(pl) * 1.4426950408889634f));
    p2 = (dt * bf_lo(pa)) * 1.4426950408889634f;
    rv = (dt * bf_lo(pi_)) * 0.15915494309189535f;
    cb = cmul(f32x2{bf_lo(pcr), bf_lo(pci)}, f32x2{bf_lo(pbr), bf_lo(pbi)} * dt);
    const float cbx = cb.x, cby = cb.y;
    u32 eb = (u32)wave_imax(max(__builtin_bit_cast(int, cbx) & 0x7fffffff, __builtin_bit_cast(int, cby) & 0x7fffffff)) >> 23;
    asm("s_max_u32 %0, %0, 27\n\ts_min_u32 %0, %0, 227" : "+s"(eb) :: "scc");
    inv_sc = __builtin_bit_cast(float, (eb - 9) << 23);
    cb *= __builtin_bit_cast(float, (263 - eb) << 23);
  }
  STAMP(0, 2);

  {
    set_m0(lds_addr(lds) + ch * KG_CH + wq * 256);
    auto cpow = [&](float mp, float mr) {
      const float mag = __builtin_amdgcn_exp2f(p2 * mp);
      const float t = __builtin_amdgcn_fractf(rv * mr);
      return f32x2{__builtin_amdgcn_cosf(t), __builtin_amdgcn_sinf(t)} * mag;
    };
    const f32x2 z1 = cpow(1.f, 1.f), c8 = cpow(8.f, -8.f), w256 = cpow(256.f, 256.f);
    const f32x2 z2 = cmul(z1, z1), c16 = cmul(c8, c8);
    const f32x2 z4 = cmul(z2, z2), c32 = cmul(c16, c16);
    f32x2 e0 = f32x2{1.f, 0.f}, e1 = c8, e2 = c16, e3 = cmul(c16, c8);
    f32x2 fa = cb, fb = cmul(cb, w256);
    put6<0, 8, 21, 22, 23, 24>(pack_h(fa), pack_h(fb), 0x3c00u, pack_h(e1), pack_h(e2), pack_h(e3));
    const f32x2 g1 = cmul(fb, z2), g2 = cmul(fb, z4);
    kg_step<1, 12>(fa, fb, e0, e1, e2, e3, z1, w256, c32);
    const f32x2 g3 = cmul(g1, z4), g4 = cmul(fb, z4);
    kg_step<2, 14>(fa, fb, e0, e1, e2, e3, z1, w256, c32);
    const u32 h772 = pack_h(cmul(fb, z4));
    put6<9, 10, 11, 13, 15, 15>(pack_h(g1), pack_h(g2), pack_h(g3), pack_h(g4), h772, h772);
    kg_step<3, 16>(fa, fb, e0, e1, e2, e3, z1, w256, c32);
    kg_step<4, 17>(fa, fb, e0, e1, e2, e3, z1, w256, c32);
    kg_step<5, 18>(fa, fb, e0, e1, e2, e3, z1, w256, c32);
    kg_step<6, 19>(fa, fb, e0, e1, e2, e3, z1, w256, c32);
    kg_step<7, 20>(fa, fb, e0, e1, e2, e3, z1, w256, c32);
  }
  STAMP(0, 3);
  {
    char* kg = lds + ch * KG_CH + wq * 256;
    const char* fa = kg + (lane & 31) * KG_ROW + (lane >> 5) * 16;
    h16x8 a[8], b[8];
    asm volatile("s_waitcnt lgkmcnt(0)" ::: "memory");
#pragma unroll
    for (int s = 0; s < 8; ++s) { a[s] = *(const h16x8*)(fa + 32 * s); b[s] = *(const h16x8*)(fa + 21 * KG_ROW + 32 * s); }
    SCHED0();
    f32x16 acc;
#pragma unroll
    for (int i = 0; i < 16; ++i) acc[i] = 0.f;
#pragma unroll
    for (int s = 0; s < 8; ++s) acc = __builtin_amdgcn_mfma_f32_32x32x16_f16(a[s], b[s], acc, 0, 0, 0);
    char* pt = kg + (lane >> 4) * KG_ROW + (lane & 15) * 16;
#pragma unroll
    for (int q = 0; q < 3; ++q)
      *(f32x4*)(pt + 4 * q * KG_ROW) = f32x4{acc[4 * q], acc[4 * q + 1], acc[4 * q + 2], acc[4 * q + 3]} * inv_sc;
  }
  const u32 xo = WS_XG + cp * (L * 8);
  u32x4 gx[4], gs[4];
  auto repoll = [&]() {
#pragma unroll
    for (int i = 0; i < 4; ++i) {
      gx[i] = __builtin_amdgcn_raw_buffer_load_b128(rs, tc * 16, xo + 4096 * i, 16);
      gs[i] = __builtin_amdgcn_raw_buffer_load_b128(rs, tc * 16, WS_SG + 4096 * i, 16);
    }
  };
  auto poll = [&]() {
#pragma unroll
    for (int i = 0; i < 4; ++i) {
      gx[i] = __builtin_amdgcn_raw_buffer_load_b128(rs, tc * 16, xo + 4096 * i, POLL_AUX);
      gs[i] = __builtin_amdgcn_raw_buffer_load_b128(rs, tc * 16, WS_SG + 4096 * i, POLL_AUX);
    }
  };
  STAMP(0, 4);
  group_barrier(cnt, 4);
  if (wq < 3) {
    const f32x4* pt = (const f32x4*)(lds + ch * KG_CH + 4 * wq * KG_ROW + (lane >> 4) * KG_ROW + (lane & 15) * 16);
    const f32x4 s = (pt[0] + pt[16]) + (pt[32] + pt[48]);
    const u32 p01 = pack_bf(s[0], s[1]), p23 = pack_bf(s[2], s[3]);
    u16* T = (u16*)(lds + SL_T + wq * 1024 + ch * 2 + (lane >> 5) * 512 + (lane & 31) * 4);
    T[0] = (u16)p01; T[64] = (u16)(p01 >> 16); T[128] = (u16)p23; T[192] = (u16)(p23 >> 16);
  }
  if (wave == 7) asm volatile("s_waitcnt vmcnt(1)" ::: "memory"); else asm volatile("s_waitcnt vmcnt(2)" ::: "memory");
  group_barrier(cnt, 8);
  poll();
  const u32 sel = ch ? 0x07060302u : 0x05040100u;
  {
    u32 e[11];
#pragma unroll
    for (int j = 0; j < 11; ++j) {
      const u32 a = j < 4 ? ga0[j & 3] : j < 8 ? ga1[j & 3] : ga2[j & 3];
      e[j] = *(const LDS3 u32*)(uintptr_t)(a + lds_addr(lds));
    }
    char* kc = lds + ch * KC_CH + tc * 16;
#pragma unroll
    for (int s = 0; s < 4; ++s)
      *(u32x4*)(kc + s * KC_STRIDE) = u32x4{__builtin_amdgcn_perm(e[s + 1], e[s], sel), __builtin_amdgcn_perm(e[s + 3], e[s + 2], sel),
                                            __builtin_amdgcn_perm(e[s + 5], e[s + 4], sel), __builtin_amdgcn_perm(e[s + 7], e[s + 6], sel)};
    if (wq == 0 && lane < 8) *(u32x4*)(lds + ch * KC_CH + (lane >> 1) * KC_STRIDE + (256 + (lane & 1)) * 16) = u32x4{0u, 0u, 0u, 0u};
  }
  STAMP(0, 5);
  if (tiler) tile_read();
  u32 cv_ka, cv_ub, ep_lo;
  {
    const u32 b = lane & 15, g = lane >> 4, r3 = 8 * (wq >> 1) + 6;
    const u32 gg = 16 * (g & 1) - 32 * (g >> 1);
    cv_ka = lds_addr(lds) + ch * KC_CH + 3 * KC_STRIDE + (2044 - 16 * r3 - 256 * 7) * 2 - (b & 3) * KC_STRIDE - 8 * (b >> 2) + gg;
    cv_ub = lds_addr(lds) + SL_U + ch * U_CH + 512 - 32 * r3 + 32 * b + gg;
    ep_lo = 32 * b + 8 * g;
    asm volatile("" : "+v"(cv_ka), "+v"(cv_ub), "+v"(ep_lo));
  }
  {
    bool clean = true;
    for (u32 spins = 0;; ++spins) {
      u32 bad = 0;
#pragma unroll
      for (int i = 0; i < 4; ++i) bad |= ((gx[i][1] ^ tag) | (gx[i][3] ^ tag)) | ((gs[i][1] ^ tag) | (gs[i][3] ^ tag));
      if (__all(bad == 0u)) break;
      if (spins >= 4096u) { clean = false; break; }
      __builtin_amdgcn_s_sleep(POLL_SLEEP);
      asm volatile("" ::: "memory");
      repoll();
    }
    u32 xs[8], st[8];
#pragma unroll
    for (int i = 0; i < 8; ++i) { xs[i] = gx[i >> 1][2 * (i & 1)]; st[i] = gs[i >> 1][2 * (i & 1)]; }
    if (!clean) {
      *(u32*)(lds + SL_FLAG) = 1u;
#pragma unroll 1
      for (int i = 0; i < 8; ++i) {
        const u32* xrow = (const u32*)(x + (512 * (i >> 1) + 2 * tc + (i & 1)) * D);
        float s = 0.f, q = 0.f;
#pragma unroll 1
        for (int j = 0; j < D / 2; ++j) { const u32 w = xrow[j]; s = dot_bf(w, 0x3f803f80u, s); q = dot_bf(w, w, q); }
        const float mean = s * (1.0f / D);
#pragma unroll
        for (int k = 0; k < 7; ++k) { st[k] = st[k + 1]; xs[k] = xs[k + 1]; }
        st[7] = finish_stats(mean, q * (1.0f / D) - mean * mean);
        xs[7] = xrow[cp];
      }
    }
    u32x4 xw, uw;
#pragma unroll
    for (int i = 0; i < 4; ++i) {
      xw[i] = __builtin_amdgcn_perm(xs[2 * i + 1], xs[2 * i], sel);
      const f32x2 u2 = (f32x2{bf_lo(xw[i]), bf_hi(xw[i])} - f32x2{bf_lo(st[2 * i]), bf_lo(st[2 * i + 1])}) *
                       f32x2{__builtin_amdgcn_rcpf(bf_hi(st[2 * i])), __builtin_amdgcn_rcpf(bf_hi(st[2 * i + 1]))};
      uw[i] = pack_bf(u2.x, u2.y);
    }
    u32* up = (u32*)(lds + SL_U + ch * U_CH + 512) + tc;
    u32* xp = (u32*)(lds + SL_XX + ch * L * 2) + tc;
#pragma unroll
    for (int i = 0; i < 4; ++i) { up[256 * i] = uw[i]; xp[256 * i] = xw[i]; }
  }
  STAMP(0, 6);
  if (tiler) tile_store();
  group_barrier(cnt, 12);
  if (blockIdx.x == 0 && tid == 0 && *(const u32*)(lds + SL_FLAG) == 0u) *(u32*)(ws + WS_EPOCH) = tag;
  if (wave < 7)
    asm volatile("s_mov_b32 m0, %0\n\ts_nop 0\n\tglobal_load_lds_dword %1, %2"
                 :: "s"(lds_addr(lds) + SL_X), "v"(idx * 57344 + tid * 128), "s"(Wd) : "memory");
  STAMP(0, 7);
  const u32 tg = wq & 1, rh = wq >> 1;
  f32x4 acc[4];
#pragma unroll
  for (int i = 0; i < 4; ++i) acc[i] = f32x4{0.f, 0.f, 0.f, 0.f};
  if (tg == 0) conv_body<0>(cv_ka, cv_ub, acc);
  else conv_body<1>(cv_ka, cv_ub, acc);
  STAMP(0, 8);
  {
    f32x4* red = (f32x4*)(lds + ch * KG_CH + SL_RED) + lane;
    f32x4 y[2];
    if (rh) { red[wq * 128] = acc[0]; red[wq * 128 + 64] = acc[3]; y[0] = acc[1]; y[1] = acc[2]; }
    else { red[wq * 128] = acc[1]; red[wq * 128 + 64] = acc[2]; y[0] = acc[0]; y[1] = acc[3]; }
    const int t0 = rh ? 5 - tg : 7 - tg, t1 = rh ? 2 + tg : tg;
    const char* up = lds + SL_U + ch * U_CH + 512 + ep_lo;
    const char* xp = lds + SL_XX + ch * L * 2 + ep_lo;
    const u32x2 uu0 = *(const u32x2*)(up + 512 * t0), uu1 = *(const u32x2*)(up + 512 * t1);
    const u32x2 xx0 = *(const u32x2*)(xp + 512 * t0), xx1 = *(const u32x2*)(xp + 512 * t1);
    const float dsk = bf_lo(pd);
    f32x2 b0[2], b1[2];
#pragma unroll
    for (int j = 0; j < 2; ++j) {
      b0[j] = __builtin_elementwise_fma(f32x2{bf_lo(uu0[j]), bf_hi(uu0[j])}, f32x2{dsk, dsk}, f32x2{bf_lo(xx0[j]), bf_hi(xx0[j])}) + f32x2{y[0][2 * j], y[0][2 * j + 1]};
      b1[j] = __builtin_elementwise_fma(f32x2{bf_lo(uu1[j]), bf_hi(uu1[j])}, f32x2{dsk, dsk}, f32x2{bf_lo(xx1[j]), bf_hi(xx1[j])}) + f32x2{y[1][2 * j], y[1][2 * j + 1]};
    }
    asm volatile("" : "+v"(b0[0]), "+v"(b0[1]), "+v"(b1[0]), "+v"(b1[1]));
    group_barrier(cnt, 16);
    const f32x4 o0 = red[(wq ^ 2) * 128], o1 = red[(wq ^ 2) * 128 + 64];
    const u32 hb = WS_HT + (2 * cp + ch) * 256, ho = (ep_lo & 255) + (ep_lo >> 8) * (D * 256);
    __builtin_amdgcn_raw_buffer_store_b64(u32x2{pack_bf(b0[0].x + o0[0], b0[0].y + o0[1]), pack_bf(b0[1].x + o0[2], b0[1].y + o0[3])}, rs, ho, hb + 2 * D * 256 * t0, 0);
    __builtin_amdgcn_raw_buffer_store_b64(u32x2{pack_bf(b1[0].x + o1[0], b1[0].y + o1[1]), pack_bf(b1[1].x + o1[2], b1[1].y + o1[3])}, rs, ho, hb + 2 * D * 256 * t1, 0);
  }
  STAMP(0, 9);
}

constexpr int F1_STAGE = 16384;
constexpr int F1_P2 = 4 * F1_STAGE;
constexpr int F1_CS = F1_P2 + 84 * 1024;
constexpr int F1_ARR = F1_CS + 7 * 256;
constexpr int F2_PS = F1_ARR + 32;
constexpr int F2_RES = F2_PS + 2048;
constexpr int F1_TOTAL = F2_RES + 4 * 2048;
static_assert(F1_TOTAL <= 163840, "lds map");
#define WD_FIRST 0
#define PUT_SP 2
#define CNT_SLEEP 2
#define F2_WIN 8

DI void f1_publish(char* ws, u32 arr_addr, int cnt) {
  u32 old, one = 1;
  asm volatile("s_mov_b64 exec, 1\n\tds_add_rtn_u32 %0, %1, %2\n\ts_mov_b64 exec, -1\n\ts_waitcnt lgkmcnt(0)" : "=&v"(old) : "v"(arr_addr), "v"(one) : "memory");
  if (__builtin_amdgcn_readfirstlane(old) == 7u && (threadIdx.x & 63) == 0)
    __hip_atomic_fetch_add((GAS u32*)(ws + WS_CNT + cnt * 256), 1u, __ATOMIC_RELAXED, __HIP_MEMORY_SCOPE_AGENT);
}
DI constexpr int f1_slot(int c) { return c <= 8 ? c : c < 13 ? c - 9 : 7; }
#define BARRIER() do { __builtin_amdgcn_s_barrier(); asm volatile("" ::: "memory"); } while (0)

__global__ __launch_bounds__(512, 2) void k_ffn(char* __restrict__ ws, u16* __restrict__ out, const char* __restrict__ Wd) {
  __shared__ __attribute__((aligned(16))) char lds[F1_TOTAL];
  STAMP_DECL;
  STAMP(1, 0);
  const int tid = threadIdx.x, lane = tid & 63;
  const int wave = __builtin_amdgcn_readfirstlane(tid >> 6);
  const int xcd = blockIdx.x & 7, bi = blockIdx.x >> 3;
  const int panel = (xcd & 1) * 8 + (bi & 7), slice = (xcd >> 1) * 4 + (bi >> 3);
  const u32 vo = lane * 16;

  if (wave >= 4) {
    const int lw = wave - 4;
    const char* hb = ws + WS_HT + panel * (D * 256) + lw * 2048;
    const char* wb = ws + WS_WT + slice * (196 * 1024);
    const u32 ho = (lane >> 4) * 256 + (((lane & 15) ^ ((lane >> 4) << 2)) * 16);
    const u32 lbase = lds_addr(lds) + lw * 2048;
    auto stage = [&](int c) {
      asm volatile("s_mov_b32 m0, %0\n\ts_nop 0\n\t"
                   "global_load_lds_dwordx4 %2, %4\n\tglobal_load_lds_dwordx4 %2, %4 offset:1024\n\t"
                   "s_mov_b32 m0, %1\n\ts_nop 0\n\t"
                   "global_load_lds_dwordx4 %3, %5\n\tglobal_load_lds_dwordx4 %3, %5 offset:1024"
                   :: "s"(lbase + f1_slot(c) * F1_STAGE), "s"(lbase + f1_slot(c) * F1_STAGE + 8192),
                      "v"(ho), "v"(vo), "s"(hb + c * 8192), "s"(wb + lw * 2048 + c * 8192)
                   : "memory");
    };
    stage(0);
    SCHED0();
    auto p2 = [&](int i) {
      asm volatile("s_mov_b32 m0, %0\n\ts_nop 0\n\tglobal_load_lds_dwordx4 %1, %2"
                   :: "s"(lds_addr(lds) + F1_P2 + (i * 4 + lw) * 1024), "v"(vo), "s"(wb + (112 + i * 4 + lw) * 1024) : "memory");
    };
    f32x2 psum = {0.f, 0.f};
    float v[14];
    {
      const u32 co = min(tid - 256, 223) * 4;
      const char* cp = ws + WS_CS + slice * (14 * 896);
      asm volatile("global_load_dword %0, %14, %15\n\tglobal_load_dword %1, %14, %15 offset:896\n\tglobal_load_dword %2, %14, %15 offset:1792\n\t"
                   "global_load_dword %3, %14, %15 offset:2688\n\tglobal_load_dword %4, %14, %15 offset:3584\n\t"
                   "global_load_dword %5, %14, %16\n\tglobal_load_dword %6, %14, %16 offset:896\n\tglobal_load_dword %7, %14, %16 offset:1792\n\t"
                   "global_load_dword %8, %14, %16 offset:2688\n\tglobal_load_dword %9, %14, %16 offset:3584\n\t"
                   "global_load_dword %10, %14, %17\n\tglobal_load_dword %11, %14, %17 offset:896\n\tglobal_load_dword %12, %14, %17 offset:1792\n\t"
                   "global_load_dword %13, %14, %17 offset:2688"
                   : "=&v"(v[0]), "=&v"(v[1]), "=&v"(v[2]), "=&v"(v[3]), "=&v"(v[4]), "=&v"(v[5]), "=&v"(v[6]),
                     "=&v"(v[7]), "=&v"(v[8]), "=&v"(v[9]), "=&v"(v[10]), "=&v"(v[11]), "=&v"(v[12]), "=&v"(v[13])
                   : "v"(co), "s"(cp), "s"(cp + 4480), "s"(cp + 8960) : "memory");
    }
    stage(1); stage(2);
    wait_vm<2 * 4 + 14>();
    BARRIER();
    auto csum_out = [&]() {
      asm volatile("" : "+v"(v[0]), "+v"(v[1]), "+v"(v[2]), "+v"(v[3]), "+v"(v[4]), "+v"(v[5]), "+v"(v[6]),
                   "+v"(v[7]), "+v"(v[8]), "+v"(v[9]), "+v"(v[10]), "+v"(v[11]), "+v"(v[12]), "+v"(v[13]));
      const float csum = (((v[0] + v[1]) + (v[2] + v[3])) + ((v[4] + v[5]) + (v[6] + v[7]))) + (((v[8] + v[9]) + (v[10] + v[11])) + (v[12] + v[13]));
      const int i = tid - 256;
      u32* ca = (u32*)(lds + F1_CS) + (i >> 5) * 64 + (i & 31);
      if (i < 224) { ca[0] = pack_bf(csum, csum - rbf(csum)); ca[32] = 0u; }
    };
    auto part = [&](int c0, int c1) {
      if (slice >= c0 && slice <= c1) {
        const int q = (lane & 15) >> 2, sl = c0 == 9 ? (slice == 13 ? 7 : slice - 9) : slice;
        const u32 a = lds_addr(lds) + sl * F1_STAGE + (8 * (lane >> 5) + q) * 256 + ((lw ^ q) * 64) + ((lane >> 4) & 1) * 32 + 8 * (lane & 3);
        const u32x4 n0 = __builtin_bit_cast(u32x4, tr2(a, 0, 1024)), n1 = __builtin_bit_cast(u32x4, tr2(a, 4096, 5120));
        float s0 = 0.f, s1 = 0.f, q0 = 0.f, q1 = 0.f;
#pragma unroll
        for (int i = 0; i < 8; ++i) {
          const u32 w = i < 4 ? n0[i & 3] : n1[i & 3];
          const float lo = bf_lo(w), hi = bf_hi(w);
          s0 += lo; q0 = __builtin_fmaf(lo, lo, q0);
          s1 += hi; q1 = __builtin_fmaf(hi, hi, q1);
          asm("" : "+v"(s0)); asm("" : "+v"(q0)); asm("" : "+v"(s1)); asm("" : "+v"(q1));
        }
        psum = f32x2{s0 + s1, q0 + q1};
        asm volatile("" : "+v"(psum));
      }
    };
#define CLOSE(n) do { asm volatile("s_waitcnt vmcnt(" #n ") lgkmcnt(0)" ::: "memory"); BARRIER(); } while (0)
    stage(3); stage(4); CLOSE(12);
    stage(5); csum_out(); CLOSE(8);
    part(0, 3); stage(6); stage(7); CLOSE(8);
    stage(8); stage(9); CLOSE(8);
    stage(10); stage(11); CLOSE(8);
    part(4, 8); stage(12); stage(13); CLOSE(8);
    p2(0); p2(1); p2(2); p2(3); p2(4); p2(5); p2(6); p2(7); CLOSE(8);
    part(9, 13); p2(8); p2(9); p2(10); p2(11); p2(16); p2(17); CLOSE(6);
#undef CLOSE
    const __amdgpu_buffer_rsrc_t rs = __builtin_amdgcn_make_buffer_rsrc((void*)ws, 0, (int)WS_END, 0x00020000);
    if (slice < 14) {
      const float a = half_sum(psum.x), b = half_sum(psum.y);
      if (lane < 32) __builtin_amdgcn_raw_buffer_store_b64(__builtin_bit_cast(u32x2, f32x2{a, b}), rs, lane * 8, WS_PS + ((panel * 14 + slice) * 128 + lw * 32) * 8, 16);
    }
    p2(12); p2(13); p2(14); p2(15); p2(18); p2(19); p2(20);
    const bool down = bi < 28;
    const int tgp = (xcd & 1) * 16 + (xcd >> 1) * 4 + (bi & 3), dp = down ? bi >> 2 : 0;
    const u32 a1 = lds_addr(lds) + lw * 16384, a2 = lds_addr(lds) + F1_P2 + lw * 21504;
    const u32 wsrc = (lane >> 3) * (D * 2) + (((lane & 7) ^ ((lane >> 2) & 4)) * 16);
    const char* wbase = Wd + lw * 448 * (D * 2) + dp * 128;
    auto wd_load = [&](u32 dst, int k0, int n2) {
#pragma unroll
      for (int i = 0; i < n2; ++i) {
        const char* b0 = wbase + (k0 + 2 * i) * 16 * (D * 2);
        asm volatile("s_mov_b32 m0, %0\n\ts_nop 0\n\t"
                     "global_load_lds_dwordx4 %1, %2\n\tglobal_load_lds_dwordx4 %1, %3 offset:1024\n\t"
                     "global_load_lds_dwordx4 %1, %4 offset:2048\n\tglobal_load_lds_dwordx4 %1, %5 offset:3072"
                     :: "s"(dst + i * 4096), "v"(wsrc), "s"(b0), "s"(b0 + 8 * (D * 2) - 1024), "s"(b0 + 16 * (D * 2) - 2048), "s"(b0 + 24 * (D * 2) - 3072)
                     : "memory");
      }
    };
    wd_load(a1, 0, WD_FIRST);
    wait_vm<4 * WD_FIRST>();
    BARRIER();
    f1_publish(ws, lds_addr(lds) + F1_ARR, panel * 4 + (slice >> 2));
    wd_load(a1 + WD_FIRST * 4096, 2 * WD_FIRST, 4 - WD_FIRST);
    wait_vm<0>();
    if (!down) { STAMP(1, 9); return; }
    const u32 q4 = (lane & 15) >> 2;
    const u32 tq = (4 * (lane >> 5) + q4) * 128 + (q4 >> 1) * 64 + ((lane >> 4) & 1) * 32 + (lane & 3) * 8;
    const u32 t10 = a1 + tq, t11 = a1 + (tq ^ 64), t20 = a2 + tq, t21 = a2 + (tq ^ 64);
    s16x8 ra[8][2];
#pragma unroll
    for (int k = 0; k < 8; ++k) { ra[k][0] = tr2(t10, k * 2048, k * 2048 + 1024); ra[k][1] = tr2(t11, k * 2048, k * 2048 + 1024); }
#pragma unroll
    for (int k = 0; k < 8; ++k) asm volatile("" : "+v"(ra[k][0]), "+v"(ra[k][1]));
    wd_load(a1, 8, 4);
    const int tok0 = tgp * 64 + (lw & 1) * 32, d0 = dp * 64 + (lw >> 1) * 32;
    const u32 aux = lds_addr(lds) + F2_RES + lw * 2048;
    {
      const char* hr = ws + WS_HT + (tok0 >> 7) * (D * 256) + d0 * 256 + (tok0 & 127) * 2;
      asm volatile("s_mov_b32 m0, %0\n\ts_nop 0\n\tglobal_load_lds_dwordx4 %1, %2\n\tglobal_load_lds_dwordx4 %1, %3 offset:1024"
                   :: "s"(aux), "v"((lane >> 2) * 256 + (lane & 3) * 16), "s"(hr), "s"(hr + 4096 - 1024) : "memory");
    }
    {
      u32 c, sv;
      asm volatile("1:\n\tds_read_b32 %0, %2\n\ts_waitcnt lgkmcnt(0)\n\tv_readfirstlane_b32 %1, %0\n\ts_cmp_lt_u32 %1, 4\n\ts_cbranch_scc0 2f\n\ts_sleep 1\n\ts_branch 1b\n2:"
                   : "=&v"(c), "=&s"(sv) : "v"(lds_addr(lds) + F1_ARR + 4) : "memory", "scc");
    }
    STAMP(1, 4);
    wd_load(a2, 16, 5);
    for (u32 spins = 0; spins < (1u << 16); ++spins) {
      const u32 c = __builtin_amdgcn_raw_buffer_load_b32(rs, 0, WS_CNT + ((tgp >> 1) * 4 + lw) * 256, 16);
      if (__builtin_amdgcn_readfirstlane(c) >= 4u) break;
      __builtin_amdgcn_s_sleep(CNT_SLEEP);
      asm volatile("" ::: "memory");
    }
    asm volatile("" ::: "memory");
    STAMP(1, 5);
    const u32 sb = WS_HF + (tgp * 224 + lw * 28) * 1024;
    s16x8 rb[F2_WIN][2];
    auto ldb = [&](int k) {
      rb[k % F2_WIN][0] = __builtin_bit_cast(s16x8, __builtin_amdgcn_raw_buffer_load_b128(rs, vo + (k & 3) * 1024, sb + (k >> 2) * 4096, 16));
      rb[k % F2_WIN][1] = __builtin_bit_cast(s16x8, __builtin_amdgcn_raw_buffer_load_b128(rs, vo + (k & 3) * 1024, sb + (k >> 2) * 4096 + 112 * 1024, 16));
    };
    ldb(0); ldb(1);
    SCHED0();
    f32x2 ps[4];
#pragma unroll
    for (int i = 0; i < 4; ++i)
      ps[i] = __builtin_bit_cast(f32x2, __builtin_amdgcn_raw_buffer_load_b64(rs, lane * 8 + (i < 2 || lw < 3 ? i : i - 2) * 1024,
                                                                             WS_PS + (tgp >> 1) * (14 * 1024) + (tgp & 1) * 512 + lw * 4096, 16));
    SCHED0();
#pragma unroll
    for (int k = 2; k < F2_WIN; ++k) { ldb(k); SCHED0(); }
    f32x16 acc[4];
#pragma unroll
    for (int a = 0; a < 4; ++a)
#pragma unroll
      for (int i = 0; i < 16; ++i) acc[a][i] = 0.f;
    u32x2 hres[4];
    s16x8 la[2][2];
    static_assert(10 + F2_WIN < 25, "the refill must be older than fragments consumed before it is read");
#pragma unroll
    for (int k = 0; k < 28; ++k) {
      const int n = k + 1;
      if (n >= 8 && n < 28) {
        const int o = (n < 16 ? n - 8 : n < 26 ? n - 16 : n - 26) * 2048;
        la[n & 1][0] = tr2(n >= 16 && n < 26 ? t20 : t10, o, o + 1024);
        la[n & 1][1] = tr2(n >= 16 && n < 26 ? t21 : t11, o, o + 1024);
      }
#pragma unroll
      for (int a = 0; a < 4; ++a) acc[a] = MFMA32(k < 8 ? ra[k < 8 ? k : 0][a >> 1] : la[k & 1][a >> 1], rb[k % F2_WIN][a & 1], acc[a]);
      SCHED0();
      if (k == 9) wd_load(a1, 26, 1);
      if (k + F2_WIN < 28) ldb(k + F2_WIN);
      if (k == 2) {
        const f32x2 t = lw < 3 ? (ps[0] + ps[1]) + (ps[2] + ps[3]) : ps[0] + ps[1];
        *(LDS3 f32x2*)(uintptr_t)(lds_addr(lds) + F2_PS + lw * 512 + lane * 8) = t;
      }
      if (k == 20) {
        const u32 a = aux + (4 * (lane >> 5) + ((lane & 15) >> 2)) * 64 + ((lane >> 4) & 1) * 32 + (lane & 3) * 8;
#pragma unroll
        for (int g = 0; g < 4; ++g) hres[g] = __builtin_bit_cast(u32x2, __builtin_amdgcn_ds_read_tr16_b64_v4i16((LDS3 s16x4*)(uintptr_t)(a + g * 512)));
      }
      SCHED0();
    }
    STAMP(1, 6);
    f32x16 mine;
#pragma unroll
    for (int a = 0; a < 4; ++a) {
      if (a == lw) { mine = acc[a]; }
      else {
        LDS3 f32x4* pp = (LDS3 f32x4*)(uintptr_t)(a1 + a * 4096 + vo);
#pragma unroll
        for (int g = 0; g < 4; ++g) pp[g * 64] = f32x4{acc[a][4 * g], acc[a][4 * g + 1], acc[a][4 * g + 2], acc[a][4 * g + 3]};
      }
    }
    __syncthreads();
    float rs2;
    {
      const LDS3 f32x2* pq = (const LDS3 f32x2*)(uintptr_t)(lds_addr(lds) + F2_PS + ((lw & 1) * 32 + (lane & 31)) * 8);
      const f32x2 t = (pq[0] + pq[64]) + (pq[128] + pq[192]);
      const float mean = t.x * (1.0f / D);
      const float var = fmaxf(t.y * (1.0f / D) - mean * mean, 0.f);
      const float r = __builtin_amdgcn_rcpf(rbf(__builtin_amdgcn_sqrtf(rbf(rbf(var) + 1e-5f))));
      rs2 = r * r;
    }
    u32 pk[8];
    const u32 rdb = lds_addr(lds) + lw * 4096 + vo;
#pragma unroll
    for (int g = 0; g < 4; ++g) {
      const f32x4 p0 = *(const LDS3 f32x4*)(uintptr_t)(rdb + ((lw + 1) & 3) * 16384 + g * 1024);
      const f32x4 p1 = *(const LDS3 f32x4*)(uintptr_t)(rdb + ((lw + 2) & 3) * 16384 + g * 1024);
      const f32x4 p2v = *(const LDS3 f32x4*)(uintptr_t)(rdb + ((lw + 3) & 3) * 16384 + g * 1024);
      const f32x4 sv = (p0 + p1) + (p2v + f32x4{mine[4 * g], mine[4 * g + 1], mine[4 * g + 2], mine[4 * g + 3]});
      const f32x2 o0 = __builtin_elementwise_fma(f32x2{sv[0], sv[1]}, f32x2{rs2, rs2}, f32x2{bf_lo(hres[g][0]), bf_hi(hres[g][0])});
      const f32x2 o1 = __builtin_elementwise_fma(f32x2{sv[2], sv[3]}, f32x2{rs2, rs2}, f32x2{bf_lo(hres[g][1]), bf_hi(hres[g][1])});
      pk[2 * g] = pack_bf(o0.x, o0.y);
      pk[2 * g + 1] = pack_bf(o1.x, o1.y);
    }
    char* ob = (char*)out + tok0 * (D * 2) + d0 * 2;
#pragma unroll
    for (int k = 0; k < 2; ++k) {
      const auto r0 = __builtin_amdgcn_permlane32_swap(pk[4 * k], pk[4 * k + 2], false, false);
      const auto r1 = __builtin_amdgcn_permlane32_swap(pk[4 * k + 1], pk[4 * k + 3], false, false);
      *(u32x4*)(ob + (u32)((lane & 31) * (D * 2) + (lane >> 5) * 16 + 32 * k)) = u32x4{r0[0], r1[0], r0[1], r1[1]};
    }
    if (blockIdx.x == 0 && tid == 256) { u32* e = (u32*)(ws + WS_EPOCH); *e = next_tag(*e) ^ 0x5bd1e995u; }
    STAMP(1, 9);
    asm volatile(".Lffn2_code_end:");
    return;
  }

  const int q = (lane & 15) >> 2;
  u32 bB[3], bA[3];
#pragma unroll
  for (int i = 0; i < 3; ++i) {
    bB[i] = lds_addr(lds) + i * 65536 + (8 * (lane >> 5) + q) * 256 + ((wave ^ q) * 64) + ((lane >> 4) & 1) * 32 + 8 * (lane & 3);
    bA[i] = lds_addr(lds) + i * 65536 + 8192 + vo;
    asm volatile("" : "+v"(bB[i]), "+v"(bA[i]));
  }
  char* dst = ws + WS_HF + ((panel * 4 + wave) * 112 + slice * 7) * 1024;
  s16x8 bk[28], fa[2][4];
  f32x16 acc[7];
  float ss0 = 0.f, ss1 = 0.f;
  auto rd1 = [&](int k) {
    const int sl = f1_slot(k >> 1), o = (sl & 3) * F1_STAGE + (k & 1) * 4096;
    bk[k] = tr2(bB[sl >> 2], o, o + 1024);
#pragma unroll
    for (int t = 0; t < 4; ++t) fa[k & 1][t] = *(const LDS3 s16x8*)(uintptr_t)(bA[sl >> 2] + o + t * 1024);
  };
  auto rd2 = [&](int k) {
#pragma unroll
    for (int t = 0; t < 3; ++t) fa[k & 1][t] = *(const LDS3 s16x8*)(uintptr_t)(k * 3 + t < 8 ? bA[0] + (56 + k * 3 + t) * 1024 - 8192 + 8192 - 0 : k * 3 + t < 72 ? bA[1] + (k * 3 + t - 8) * 1024 : bA[2] + (k * 3 + t - 72) * 1024);
  };
  auto msum = [&](int k) {
    const u32x4 bw = __builtin_bit_cast(u32x4, bk[k]);
#pragma unroll
    for (int i = 0; i < 4; ++i) {
      ss0 += bf_lo(bw[i]); ss1 += bf_hi(bw[i]);
      asm("" : "+v"(ss0)); asm("" : "+v"(ss1));
    }
  };
  auto put = [&](int t) {
    u32x4 o;
#pragma unroll
    for (int j = 0; j < 4; ++j) {
      float a0 = relu(acc[t][2 * j]) * acc[t][8 + 2 * j], a1 = relu(acc[t][2 * j + 1]) * acc[t][9 + 2 * j];
      asm("" : "+v"(a0)); asm("" : "+v"(a1));
      o[j] = pack_bf(a0, a1);
    }
    st128_dev(dst + (t >> 2) * 4096, vo + (t & 3) * 1024, o);
  };
  const f32x16 zero = {0.f, 0.f, 0.f, 0.f, 0.f, 0.f, 0.f, 0.f, 0.f, 0.f, 0.f, 0.f, 0.f, 0.f, 0.f, 0.f};
  if (tid == 0) *(u32x2*)(lds + F1_ARR) = u32x2{0u, 0u};
  asm volatile("s_waitcnt lgkmcnt(0)" ::: "memory");
  BARRIER();
  STAMP(1, 1);
  rd1(0);
  SCHED0();
#pragma unroll
  for (int k = 0; k < 28; ++k) {
    if (k == 1 || (k & 3) == 3) {
      asm volatile("s_waitcnt lgkmcnt(0)" ::: "memory");
      BARRIER();
      SCHED0();
    }
    if (k < 27) rd1(k + 1); else rd2(0);
#pragma unroll
    for (int t = 0; t < 4; ++t) acc[t] = MFMA32(fa[k & 1][t], bk[k], k == 0 ? zero : acc[t]);
    msum(k);
    __builtin_amdgcn_sched_group_barrier(0x8, 1, 0);
    __builtin_amdgcn_sched_group_barrier(0x100, 3, 0);
    __builtin_amdgcn_sched_group_barrier(0x2, 2, 0);
    __builtin_amdgcn_sched_group_barrier(0x8, 1, 0);
    __builtin_amdgcn_sched_group_barrier(0x100, 3, 0);
    __builtin_amdgcn_sched_group_barrier(0x2, 2, 0);
    __builtin_amdgcn_sched_group_barrier(0x8, 1, 0);
    __builtin_amdgcn_sched_group_barrier(0x2, 6, 0);
    __builtin_amdgcn_sched_group_barrier(0x8, 1, 0);
    __builtin_amdgcn_sched_group_barrier(0x2, 6, 0);
    SCHED0();
  }
  STAMP(1, 2);
  {
    const u32* ca = (const u32*)(lds + F1_CS) + lane;
    u32 cw[7];
#pragma unroll
    for (int t = 0; t < 7; ++t) cw[t] = ca[t * 64];
    const float nmean = half_sum(ss0 + ss1) * (-1.0f / D);
    const s16x8 mo = __builtin_bit_cast(s16x8, u32x4{pack_bf(nmean, nmean), 0u, 0u, 0u});
#pragma unroll
    for (int t = 0; t < 4; ++t) acc[t] = MFMA32(__builtin_bit_cast(s16x8, u32x4{cw[t], 0u, 0u, 0u}), mo, acc[t]);
    put(0);
    SCHED0();
#pragma unroll
    for (int t = 4; t < 7; ++t) acc[t] = MFMA32(__builtin_bit_cast(s16x8, u32x4{cw[t], 0u, 0u, 0u}), mo, zero);
    SCHED0();
  }
#pragma unroll
  for (int k = 0; k < 26; ++k) {
    if (k == 9) BARRIER();
    rd2(k + 1);
#pragma unroll
    for (int t = 0; t < 3; ++t) acc[4 + t] = MFMA32(fa[k & 1][t], bk[k], acc[4 + t]);
    if (k % PUT_SP == 0 && k < 3 * PUT_SP) put(1 + k / PUT_SP);
#pragma unroll
    for (int i = 0; i < 3; ++i) {
      __builtin_amdgcn_sched_group_barrier(0x8, 1, 0);
      __builtin_amdgcn_sched_group_barrier(0x100, 1, 0);
      __builtin_amdgcn_sched_group_barrier(0x2, 7, 0);
    }
    SCHED0();
  }
  STAMP(1, 3);
  rd2(27);
  {
    u32 one = 1;
    asm volatile("s_mov_b64 exec, 1\n\tds_add_u32 %0, %1\n\ts_mov_b64 exec, -1" :: "v"(lds_addr(lds) + F1_ARR + 4), "v"(one) : "memory");
  }
  SCHED0();
  acc[4] = MFMA32(fa[0][0], bk[26], acc[4]); acc[4] = MFMA32(fa[1][0], bk[27], acc[4]);
  acc[5] = MFMA32(fa[0][1], bk[26], acc[5]); acc[5] = MFMA32(fa[1][1], bk[27], acc[5]);
  SCHED0();
  acc[6] = MFMA32(fa[0][2], bk[26], acc[6]); acc[6] = MFMA32(fa[1][2], bk[27], acc[6]);
  put(4);
  SCHED0();
  put(5); put(6);
  STAMP(1, 4);
  wait_vm<0>();
  f1_publish(ws, lds_addr(lds) + F1_ARR, panel * 4 + (slice >> 2));
  STAMP(1, 9);
}

extern "C" void kernel_launch(void* const* d_in, const int* in_sizes, int n_in,
                              void* d_out, int out_size, void* d_ws, size_t ws_size,
                              hipStream_t stream) {
  const u16* x = (const u16*)d_in[0];
  char* ws = (char*)d_ws;
  k_ssm<<<256, 512, 0, stream>>>(x, (const u16*)d_in[1], (const u16*)d_in[2], (const u16*)d_in[3], (const u16*)d_in[4],
                                 (const u16*)d_in[5], (const u16*)d_in[6], (const u16*)d_in[7], (const u16*)d_in[8],
                                 (const u16*)d_in[10], (const u16*)d_in[11], (const u16*)d_in[12], ws);
  k_ffn<<<256, 512, 0, stream>>>(ws, (u16*)d_out, (const char*)d_in[12]);
}
```

```cpp
#include <hip/hip_runtime.h>
#include <hip/hip_bf16.h>
#include <stdint.h>

typedef unsigned short u16;
typedef unsigned int u32;
typedef unsigned long long u64;
typedef short s16x4 __attribute__((ext_vector_type(4)));
typedef short s16x8 __attribute__((ext_vector_type(8)));
typedef _Float16 h16x8 __attribute__((ext_vector_type(8)));
typedef _Float16 h16x2 __attribute__((ext_vector_type(2)));
typedef __bf16 bf16x2 __attribute__((ext_vector_type(2)));
typedef float f32x2 __attribute__((ext_vector_type(2)));
typedef float f32x4 __attribute__((ext_vector_type(4)));
typedef float f32x16 __attribute__((ext_vector_type(16)));
typedef u32 u32x2 __attribute__((ext_vector_type(2)));
typedef u32 u32x4 __attribute__((ext_vector_type(4)));
#define LDS3 __attribute__((address_space(3)))
#define GAS __attribute__((address_space(1)))
#define DI __device__ __forceinline__
#define SCHED0() __builtin_amdgcn_sched_barrier(0)

constexpr int L = 2048, D = 448, NM = 256, F = 1792;

constexpr u32 WS_SG = 0;
constexpr u32 WS_EPOCH = 16384;
constexpr u32 WS_CNT = 32768;
constexpr u32 WS_HT = 65536;
constexpr u32 WS_HF = WS_HT + D * L * 2;
constexpr u32 WS_XG = WS_HF + L * F * 2;
constexpr u32 WS_WT = WS_XG + (D / 2) * L * 8;
constexpr u32 WS_CS = WS_WT + 2 * D * F * 2;
constexpr u32 WS_PS = WS_CS + 16 * 14 * 896;
constexpr u32 WS_END = WS_PS + 16 * 14 * 1024;

DI float bf_lo(u32 w) { return __builtin_bit_cast(float, w << 16); }
DI float bf_hi(u32 w) { return __builtin_bit_cast(float, w & 0xffff0000u); }
DI float rbf(float a) { return (float)(__bf16)a; }
DI u32 pack_bf(float a, float b) { bf16x2 v; v.x = (__bf16)a; v.y = (__bf16)b; return __builtin_bit_cast(u32, v); }
DI u32 pack_h(f32x2 a) { h16x2 v; v.x = (_Float16)a.x; v.y = (_Float16)a.y; return __builtin_bit_cast(u32, v); }
DI u32 lds_addr(const void* p) { return (u32)(uintptr_t)(const LDS3 void*)p; }
DI float relu(float v) { return __builtin_bit_cast(float, max(__builtin_bit_cast(int, v), 0)); }
DI u32 next_tag(u32 e) { return e * 2654435761u + 0x9E3779B9u; }

DI int wave_imax(int c) {
#define RED1(ctl) "s_nop 1\n\tv_max_i32_dpp %0, %0, %0 " ctl "\n\t"
  asm(RED1("row_shr:1 row_mask:0xf bank_mask:0xf bound_ctrl:1") RED1("row_shr:2 row_mask:0xf bank_mask:0xf bound_ctrl:1")
      RED1("row_shr:4 row_mask:0xf bank_mask:0xf bound_ctrl:1") RED1("row_shr:8 row_mask:0xf bank_mask:0xf bound_ctrl:1")
      RED1("row_bcast:15 row_mask:0xa bank_mask:0xf") RED1("row_bcast:31 row_mask:0xc bank_mask:0xf")
      "s_nop 0"
      : "+v"(c));
#undef RED1
  return __builtin_amdgcn_readlane(c, 63);
}
DI void oct_sum2(float& a, float& b) {
#define RED2(ctl) "s_nop 0\n\tv_add_f32_dpp %0, %0, %0 " ctl " row_mask:0xf bank_mask:0xf\n\tv_add_f32_dpp %1, %1, %1 " ctl " row_mask:0xf bank_mask:0xf\n\t"
  asm("s_nop 0\n\t" RED2("quad_perm:[1,0,3,2]") RED2("quad_perm:[2,3,0,1]") RED2("row_half_mirror") "s_nop 0" : "+v"(a), "+v"(b));
#undef RED2
}
DI void st128_dev(const char* base, u32 off, u32x4 v) {
  asm volatile("global_store_dwordx4 %0, %1, %2 sc1\n\ts_nop 1" :: "v"(off), "v"(v), "s"(base) : "memory");
}
DI f32x2 cmul(f32x2 a, f32x2 b) {
  f32x2 t, o;
  asm("v_pk_mul_f32 %0, %1, %2 op_sel_hi:[0,1]" : "=v"(t) : "v"(a), "v"(b));
  asm("v_pk_fma_f32 %0, %1, %2, %3 op_sel:[1,1,0] op_sel_hi:[1,0,1] neg_lo:[0,1,0]" : "=v"(o) : "v"(a), "v"(b), "v"(t));
  return o;
}
DI u32 finish_stats(float mean, float var) { return pack_bf(mean, __builtin_amdgcn_sqrtf(rbf(rbf(fmaxf(var, 0.f)) + 1e-5f))); }
DI float dot_bf(u32 a, u32 b, float c) {
  return __builtin_amdgcn_fdot2_f32_bf16(__builtin_bit_cast(bf16x2, a), __builtin_bit_cast(bf16x2, b), c, false);
}

constexpr int KG_ROW = 1040;
constexpr int KG_CH = 53 * KG_ROW;
constexpr int KC_STRIDE = 4160;
constexpr int KC_CH = KG_CH;
constexpr int SL_RED = 4 * KC_STRIDE;
constexpr int SL_U = 2 * KG_CH;
constexpr int U_CH = (256 + L) * 2;
constexpr int SL_X = SL_U + 2 * U_CH;
constexpr int XR_ROW = 144;
constexpr int SL_XX = SL_X + 64 * XR_ROW;
constexpr int SL_T = SL_XX + 2 * L * 2;
constexpr int SL_FLAG = SL_T + 769 * 4;
constexpr int SL_CNT = SL_T + 770 * 4;
constexpr int SL_W = SL_T + 772 * 4;
constexpr int SL_DUMP = SL_W + 32 * 448 + 64;
constexpr int SL_TOTAL = SL_DUMP + 256;
static_assert(SL_RED + 4 * 2048 <= KG_CH && KG_CH % 16 == 0 && SL_W % 16 == 0 && SL_TOTAL <= 163840 && KG_CH + 768 < 65536, "lds map");

struct GatherTab { u32 a[256 * 12]; };
constexpr GatherTab make_gather_tab() {
  GatherTab t = {};
  for (int tc = 0; tc < 256; ++tc)
    for (int j = 0; j < 12; ++j) {
      const int i = 2047 - 8 * tc - j;
      int slot = 768;
      if (i >= 0) {
        const int s = i < 256 ? 0 : i < 512 ? 1 : i < 1024 ? 2 : 3;
        const int lag = s ? ((i + (1 << (s - 1)) - 1 + ((i >> s) & 1)) >> s) << s : i;
        const int lo = (lag & 255) >> 3, b = lag >> 8, r = lag & 7;
        const int row = b == 0 ? r : b == 1 ? 8 + (r >> 1) : b < 4 ? 12 + 2 * (b - 2) + (r >> 2) : 12 + b;
        slot = row * 32 + lo;
      }
      t.a[(j >> 2) * 1024 + tc * 4 + (j & 3)] = SL_T + 4 * slot;
    }
  return t;
}
static __device__ const GatherTab gather_tab = make_gather_tab();

DI s16x8 tr2(u32 a, int o0, int o1) {
  const s16x4 lo = __builtin_amdgcn_ds_read_tr16_b64_v4i16((LDS3 s16x4*)(uintptr_t)(a + o0));
  const s16x4 hi = __builtin_amdgcn_ds_read_tr16_b64_v4i16((LDS3 s16x4*)(uintptr_t)(a + o1));
  return __builtin_shufflevector(lo, hi, 0, 1, 2, 3, 4, 5, 6, 7);
}
template <int N> DI void wait_vm() { asm volatile("s_waitcnt vmcnt(%c0)" :: "i"(N) : "memory"); }
DI float dot_ones(s16x8 f, float s) {
  const u32x4 w4 = __builtin_bit_cast(u32x4, f);
#pragma unroll
  for (int i = 0; i < 4; ++i) s = dot_bf(w4[i], 0x3f803f80u, s);
  return s;
}
DI float half_sum(float v) {
  const u32 w = __builtin_bit_cast(u32, v);
  const auto r = __builtin_amdgcn_permlane32_swap(w, w, false, false);
  const u32 r0 = r[0], r1 = r[1];
  return __builtin_bit_cast(float, r0) + __builtin_bit_cast(float, r1);
}
DI void set_m0(u32 v) { asm volatile("s_mov_b32 m0, %0\n\ts_nop 0" :: "s"(v) : "memory"); }
template <int R0, int R1, int R2, int R3, int R4, int R5>
DI void put6(u32 v0, u32 v1, u32 v2, u32 v3, u32 v4, u32 v5) {
  asm volatile("ds_write_addtid_b32 %0 offset:%c6\n\tds_write_addtid_b32 %1 offset:%c7\n\tds_write_addtid_b32 %2 offset:%c8\n\t"
               "ds_write_addtid_b32 %3 offset:%c9\n\tds_write_addtid_b32 %4 offset:%c10\n\tds_write_addtid_b32 %5 offset:%c11"
               :: "v"(v0), "v"(v1), "v"(v2), "v"(v3), "v"(v4), "v"(v5),
                  "i"(R0 * KG_ROW), "i"(R1 * KG_ROW), "i"(R2 * KG_ROW), "i"(R3 * KG_ROW), "i"(R4 * KG_ROW), "i"(R5 * KG_ROW)
               : "memory");
}
template <int K, int RB>
DI void kg_step(f32x2& fa, f32x2& fb, f32x2& e0, f32x2& e1, f32x2& e2, f32x2& e3, f32x2 z, f32x2 w, f32x2 c) {
  f32x2 t0, t1, t2, t3, t4, t5;
  u32 h0, h1, h2, h3, h4, h5;
#define KG_M(t, a, b) "v_pk_mul_f32 " t ", " a ", " b " op_sel_hi:[0,1]\n\t"
#define KG_F(a, b, t) "v_pk_fma_f32 " a ", " a ", " b ", " t " op_sel:[1,1,0] op_sel_hi:[1,0,1] neg_lo:[0,1,0]\n\t"
  asm volatile(KG_M("%6", "%0", "%12") KG_M("%7", "%1", "%13") KG_M("%8", "%2", "%14") KG_M("%9", "%3", "%14") KG_M("%10", "%4", "%14") KG_M("%11", "%5", "%14")
               KG_F("%0", "%12", "%6") KG_F("%1", "%13", "%7") KG_F("%2", "%14", "%8") KG_F("%3", "%14", "%9") KG_F("%4", "%14", "%10") KG_F("%5", "%14", "%11")
               : "+v"(fa), "+v"(fb), "+v"(e0), "+v"(e1), "+v"(e2), "+v"(e3),
                 "=&v"(t0), "=&v"(t1), "=&v"(t2), "=&v"(t3), "=&v"(t4), "=&v"(t5)
               : "v"(z), "v"(w), "v"(c));
#undef KG_M
#undef KG_F
  h0 = pack_h(fa); h1 = pack_h(fb); h2 = pack_h(e0); h3 = pack_h(e1); h4 = pack_h(e2); h5 = pack_h(e3);
  put6<K, RB, 21 + 4 * K, 22 + 4 * K, 23 + 4 * K, 24 + 4 * K>(h0, h1, h2, h3, h4, h5);
}
DI void group_barrier(u32 cnt_addr, u32 target) {
  u32 one = 1, v, sv;
  asm volatile("s_waitcnt lgkmcnt(0)\n\t"
               "s_mov_b64 exec, 1\n\t"
               "ds_add_u32 %2, %3\n\t"
               "s_mov_b64 exec, -1\n"
               "1:\n\t"
               "ds_read_b32 %0, %2\n\t"
               "s_waitcnt lgkmcnt(0)\n\t"
               "v_readfirstlane_b32 %1, %0\n\t"
               "s_cmp_lt_u32 %1, %4\n\t"
               "s_cbranch_scc1 1b"
               : "=&v"(v), "=&s"(sv) : "v"(cnt_addr), "v"(one), "s"(target) : "memory", "scc");
}

#define MFMA16(a, b, c) __builtin_amdgcn_mfma_f32_16x16x32_bf16((a), (b), (c), 0, 0, 0)
#define MFMA32(a, b, c) __builtin_amdgcn_mfma_f32_32x32x16_bf16((a), (b), (c), 0, 0, 0)

template <int TG> DI constexpr int conv_tile(int i) { return TG == 0 ? (i == 0 ? 7 : i == 1 ? 5 : i == 2 ? 2 : 0) : (i == 0 ? 6 : i == 1 ? 4 : i == 2 ? 3 : 1); }

struct ConvSet { u32x2 al[8], ah[8]; s16x8 b[8]; };
template <int RR, int P> DI void conv_rd(ConvSet& s, u32 ka, u32 ub) {
  asm volatile("ds_read_b64 %0, %3 offset:%c5\n\tds_read_b64 %1, %3 offset:%c6\n\tds_read_b128 %2, %4 offset:%c7"
               : "=&v"(s.al[P]), "=&v"(s.ah[P]), "=&v"(s.b[P])
               : "v"(ka), "v"(ub), "i"(512 * (7 - P) + 64 * (3 - RR)), "i"(512 * (7 - P) + 64 * (3 - RR) + 8), "i"(512 * P + 64 * (3 - RR)));
}
DI s16x8 conv_a(const ConvSet& s, int p) {
  return __builtin_bit_cast(s16x8, __builtin_shufflevector(s.al[p], s.ah[p], 0, 1, 2, 3));
}
template <int TG, int RR, bool PRE> DI void conv_step(const ConvSet& c, ConvSet& n, u32 ka, u32 ub, f32x4 (&acc)[4]) {
  constexpr int NP = TG == 0 ? 8 : 7;
  asm volatile("s_waitcnt lgkmcnt(0)" ::: "memory");
  SCHED0();
#define CV_MM(i, p) acc[i] = MFMA16(conv_a(c, p), c.b[conv_tile<TG>(i) - (p)], acc[i])
#define CV_RD(p) do { if (PRE && (p) < NP) conv_rd<PRE ? RR + 1 : RR, (p)>(n, ka, ub); SCHED0(); } while (0)
  CV_MM(0, 0); CV_RD(0); CV_MM(1, 0); CV_RD(1);
  CV_MM(0, 1); CV_RD(2); CV_MM(1, 1); CV_RD(3);
  CV_MM(0, 2); CV_RD(4); CV_MM(1, 2); CV_RD(5);
  CV_MM(0, 3); CV_RD(6); CV_MM(1, 3); CV_RD(7);
  CV_MM(0, 4); CV_MM(1, 4); CV_MM(0, 5); CV_MM(2, 0); CV_MM(0, 6); CV_MM(2, 1); CV_MM(3, 0); CV_MM(2, 2);
  if (TG == 0) { CV_MM(0, 7); CV_MM(1, 5); } else { CV_MM(2, 3); CV_MM(3, 1); }
  SCHED0();
#undef CV_MM
#undef CV_RD
}
template <int TG> DI void conv_body(u32 ka, u32 ub, f32x4 (&acc)[4]) {
  ConvSet s0, s1;
  conv_rd<0, 0>(s0, ka, ub); conv_rd<0, 1>(s0, ka, ub); conv_rd<0, 2>(s0, ka, ub); conv_rd<0, 3>(s0, ka, ub);
  conv_rd<0, 4>(s0, ka, ub); conv_rd<0, 5>(s0, ka, ub); conv_rd<0, 6>(s0, ka, ub);
  if (TG == 0) conv_rd<0, 7>(s0, ka, ub);
  conv_step<TG, 0, true>(s0, s1, ka, ub, acc);
  conv_step<TG, 1, true>(s1, s0, ka, ub, acc);
  conv_step<TG, 2, true>(s0, s1, ka, ub, acc);
  conv_step<TG, 3, false>(s1, s0, ka, ub, acc);
}

__global__ __launch_bounds__(512, 2) void k_ssm(
    const u16* x, const u16* A_re, const u16* A_im, const u16* B_re, const u16* B_im, const u16* C_re,
    const u16* C_im, const u16* log_dt, const u16* Dskip, const u16* Wg, const u16* Wu, const u16* Wd, char* ws) {
  __shared__ __attribute__((aligned(16))) char lds[SL_TOTAL];
  const u32 tid = threadIdx.x;
  const u32 xcd = blockIdx.x & 7, idx = blockIdx.x >> 3;
  u32x4 cpf;
  if (idx < 4) {
    const GAS char* pc; u32 n1;
    asm volatile("s_getpc_b64 %0\n.Lssm_code_pc:\n\ts_mov_b32 %1, .Lffn2_code_end-.Lssm_code_pc-16" : "=s"(pc), "=s"(n1));
    asm volatile("global_load_dwordx4 %0, %1, %2" : "=&v"(cpf) : "v"(min(idx * 8192u + tid * 16u, n1)), "s"(pc) : "memory");
  }
  SCHED0();
  const bool chore = idx >= 28;
  const u32 cp = xcd * 28 + min(idx, 27u);
  const u32 xrow = (tid >> 3) * (D * 2), xb_off = xrow + min(tid & 7, 6u) * 16 + idx * (64 * D * 2) + xcd * 112;
  const u32 lane = tid & 63, tc = tid & 255;
  const u32 wave = __builtin_amdgcn_readfirstlane(tid >> 6);
  const u32 ch = wave >> 2, wq = wave & 3;
  u32 xb_lds = lds_addr(lds) + SL_X + (tid >> 3) * XR_ROW + (tid & 7) * 16;
  u32 xg_lds = lds_addr(lds) + SL_X + (tid & 31) * 2 * XR_ROW + (tid >> 5) * 4;
  u32 xg_off = WS_XG + xcd * 28 * (L * 8) + idx * 512 + (tid >> 5) * (L * 8) + (tid & 31) * 16;
  auto publish = [&](u32 tag, u32 a0, u32 a1, u32 a2, u32 a3) {
    st128_dev(ws, xg_off, u32x4{a0, tag, a1, tag});
    st128_dev(ws, xg_off + 14 * (L * 8), u32x4{a2, tag, a3, tag});
  };
  u32 xb_o = xb_off, cp4 = cp * 4;
  asm volatile("" : "+v"(xb_o), "+v"(xb_lds), "+v"(xg_lds), "+v"(xg_off));
  asm volatile("" : "+s"(cp4));
  SCHED0();
  asm volatile("" :: "s"(x), "s"(A_re), "s"(A_im), "s"(B_re), "s"(B_im), "s"(C_re), "s"(C_im), "s"(log_dt), "s"(Dskip),
               "s"(Wg), "s"(Wu), "s"(Wd), "s"(ws));
  u32x4 xr[7];
  if (wave == 7) {
    asm volatile("s_setprio 2\n\t"
                 "global_load_dwordx4 %0, %7, %8\n\tglobal_load_dwordx4 %1, %7, %8 offset:128\n\tglobal_load_dwordx4 %2, %7, %8 offset:256\n\t"
                 "global_load_dwordx4 %3, %7, %8 offset:384\n\tglobal_load_dwordx4 %4, %7, %8 offset:512\n\tglobal_load_dwordx4 %5, %7, %8 offset:640\n\t"
                 "global_load_dwordx4 %6, %7, %8 offset:768"
                 : "=&v"(xr[0]), "=&v"(xr[1]), "=&v"(xr[2]), "=&v"(xr[3]), "=&v"(xr[4]), "=&v"(xr[5]), "=&v"(xr[6])
                 : "v"(xrow + (tid & 7) * 16 + (blockIdx.x * 8 - 56) * (D * 2)), "s"(x) : "memory");
  }
  auto stats = [&](u32 tag) {
    asm volatile("" : "+v"(xr[0]), "+v"(xr[1]), "+v"(xr[2]), "+v"(xr[3]), "+v"(xr[4]), "+v"(xr[5]), "+v"(xr[6]));
    float s[4] = {0.f, 0.f, 0.f, 0.f}, q[4] = {0.f, 0.f, 0.f, 0.f};
#pragma unroll
    for (int j = 0; j < 7; ++j)
#pragma unroll
      for (int i = 0; i < 4; ++i) { s[i] = dot_bf(xr[j][i], 0x3f803f80u, s[i]); q[i] = dot_bf(xr[j][i], xr[j][i], q[i]); }
    float st = (s[0] + s[1]) + (s[2] + s[3]), qt = (q[0] + q[1]) + (q[2] + q[3]);
    oct_sum2(st, qt);
    const float mean = st * (1.0f / D);
    const u32x2 gr = u32x2{finish_stats(mean, qt * (1.0f / D) - mean * mean), tag};
    asm volatile("s_mov_b64 exec, %3\n\tglobal_store_dwordx2 %0, %1, %2 sc1\n\ts_mov_b64 exec, -1\n\ts_setprio 0"
                 :: "v"(WS_SG + blockIdx.x * 64 + lane), "v"(gr), "s"(ws), "s"(0x0101010101010101ull) : "memory");
  };
  u32x4 xb, ga0, ga1, ga2;
  u32 epoch, pa, pi_, pbr, pbi, pcr, pci, sl, sd;
  asm volatile("global_load_dwordx4 %0, %4, %5\n\t"
               "s_load_dword %1, %6, %c7\n\ts_load_dword %2, %8, %10\n\ts_load_dword %3, %9, %10"
               : "=&v"(xb), "=&s"(epoch), "=&s"(sl), "=&s"(sd)
               : "v"(xb_o), "s"(x), "s"(ws), "i"(WS_EPOCH), "s"(log_dt), "s"(Dskip), "s"(cp4) : "memory");
  SCHED0();
  const bool tiler = wave < 4 && !chore;
  const u32 wslice = 4 * (xcd >> 1) + (idx & 3), wchunk = (xcd & 1) * 7 + (idx >> 2);
  u32 gu0 = 0, gu1 = 0, ga_off = tc * 16;
  if (tiler) {
    gu0 = lane * 16 + ((lane * 37) >> 9) * (F * 2 - 224) + (wchunk * 32 + wq * 8) * (F * 2) + wslice * 224;
    gu1 = gu0 + 4 * (F * 2) - 1792;
    asm volatile("" : "+v"(gu0), "+v"(gu1));
    __builtin_amdgcn_s_setprio(1);
  }
  const char* gtab = (const char*)gather_tab.a + 4096;
  const char* gtab2 = gtab + 4096;
  asm volatile("" : "+s"(gtab), "+s"(gtab2));
  asm volatile("" : "+v"(ga_off));
  if (tid == 0) *(u32x4*)(lds + SL_T + 768 * 4) = u32x4{0u, 0u, 0u, 0u};
  if (wq == 3) *(u32x2*)(lds + SL_U + ch * U_CH + lane * 8) = u32x2{0u, 0u};
  SCHED0();
  asm volatile("global_load_ushort %0, %6, %7\n\tglobal_load_ushort %1, %6, %8\n\tglobal_load_ushort %2, %6, %9\n\t"
               "global_load_ushort %3, %6, %10\n\tglobal_load_ushort %4, %6, %11\n\tglobal_load_ushort %5, %6, %12"
               : "=&v"(pa), "=&v"(pi_), "=&v"(pbr), "=&v"(pbi), "=&v"(pcr), "=&v"(pci)
               : "v"(cp * 1024 + tid * 2), "s"(A_re), "s"(A_im), "s"(B_re), "s"(B_im), "s"(C_re), "s"(C_im) : "memory");
  asm volatile("global_load_dwordx4 %0, %3, %4 offset:-4096\n\tglobal_load_dwordx4 %1, %3, %4\n\tglobal_load_dwordx4 %2, %3, %5"
               : "=&v"(ga0), "=&v"(ga1), "=&v"(ga2) : "v"(ga_off), "s"(gtab), "s"(gtab2) : "memory");

  asm volatile("s_waitcnt vmcnt(9) lgkmcnt(0)" : "+v"(xb), "+v"(cpf) :: "memory");
  asm volatile("" : "+s"(epoch), "+s"(sl), "+s"(sd));
  const u32 pl = ch ? sl >> 16 : sl, pd = ch ? sd >> 16 : sd;
  *(LDS3 u32x4*)(uintptr_t)xb_lds = xb;
  const u32 tag = next_tag(epoch);
  asm volatile("s_waitcnt lgkmcnt(0)" ::: "memory");
  __builtin_amdgcn_s_barrier();
  asm volatile("" ::: "memory");
  {
    const LDS3 u32* xr_l = (const LDS3 u32*)(uintptr_t)xg_lds;
    const u32 a0 = xr_l[0], a1 = xr_l[XR_ROW / 4], a2 = xr_l[14], a3 = xr_l[XR_ROW / 4 + 14];
    asm volatile("s_waitcnt vmcnt(0)" : "+v"(pa), "+v"(pi_), "+v"(pbr), "+v"(pbi), "+v"(pcr), "+v"(pci), "+v"(ga0), "+v"(ga1), "+v"(ga2) :: "memory");
    if (tiler) {
      const u32 lw = lds_addr(lds) + SL_W + wq * 3584;
      asm volatile("s_mov_b32 m0, %0\n\ts_mov_b64 exec, %6\n\t"
                   "global_load_lds_dwordx4 %2, %4\n\tglobal_load_lds_dwordx4 %3, %4 offset:1792\n\t"
                   "s_mov_b32 m0, %1\n\ts_nop 0\n\t"
                   "global_load_lds_dwordx4 %2, %5\n\tglobal_load_lds_dwordx4 %3, %5 offset:1792\n\t"
                   "s_mov_b64 exec, -1"
                   :: "s"(lw), "s"(lw + 896), "v"(gu0), "v"(gu1), "s"(Wg), "s"(Wu), "s"(0x00ffffffffffffffull)
                   : "memory");
    }
    if (wave == 7) stats(tag); else publish(tag, a0, a1, a2, a3);
    if (chore) {
      if (blockIdx.x == 255 && tid < 128) *(u32*)(ws + WS_CNT + tid * 256) = 0u;
      return;
    }
  }
  s16x8 tf[2][2];
  float tcs[2];
  auto tile_read = [&]() {
    const u32 hh = lane >> 5, blk = (lane >> 4) & 1, q = (lane & 15) >> 2, p = lane & 3;
    const u32 a = lds_addr(lds) + SL_W + wq * 64 + hh * 3584 + q * 224 + blk * 896 + 8 * p;
#pragma unroll
    for (int i = 0; i < 2; ++i) {
      tf[i][0] = tr2(a + 32 * i, 0, 1792); tf[i][1] = tr2(a + 32 * i, 4 * 1792, 5 * 1792);
    }
#pragma unroll
    for (int i = 0; i < 2; ++i) tcs[i] = half_sum(dot_ones(tf[i][1], dot_ones(tf[i][0], 0.f)));
  };
  auto tile_store = [&]() {
    const u32 un = wslice * 14 + wchunk;
    const u32 wt = WS_WT + wslice * (196 * 1024) + (wq < 2 ? wchunk * 8 + 2 * wq : 112 + wchunk * 6 + 2 * wq - 4) * 1024 + lane * 16;
    const u32 ks = wq < 2 ? 4096 : 3072;
    const u32 cs = WS_CS + un * 896 + wq * 256 + lane * 4;
#pragma unroll
    for (int i = 0; i < 2; ++i) {
      if (i == 0 || wq < 3) {
        st128_dev(ws, wt + 1024 * i, __builtin_bit_cast(u32x4, tf[i][0]));
        st128_dev(ws, wt + 1024 * i + ks, __builtin_bit_cast(u32x4, tf[i][1]));
        asm volatile("s_mov_b64 exec, %3\n\tglobal_store_dword %0, %1, %2 sc1\n\ts_mov_b64 exec, -1"
                     :: "v"(cs + 128 * i), "v"(tcs[i]), "s"(ws), "s"(0xffffffffull) : "memory");
      }
    }
  };
  const __amdgpu_buffer_rsrc_t rs = __builtin_amdgcn_make_buffer_rsrc((void*)ws, 0, (int)WS_END, 0x00020000);
  const u32 cnt = lds_addr(lds) + SL_CNT + ch * 4;
  f32x2 cb;
  float inv_sc, p2, rv;
  {
    const float dt = rbf(__builtin_amdgcn_exp2f(bf_lo(pl) * 1.4426950408889634f));
    p2 = (dt * bf_lo(pa)) * 1.4426950408889634f;
    rv = (dt * bf_lo(pi_)) * 0.15915494309189535f;
    cb = cmul(f32x2{bf_lo(pcr), bf_lo(pci)}, f32x2{bf_lo(pbr), bf_lo(pbi)} * dt);
    const float cbx = cb.x, cby = cb.y;
    u32 eb = (u32)wave_imax(max(__builtin_bit_cast(int, cbx) & 0x7fffffff, __builtin_bit_cast(int, cby) & 0x7fffffff)) >> 23;
    asm("s_max_u32 %0, %0, 27\n\ts_min_u32 %0, %0, 227" : "+s"(eb) :: "scc");
    inv_sc = __builtin_bit_cast(float, (eb - 9) << 23);
    cb *= __builtin_bit_cast(float, (263 - eb) << 23);
  }

  {
    set_m0(lds_addr(lds) + ch * KG_CH + wq * 256);
    auto cpow = [&](float mp, float mr) {
      const float mag = __builtin_amdgcn_exp2f(p2 * mp);
      const float t = __builtin_amdgcn_fractf(rv * mr);
      return f32x2{__builtin_amdgcn_cosf(t), __builtin_amdgcn_sinf(t)} * mag;
    };
    const f32x2 z1 = cpow(1.f, 1.f), c8 = cpow(8.f, -8.f), w256 = cpow(256.f, 256.f);
    const f32x2 z2 = cmul(z1, z1), c16 = cmul(c8, c8);
    const f32x2 z4 = cmul(z2, z2), c32 = cmul(c16, c16);
    f32x2 e0 = f32x2{1.f, 0.f}, e1 = c8, e2 = c16, e3 = cmul(c16, c8);
    f32x2 fa = cb, fb = cmul(cb, w256);
    put6<0, 8, 21, 22, 23, 24>(pack_h(fa), pack_h(fb), 0x3c00u, pack_h(e1), pack_h(e2), pack_h(e3));
    const f32x2 g1 = cmul(fb, z2), g2 = cmul(fb, z4);
    kg_step<1, 12>(fa, fb, e0, e1, e2, e3, z1, w256, c32);
    const f32x2 g3 = cmul(g1, z4), g4 = cmul(fb, z4);
    kg_step<2, 14>(fa, fb, e0, e1, e2, e3, z1, w256, c32);
    const u32 h772 = pack_h(cmul(fb, z4));
    put6<9, 10, 11, 13, 15, 15>(pack_h(g1), pack_h(g2), pack_h(g3), pack_h(g4), h772, h772);
    kg_step<3, 16>(fa, fb, e0, e1, e2, e3, z1, w256, c32);
    kg_step<4, 17>(fa, fb, e0, e1, e2, e3, z1, w256, c32);
    kg_step<5, 18>(fa, fb, e0, e1, e2, e3, z1, w256, c32);
    kg_step<6, 19>(fa, fb, e0, e1, e2, e3, z1, w256, c32);
    kg_step<7, 20>(fa, fb, e0, e1, e2, e3, z1, w256, c32);
  }
  {
    char* kg = lds + ch * KG_CH + wq * 256;
    const char* fa = kg + (lane & 31) * KG_ROW + (lane >> 5) * 16;
    h16x8 a[8], b[8];
    asm volatile("s_waitcnt lgkmcnt(0)" ::: "memory");
#pragma unroll
    for (int s = 0; s < 8; ++s) { a[s] = *(const h16x8*)(fa + 32 * s); b[s] = *(const h16x8*)(fa + 21 * KG_ROW + 32 * s); }
    SCHED0();
    f32x16 acc;
#pragma unroll
    for (int i = 0; i < 16; ++i) acc[i] = 0.f;
#pragma unroll
    for (int s = 0; s < 8; ++s) acc = __builtin_amdgcn_mfma_f32_32x32x16_f16(a[s], b[s], acc, 0, 0, 0);
    char* pt = kg + (lane >> 4) * KG_ROW + (lane & 15) * 16;
#pragma unroll
    for (int q = 0; q < 3; ++q)
      *(f32x4*)(pt + 4 * q * KG_ROW) = f32x4{acc[4 * q], acc[4 * q + 1], acc[4 * q + 2], acc[4 * q + 3]} * inv_sc;
  }
  const u32 xo = WS_XG + cp * (L * 8);
  u32x4 gx[4], gs[4];
  auto poll = [&]() {
#pragma unroll
    for (int i = 0; i < 4; ++i) {
      gx[i] = __builtin_amdgcn_raw_buffer_load_b128(rs, tc * 16, xo + 4096 * i, 16);
      gs[i] = __builtin_amdgcn_raw_buffer_load_b128(rs, tc * 16, WS_SG + 4096 * i, 16);
    }
  };
  group_barrier(cnt, 4);
  if (wq < 3) {
    const f32x4* pt = (const f32x4*)(lds + ch * KG_CH + 4 * wq * KG_ROW + (lane >> 4) * KG_ROW + (lane & 15) * 16);
    const f32x4 s = (pt[0] + pt[16]) + (pt[32] + pt[48]);
    const u32 p01 = pack_bf(s[0], s[1]), p23 = pack_bf(s[2], s[3]);
    u16* T = (u16*)(lds + SL_T + wq * 1024 + ch * 2 + (lane >> 5) * 512 + (lane & 31) * 4);
    T[0] = (u16)p01; T[64] = (u16)(p01 >> 16); T[128] = (u16)p23; T[192] = (u16)(p23 >> 16);
  }
  group_barrier(cnt, 8);
  poll();
  const u32 sel = ch ? 0x07060302u : 0x05040100u;
  {
    u32 e[11];
#pragma unroll
    for (int j = 0; j < 11; ++j) {
      const u32 a = j < 4 ? ga0[j & 3] : j < 8 ? ga1[j & 3] : ga2[j & 3];
      e[j] = *(const LDS3 u32*)(uintptr_t)(a + lds_addr(lds));
    }
    char* kc = lds + ch * KC_CH + tc * 16;
#pragma unroll
    for (int s = 0; s < 4; ++s)
      *(u32x4*)(kc + s * KC_STRIDE) = u32x4{__builtin_amdgcn_perm(e[s + 1], e[s], sel), __builtin_amdgcn_perm(e[s + 3], e[s + 2], sel),
                                            __builtin_amdgcn_perm(e[s + 5], e[s + 4], sel), __builtin_amdgcn_perm(e[s + 7], e[s + 6], sel)};
    if (wq == 0 && lane < 8) *(u32x4*)(lds + ch * KC_CH + (lane >> 1) * KC_STRIDE + (256 + (lane & 1)) * 16) = u32x4{0u, 0u, 0u, 0u};
  }
  u32 cv_ka, cv_ub, ep_lo;
  {
    const u32 b = lane & 15, g = lane >> 4, r3 = 8 * (wq >> 1) + 6;
    const u32 gg = 16 * (g & 1) - 32 * (g >> 1);
    cv_ka = lds_addr(lds) + ch * KC_CH + 3 * KC_STRIDE + (2044 - 16 * r3 - 256 * 7) * 2 - (b & 3) * KC_STRIDE - 8 * (b >> 2) + gg;
    cv_ub = lds_addr(lds) + SL_U + ch * U_CH + 512 - 32 * r3 + 32 * b + gg;
    ep_lo = 32 * b + 8 * g;
    asm volatile("" : "+v"(cv_ka), "+v"(cv_ub), "+v"(ep_lo));
  }
  {
    bool clean = true;
    for (u32 spins = 0;; ++spins) {
      u32 bad = 0;
#pragma unroll
      for (int i = 0; i < 4; ++i) bad |= ((gx[i][1] ^ tag) | (gx[i][3] ^ tag)) | ((gs[i][1] ^ tag) | (gs[i][3] ^ tag));
      if (__all(bad == 0u)) break;
      if (spins >= 4096u) { clean = false; break; }
      __builtin_amdgcn_s_sleep(4);
      asm volatile("" ::: "memory");
      poll();
    }
    u32 xs[8], st[8];
#pragma unroll
    for (int i = 0; i < 8; ++i) { xs[i] = gx[i >> 1][2 * (i & 1)]; st[i] = gs[i >> 1][2 * (i & 1)]; }
    if (!clean) {
      *(u32*)(lds + SL_FLAG) = 1u;
#pragma unroll 1
      for (int i = 0; i < 8; ++i) {
        const u32* xrow = (const u32*)(x + (512 * (i >> 1) + 2 * tc + (i & 1)) * D);
        float s = 0.f, q = 0.f;
#pragma unroll 1
        for (int j = 0; j < D / 2; ++j) { const u32 w = xrow[j]; s = dot_bf(w, 0x3f803f80u, s); q = dot_bf(w, w, q); }
        const float mean = s * (1.0f / D);
#pragma unroll
        for (int k = 0; k < 7; ++k) { st[k] = st[k + 1]; xs[k] = xs[k + 1]; }
        st[7] = finish_stats(mean, q * (1.0f / D) - mean * mean);
        xs[7] = xrow[cp];
      }
    }
    u32x4 xw, uw;
#pragma unroll
    for (int i = 0; i < 4; ++i) {
      xw[i] = __builtin_amdgcn_perm(xs[2 * i + 1], xs[2 * i], sel);
      const f32x2 u2 = (f32x2{bf_lo(xw[i]), bf_hi(xw[i])} - f32x2{bf_lo(st[2 * i]), bf_lo(st[2 * i + 1])}) *
                       f32x2{__builtin_amdgcn_rcpf(bf_hi(st[2 * i])), __builtin_amdgcn_rcpf(bf_hi(st[2 * i + 1]))};
      uw[i] = pack_bf(u2.x, u2.y);
    }
    u32* up = (u32*)(lds + SL_U + ch * U_CH + 512) + tc;
    u32* xp = (u32*)(lds + SL_XX + ch * L * 2) + tc;
#pragma unroll
    for (int i = 0; i < 4; ++i) { up[256 * i] = uw[i]; xp[256 * i] = xw[i]; }
  }
  group_barrier(cnt, 12);
  if (tiler) { tile_read(); tile_store(); }
  if (blockIdx.x == 0 && tid == 0 && *(const u32*)(lds + SL_FLAG) == 0u)
    __hip_atomic_store((GAS u32*)(ws + WS_EPOCH), tag, __ATOMIC_RELAXED, __HIP_MEMORY_SCOPE_AGENT);
  if (wave < 7)
    asm volatile("s_mov_b32 m0, %0\n\ts_nop 0\n\tglobal_load_lds_dword %1, %2"
                 :: "s"(lds_addr(lds) + SL_DUMP), "v"(idx * 57344 + tid * 128), "s"(Wd) : "memory");
  const u32 tg = wq & 1, rh = wq >> 1;
  f32x4 acc[4];
#pragma unroll
  for (int i = 0; i < 4; ++i) acc[i] = f32x4{0.f, 0.f, 0.f, 0.f};
  if (tg == 0) conv_body<0>(cv_ka, cv_ub, acc);
  else conv_body<1>(cv_ka, cv_ub, acc);
  {
    f32x4* red = (f32x4*)(lds + ch * KG_CH + SL_RED) + lane;
    f32x4 y[2];
    if (rh) { red[wq * 128] = acc[0]; red[wq * 128 + 64] = acc[3]; y[0] = acc[1]; y[1] = acc[2]; }
    else { red[wq * 128] = acc[1]; red[wq * 128 + 64] = acc[2]; y[0] = acc[0]; y[1] = acc[3]; }
    const int t0 = rh ? 5 - tg : 7 - tg, t1 = rh ? 2 + tg : tg;
    const char* up = lds + SL_U + ch * U_CH + 512 + ep_lo;
    const char* xp = lds + SL_XX + ch * L * 2 + ep_lo;
    const u32x2 uu0 = *(const u32x2*)(up + 512 * t0), uu1 = *(const u32x2*)(up + 512 * t1);
    const u32x2 xx0 = *(const u32x2*)(xp + 512 * t0), xx1 = *(const u32x2*)(xp + 512 * t1);
    const float dsk = bf_lo(pd);
    f32x2 b0[2], b1[2];
#pragma unroll
    for (int j = 0; j < 2; ++j) {
      b0[j] = __builtin_elementwise_fma(f32x2{bf_lo(uu0[j]), bf_hi(uu0[j])}, f32x2{dsk, dsk}, f32x2{bf_lo(xx0[j]), bf_hi(xx0[j])}) + f32x2{y[0][2 * j], y[0][2 * j + 1]};
      b1[j] = __builtin_elementwise_fma(f32x2{bf_lo(uu1[j]), bf_hi(uu1[j])}, f32x2{dsk, dsk}, f32x2{bf_lo(xx1[j]), bf_hi(xx1[j])}) + f32x2{y[1][2 * j], y[1][2 * j + 1]};
    }
    asm volatile("" : "+v"(b0[0]), "+v"(b0[1]), "+v"(b1[0]), "+v"(b1[1]));
    group_barrier(cnt, 16);
    const f32x4 o0 = red[(wq ^ 2) * 128], o1 = red[(wq ^ 2) * 128 + 64];
    const u32 hb = WS_HT + (2 * cp + ch) * 256, ho = (ep_lo & 255) + (ep_lo >> 8) * (D * 256);
    __builtin_amdgcn_raw_buffer_store_b64(u32x2{pack_bf(b0[0].x + o0[0], b0[0].y + o0[1]), pack_bf(b0[1].x + o0[2], b0[1].y + o0[3])}, rs, ho, hb + 2 * D * 256 * t0, 0);
    __builtin_amdgcn_raw_buffer_store_b64(u32x2{pack_bf(b1[0].x + o1[0], b1[0].y + o1[1]), pack_bf(b1[1].x + o1[2], b1[1].y + o1[3])}, rs, ho, hb + 2 * D * 256 * t1, 0);
    wait_vm<2>();
  }
}

constexpr int F1_STAGE = 16384;
constexpr int F1_P2 = 4 * F1_STAGE;
constexpr int F1_CS = F1_P2 + 84 * 1024;
constexpr int F1_ARR = F1_CS + 7 * 256;
constexpr int F2_PS = F1_ARR + 32;
constexpr int F2_RES = F2_PS + 2048;
constexpr int F1_TOTAL = F2_RES + 4 * 2048;
static_assert(F1_TOTAL <= 163840, "lds map");
constexpr int F2_WIN = 8;
constexpr int PUB1 = 16;
constexpr int LOOK2 = 3;

DI void f1_publish(char* ws, u32 arr_addr, u32 last, int cnt) {
  u32 old, one = 1;
  asm volatile("s_mov_b64 exec, 1\n\tds_add_rtn_u32 %0, %1, %2\n\ts_mov_b64 exec, -1\n\ts_waitcnt lgkmcnt(0)" : "=&v"(old) : "v"(arr_addr), "v"(one) : "memory");
  if (__builtin_amdgcn_readfirstlane(old) == last && (threadIdx.x & 63) == 0)
    __hip_atomic_fetch_add((GAS u32*)(ws + WS_CNT + cnt * 256), 1u, __ATOMIC_RELAXED, __HIP_MEMORY_SCOPE_AGENT);
}
DI constexpr int f1_slot(int c) { return c <= 8 ? c : c < 13 ? c - 9 : 7; }
DI constexpr int f2_ks(int i) { return i < 16 ? (i >> 2) * 7 + (i & 3) : ((i - 16) / 3) * 7 + 4 + (i - 16) % 3; }
#define BARRIER() do { __builtin_amdgcn_s_barrier(); asm volatile("" ::: "memory"); } while (0)

__global__ __launch_bounds__(512, 2) void k_ffn(char* __restrict__ ws, u16* __restrict__ out, const char* __restrict__ Wd) {
  __shared__ __attribute__((aligned(16))) char lds[F1_TOTAL];
  const int tid = threadIdx.x, lane = tid & 63;
  const int wave = __builtin_amdgcn_readfirstlane(tid >> 6);
  const int xcd = blockIdx.x & 7, bi = blockIdx.x >> 3;
  const int panel = (xcd & 1) * 8 + (bi & 7), slice = (xcd >> 1) * 4 + (bi >> 3);
  const u32 vo = lane * 16;

  if (wave >= 4) {
    const int lw = wave - 4;
    const char* hb = ws + WS_HT + panel * (D * 256) + lw * 2048;
    const char* wb = ws + WS_WT + slice * (196 * 1024);
    const u32 ho = (lane >> 4) * 256 + (((lane & 15) ^ ((lane >> 4) << 2)) * 16);
    const u32 lbase = lds_addr(lds) + lw * 2048;
    auto stage = [&](int c) {
      asm volatile("s_mov_b32 m0, %0\n\ts_nop 0\n\t"
                   "global_load_lds_dwordx4 %2, %4\n\tglobal_load_lds_dwordx4 %2, %4 offset:1024\n\t"
                   "s_mov_b32 m0, %1\n\ts_nop 0\n\t"
                   "global_load_lds_dwordx4 %3, %5\n\tglobal_load_lds_dwordx4 %3, %5 offset:1024"
                   :: "s"(lbase + f1_slot(c) * F1_STAGE), "s"(lbase + f1_slot(c) * F1_STAGE + 8192),
                      "v"(ho), "v"(vo), "s"(hb + c * 8192), "s"(wb + lw * 2048 + c * 8192)
                   : "memory");
    };
    stage(0);
    SCHED0();
    auto p2 = [&](int i) {
      asm volatile("s_mov_b32 m0, %0\n\ts_nop 0\n\tglobal_load_lds_dwordx4 %1, %2"
                   :: "s"(lds_addr(lds) + F1_P2 + (i * 4 + lw) * 1024), "v"(vo), "s"(wb + (112 + i * 4 + lw) * 1024) : "memory");
    };
    f32x2 psum = {0.f, 0.f};
    stage(1); stage(2);
    f32x4 v[4];
    asm volatile("global_load_dwordx4 %0, %4, %6\n\tglobal_load_dwordx4 %1, %4, %6 offset:3584\n\t"
                 "global_load_dwordx4 %2, %4, %7\n\tglobal_load_dwordx4 %3, %5, %6"
                 : "=&v"(v[0]), "=&v"(v[1]), "=&v"(v[2]), "=&v"(v[3])
                 : "v"(lw * 896 + min(lane, 55) * 16), "v"((lw < 2 ? lw + 12 : lw) * 896 + min(lane, 55) * 16),
                   "s"(ws + WS_CS + slice * (14 * 896)), "s"(ws + WS_CS + slice * (14 * 896) + 7168) : "memory");
    wait_vm<2 * 4 + 4>();
    BARRIER();
    auto csum_a = [&]() {
      asm volatile("" : "+v"(v[0]), "+v"(v[1]), "+v"(v[2]), "+v"(v[3]));
      f32x4 t = (v[0] + v[1]) + v[2];
      if (lw < 2) t += v[3];
      *(f32x4*)(lds + F2_RES + lw * 1024 + lane * 16) = t;
    };
    auto csum_b = [&]() {
      const int i = tid - 256;
      const float* q = (const float*)(lds + F2_RES) + i;
      const float csum = (q[0] + q[256]) + (q[512] + q[768]);
      u32* ca = (u32*)(lds + F1_CS) + (i >> 5) * 64 + (i & 31);
      if (i < 224) { ca[0] = pack_bf(csum, csum - rbf(csum)); ca[32] = 0u; }
    };
    auto part = [&](int c0, int c1) {
      if (slice >= c0 && slice <= c1) {
        const int q = (lane & 15) >> 2, sl = c0 == 8 ? (slice == 13 ? 7 : slice == 8 ? 8 : slice - 9) : slice;
        const u32 a = lds_addr(lds) + sl * F1_STAGE + (8 * (lane >> 5) + q) * 256 + ((lw ^ q) * 64) + ((lane >> 4) & 1) * 32 + 8 * (lane & 3);
        const u32x4 n0 = __builtin_bit_cast(u32x4, tr2(a, 0, 1024)), n1 = __builtin_bit_cast(u32x4, tr2(a, 4096, 5120));
        float s0 = 0.f, s1 = 0.f, q0 = 0.f, q1 = 0.f;
#pragma unroll
        for (int i = 0; i < 8; ++i) {
          const u32 w = i < 4 ? n0[i & 3] : n1[i & 3];
          const float lo = bf_lo(w), hi = bf_hi(w);
          s0 += lo; q0 = __builtin_fmaf(lo, lo, q0);
          s1 += hi; q1 = __builtin_fmaf(hi, hi, q1);
          asm("" : "+v"(s0)); asm("" : "+v"(q0)); asm("" : "+v"(s1)); asm("" : "+v"(q1));
        }
        psum = f32x2{s0 + s1, q0 + q1};
        asm volatile("" : "+v"(psum));
      }
    };
#define CLOSE(n) do { asm volatile("s_waitcnt vmcnt(" #n ") lgkmcnt(0)" ::: "memory"); BARRIER(); } while (0)
    stage(3); stage(4); CLOSE(16);
    stage(5); stage(6); CLOSE(12);
    part(0, 3); stage(7); stage(8); csum_a(); CLOSE(12);
    stage(9); stage(10); csum_b(); CLOSE(12);
    part(4, 7); stage(11); stage(12); CLOSE(12);
    stage(13); p2(0); p2(1); p2(2); p2(3); CLOSE(12);
    p2(4); p2(5); p2(6); p2(7); p2(8); p2(9); p2(10); p2(11); CLOSE(12);
    part(8, 13); CLOSE(0);
#undef CLOSE
    const __amdgpu_buffer_rsrc_t rs = __builtin_amdgcn_make_buffer_rsrc((void*)ws, 0, (int)WS_END, 0x00020000);
    if (slice < 14) {
      const float a = half_sum(psum.x), b = half_sum(psum.y);
      if (lane < 32) __builtin_amdgcn_raw_buffer_store_b64(__builtin_bit_cast(u32x2, f32x2{a, b}), rs, lane * 8, WS_PS + ((panel * 14 + slice) * 128 + lw * 32) * 8, 16);
    }
    p2(12); p2(13); p2(14); p2(15); p2(16); p2(17); p2(18); p2(19); p2(20);
    const bool down = bi < 28;
    const int tgp = (xcd & 1) * 16 + (xcd >> 1) * 4 + (bi & 3), dp = down ? bi >> 2 : 0;
    const u32 a1 = lds_addr(lds) + lw * 16384, a2 = lds_addr(lds) + F1_P2 + lw * 21504;
    const u32 wsrc = (lane >> 3) * (D * 2) + (((lane & 7) ^ ((lane >> 2) & 4)) * 16);
    const char* wbase = Wd + lw * 448 * (D * 2) + dp * 128;
    auto wd_load = [&](u32 dst, int i0, int n) {
#pragma unroll
      for (int i = 0; i < n; ++i) {
        const char* b0 = wbase + f2_ks(i0 + i) * 16 * (D * 2);
        asm volatile("s_mov_b32 m0, %0\n\ts_nop 0\n\tglobal_load_lds_dwordx4 %1, %2\n\tglobal_load_lds_dwordx4 %1, %3 offset:1024"
                     :: "s"(dst + i * 2048), "v"(wsrc), "s"(b0), "s"(b0 + 8 * (D * 2) - 1024) : "memory");
      }
    };
    const u32 q4 = (lane & 15) >> 2;
    const u32 tq = (4 * (lane >> 5) + q4) * 128 + (q4 >> 1) * 64 + ((lane >> 4) & 1) * 32 + (lane & 3) * 8;
    const u32 t10 = a1 + tq, t11 = a1 + (tq ^ 64), t20 = a2 + tq, t21 = a2 + (tq ^ 64);
    wd_load(a1, 0, 8);
    wait_vm<16>();
    BARRIER();
    f1_publish(ws, lds_addr(lds) + F1_ARR, 7u, 64 + panel * 4 + (slice >> 2));
    if (!down) { wait_vm<0>(); return; }
    const u32 cnt_off = WS_CNT + ((tgp >> 1) * 4 + lw) * 256;
    s16x8 ra[8][2];
    u32 look;
#pragma unroll
    for (int k = 0; k < 8; ++k) {
      wait_vm<14>();
      ra[k][0] = tr2(t10, k * 2048, k * 2048 + 1024); ra[k][1] = tr2(t11, k * 2048, k * 2048 + 1024);
      asm volatile("" : "+v"(ra[k][0]), "+v"(ra[k][1]));
      wd_load(a1 + k * 2048, 8 + k, 1);
      if (k == 4) asm volatile("global_load_dword %0, %1, %2 sc1" : "=&v"(look) : "v"(cnt_off), "s"(ws) : "memory");
    }
    const int tok0 = tgp * 64 + (lw & 1) * 32, d0 = dp * 64 + (lw >> 1) * 32;
    const u32 aux = lds_addr(lds) + F2_RES + lw * 2048;
    {
      const char* hr = ws + WS_HT + (tok0 >> 7) * (D * 256) + d0 * 256 + (tok0 & 127) * 2;
      asm volatile("s_mov_b32 m0, %0\n\ts_nop 0\n\tglobal_load_lds_dwordx4 %1, %2\n\tglobal_load_lds_dwordx4 %1, %3 offset:1024"
                   :: "s"(aux), "v"((lane >> 2) * 256 + (lane & 3) * 16), "s"(hr), "s"(hr + 4096 - 1024) : "memory");
    }
    auto wait_cnt = [&](int set) {
      for (u32 spins = 0; spins < (1u << 16); ++spins) {
        const u32 c = __builtin_amdgcn_raw_buffer_load_b32(rs, 0, cnt_off + set * 64 * 256, 16);
        if (__builtin_amdgcn_readfirstlane(c) >= 4u) break;
        __builtin_amdgcn_s_sleep(2);
        asm volatile("" ::: "memory");
      }
      asm volatile("" ::: "memory");
    };
    asm volatile("s_waitcnt vmcnt(8)" : "+v"(look) :: "memory");
    if (__builtin_amdgcn_readfirstlane(look) < 4u) wait_cnt(0);
    const u32 sb = WS_HF + (tgp * 224 + lw * 28) * 1024;
    s16x8 rb[F2_WIN][2];
    auto ldb = [&](int i) {
      const int k = f2_ks(i);
      rb[i % F2_WIN][0] = __builtin_bit_cast(s16x8, __builtin_amdgcn_raw_buffer_load_b128(rs, vo + (k & 3) * 1024, sb + (k >> 2) * 4096, 16));
      rb[i % F2_WIN][1] = __builtin_bit_cast(s16x8, __builtin_amdgcn_raw_buffer_load_b128(rs, vo + (k & 3) * 1024, sb + (k >> 2) * 4096 + 112 * 1024, 16));
    };
#pragma unroll
    for (int k = 0; k < F2_WIN; ++k) { ldb(k); SCHED0(); }
    {
      u32 c, sv;
      asm volatile("1:\n\tds_read_b32 %0, %2\n\ts_waitcnt lgkmcnt(0)\n\tv_readfirstlane_b32 %1, %0\n\ts_cmp_lt_u32 %1, 4\n\ts_cbranch_scc0 2f\n\ts_sleep 1\n\ts_branch 1b\n2:"
                   : "=&v"(c), "=&s"(sv) : "v"(lds_addr(lds) + F1_ARR + 4) : "memory", "scc");
    }
    wd_load(a2, 16, 10);
    SCHED0();
    f32x2 ps[4];
    f32x16 acc[4];
#pragma unroll
    for (int a = 0; a < 4; ++a)
#pragma unroll
      for (int i = 0; i < 16; ++i) acc[a][i] = 0.f;
    u32x2 hres[4];
    s16x8 la[2][2];
    static_assert(F2_WIN <= 8 && F2_WIN >= 2, "LDS-DMA must be older than fragments consumed before it is read");
#pragma unroll
    for (int k = 0; k < 28; ++k) {
      const int n = k + 1;
      if (n >= 8 && n < 28) {
        const int o = (n < 16 ? n - 8 : n < 26 ? n - 16 : n - 26) * 2048;
        la[n & 1][0] = tr2(n >= 16 && n < 26 ? t20 : t10, o, o + 1024);
        la[n & 1][1] = tr2(n >= 16 && n < 26 ? t21 : t11, o, o + 1024);
      }
#pragma unroll
      for (int a = 0; a < 4; ++a) acc[a] = MFMA32(k < 8 ? ra[k < 8 ? k : 0][a >> 1] : la[k & 1][a >> 1], rb[k % F2_WIN][a & 1], acc[a]);
      SCHED0();
      if (k == 9) wd_load(a1, 26, 2);
      if (k + F2_WIN == 16) {
        asm volatile("s_waitcnt vmcnt(%c1)" : "+v"(look) : "i"(2 * LOOK2) : "memory");
        if (__builtin_amdgcn_readfirstlane(look) < 4u) wait_cnt(1);
#pragma unroll
        for (int i = 0; i < 4; ++i)
          ps[i] = __builtin_bit_cast(f32x2, __builtin_amdgcn_raw_buffer_load_b64(rs, lane * 8 + (i < 2 || lw < 3 ? i : i - 2) * 1024,
                                                                                 WS_PS + (tgp >> 1) * (14 * 1024) + (tgp & 1) * 512 + lw * 4096, 16));
        SCHED0();
      }
      if (k + F2_WIN + LOOK2 == 16) asm volatile("global_load_dword %0, %1, %2 sc1" : "=&v"(look) : "v"(cnt_off + 64 * 256), "s"(ws) : "memory");
      if (k + F2_WIN < 28) ldb(k + F2_WIN);
      if (k == 18) {
        const f32x2 t = lw < 3 ? (ps[0] + ps[1]) + (ps[2] + ps[3]) : ps[0] + ps[1];
        *(LDS3 f32x2*)(uintptr_t)(lds_addr(lds) + F2_PS + lw * 512 + lane * 8) = t;
      }
      if (k == 20) {
        const u32 a = aux + (4 * (lane >> 5) + ((lane & 15) >> 2)) * 64 + ((lane >> 4) & 1) * 32 + (lane & 3) * 8;
#pragma unroll
        for (int g = 0; g < 4; ++g) hres[g] = __builtin_bit_cast(u32x2, __builtin_amdgcn_ds_read_tr16_b64_v4i16((LDS3 s16x4*)(uintptr_t)(a + g * 512)));
      }
      SCHED0();
    }
    f32x16 mine;
#pragma unroll
    for (int a = 0; a < 4; ++a) {
      if (a == lw) { mine = acc[a]; }
      else {
        LDS3 f32x4* pp = (LDS3 f32x4*)(uintptr_t)(a1 + a * 4096 + vo);
#pragma unroll
        for (int g = 0; g < 4; ++g) pp[g * 64] = f32x4{acc[a][4 * g], acc[a][4 * g + 1], acc[a][4 * g + 2], acc[a][4 * g + 3]};
      }
    }
    __syncthreads();
    float rs2;
    {
      const LDS3 f32x2* pq = (const LDS3 f32x2*)(uintptr_t)(lds_addr(lds) + F2_PS + ((lw & 1) * 32 + (lane & 31)) * 8);
      const f32x2 t = (pq[0] + pq[64]) + (pq[128] + pq[192]);
      const float mean = t.x * (1.0f / D);
      const float var = fmaxf(t.y * (1.0f / D) - mean * mean, 0.f);
      const float r = __builtin_amdgcn_rcpf(rbf(__builtin_amdgcn_sqrtf(rbf(rbf(var) + 1e-5f))));
      rs2 = r * r;
    }
    u32 pk[8];
    const u32 rdb = lds_addr(lds) + lw * 4096 + vo;
#pragma unroll
    for (int g = 0; g < 4; ++g) {
      const f32x4 p0 = *(const LDS3 f32x4*)(uintptr_t)(rdb + ((lw + 1) & 3) * 16384 + g * 1024);
      const f32x4 p1 = *(const LDS3 f32x4*)(uintptr_t)(rdb + ((lw + 2) & 3) * 16384 + g * 1024);
      const f32x4 p2v = *(const LDS3 f32x4*)(uintptr_t)(rdb + ((lw + 3) & 3) * 16384 + g * 1024);
      const f32x4 sv = (p0 + p1) + (p2v + f32x4{mine[4 * g], mine[4 * g + 1], mine[4 * g + 2], mine[4 * g + 3]});
      const f32x2 o0 = __builtin_elementwise_fma(f32x2{sv[0], sv[1]}, f32x2{rs2, rs2}, f32x2{bf_lo(hres[g][0]), bf_hi(hres[g][0])});
      const f32x2 o1 = __builtin_elementwise_fma(f32x2{sv[2], sv[3]}, f32x2{rs2, rs2}, f32x2{bf_lo(hres[g][1]), bf_hi(hres[g][1])});
      pk[2 * g] = pack_bf(o0.x, o0.y);
      pk[2 * g + 1] = pack_bf(o1.x, o1.y);
    }
    char* ob = (char*)out + tok0 * (D * 2) + d0 * 2;
#pragma unroll
    for (int k = 0; k < 2; ++k) {
      const auto r0 = __builtin_amdgcn_permlane32_swap(pk[4 * k], pk[4 * k + 2], false, false);
      const auto r1 = __builtin_amdgcn_permlane32_swap(pk[4 * k + 1], pk[4 * k + 3], false, false);
      *(u32x4*)(ob + (u32)((lane & 31) * (D * 2) + (lane >> 5) * 16 + 32 * k)) = u32x4{r0[0], r1[0], r0[1], r1[1]};
    }
    if (blockIdx.x == 0 && tid == 256) { u32* e = (u32*)(ws + WS_EPOCH); *e = next_tag(*e) ^ 0x5bd1e995u; }
    asm volatile(".Lffn2_code_end:");
    return;
  }

  const int q = (lane & 15) >> 2;
  u32 bB[3], bA[3];
#pragma unroll
  for (int i = 0; i < 3; ++i) {
    bB[i] = lds_addr(lds) + i * 65536 + (8 * (lane >> 5) + q) * 256 + ((wave ^ q) * 64) + ((lane >> 4) & 1) * 32 + 8 * (lane & 3);
    bA[i] = lds_addr(lds) + i * 65536 + 8192 + vo;
    asm volatile("" : "+v"(bB[i]), "+v"(bA[i]));
  }
  char* dst = ws + WS_HF + ((panel * 4 + wave) * 112 + slice * 7) * 1024;
  s16x8 bk[28], fa[2][4];
  f32x16 acc[7];
  auto rd1 = [&](int k) {
    const int sl = f1_slot(k >> 1), o = (sl & 3) * F1_STAGE + (k & 1) * 4096;
    bk[k] = tr2(bB[sl >> 2], o, o + 1024);
#pragma unroll
    for (int t = 0; t < 4; ++t) fa[k & 1][t] = *(const LDS3 s16x8*)(uintptr_t)(bA[sl >> 2] + o + t * 1024);
  };
  auto rd2 = [&](int k) {
#pragma unroll
    for (int t = 0; t < 3; ++t) {
      const int kb = 56 + k * 3 + t;
      fa[k & 1][t] = *(const LDS3 s16x8*)(uintptr_t)(bA[kb >> 6] + (kb & 63) * 1024);
    }
  };
  const u32 sw = (lane & 15) == ((lane >> 4) & 1) ? 0x3f803f80u : 0u;
  const s16x8 sel = __builtin_bit_cast(s16x8, u32x4{sw, sw, sw, sw});
  f32x4 msum = {0.f, 0.f, 0.f, 0.f};
  auto put = [&](int t) {
    u32x4 o;
#pragma unroll
    for (int j = 0; j < 4; ++j) {
      float a0 = relu(acc[t][2 * j]) * acc[t][8 + 2 * j], a1 = relu(acc[t][2 * j + 1]) * acc[t][9 + 2 * j];
      asm("" : "+v"(a0)); asm("" : "+v"(a1));
      o[j] = pack_bf(a0, a1);
    }
    st128_dev(dst + (t >> 2) * 4096, vo + (t & 3) * 1024, o);
  };
  const f32x16 zero = {0.f, 0.f, 0.f, 0.f, 0.f, 0.f, 0.f, 0.f, 0.f, 0.f, 0.f, 0.f, 0.f, 0.f, 0.f, 0.f};
  if (tid == 0) *(u32x4*)(lds + F1_ARR) = u32x4{0u, 0u, 0u, 0u};
  asm volatile("s_waitcnt lgkmcnt(0)" ::: "memory");
  BARRIER();
  rd1(0);
  SCHED0();
#pragma unroll
  for (int k = 0; k < 28; ++k) {
    if (k == 1 || (k & 3) == 3) {
      asm volatile("s_waitcnt lgkmcnt(0)" ::: "memory");
      BARRIER();
      SCHED0();
    }
    if (k < 27) rd1(k + 1); else rd2(0);
#pragma unroll
    for (int t = 0; t < 4; ++t) acc[t] = MFMA32(fa[k & 1][t], bk[k], k == 0 ? zero : acc[t]);
    msum = MFMA16(sel, bk[k], msum);
    __builtin_amdgcn_sched_group_barrier(0x8, 1, 0);
    __builtin_amdgcn_sched_group_barrier(0x100, 3, 0);
    __builtin_amdgcn_sched_group_barrier(0x8, 1, 0);
    __builtin_amdgcn_sched_group_barrier(0x100, 3, 0);
    __builtin_amdgcn_sched_group_barrier(0x8, 3, 0);
    SCHED0();
  }
  {
    const u32* ca = (const u32*)(lds + F1_CS) + lane;
    u32 cw[7];
#pragma unroll
    for (int t = 0; t < 7; ++t) cw[t] = ca[t * 64];
    const float m0 = msum[0], m1 = msum[1];
    const auto mr = __builtin_amdgcn_permlane16_swap(__builtin_bit_cast(u32, m0), __builtin_bit_cast(u32, m1), false, false);
    const u32 mt = mr[0];
    const float nmean = __builtin_bit_cast(float, mt) * (-1.0f / D);
    const s16x8 mo = __builtin_bit_cast(s16x8, u32x4{pack_bf(nmean, nmean), 0u, 0u, 0u});
#pragma unroll
    for (int t = 0; t < 4; ++t) acc[t] = MFMA32(__builtin_bit_cast(s16x8, u32x4{cw[t], 0u, 0u, 0u}), mo, acc[t]);
    put(0);
    SCHED0();
#pragma unroll
    for (int t = 4; t < 7; ++t) acc[t] = MFMA32(__builtin_bit_cast(s16x8, u32x4{cw[t], 0u, 0u, 0u}), mo, zero);
    SCHED0();
  }
#pragma unroll
  for (int k = 0; k < 26; ++k) {
    if (k == 15) BARRIER();
    rd2(k + 1);
#pragma unroll
    for (int t = 0; t < 3; ++t) acc[4 + t] = MFMA32(fa[k & 1][t], bk[k], acc[4 + t]);
    if (k == 0 || k == 2 || k == 4) put(1 + k / 2);
    if (k == PUB1) {
      wait_vm<0>();
      f1_publish(ws, lds_addr(lds) + F1_ARR + 8, 3u, panel * 4 + (slice >> 2));
    }
#pragma unroll
    for (int i = 0; i < 3; ++i) {
      __builtin_amdgcn_sched_group_barrier(0x8, 1, 0);
      __builtin_amdgcn_sched_group_barrier(0x100, 1, 0);
      __builtin_amdgcn_sched_group_barrier(0x2, 7, 0);
    }
    SCHED0();
  }
  rd2(27);
  {
    u32 one = 1;
    asm volatile("s_mov_b64 exec, 1\n\tds_add_u32 %0, %1\n\ts_mov_b64 exec, -1" :: "v"(lds_addr(lds) + F1_ARR + 4), "v"(one) : "memory");
  }
  SCHED0();
  acc[4] = MFMA32(fa[0][0], bk[26], acc[4]); acc[4] = MFMA32(fa[1][0], bk[27], acc[4]);
  acc[5] = MFMA32(fa[0][1], bk[26], acc[5]); acc[5] = MFMA32(fa[1][1], bk[27], acc[5]);
  SCHED0();
  acc[6] = MFMA32(fa[0][2], bk[26], acc[6]); acc[6] = MFMA32(fa[1][2], bk[27], acc[6]);
  put(4);
  SCHED0();
  put(5); put(6);
  wait_vm<0>();
  f1_publish(ws, lds_addr(lds) + F1_ARR, 7u, 64 + panel * 4 + (slice >> 2));
}

extern "C" void kernel_launch(void* const* d_in, const int* in_sizes, int n_in,
                              void* d_out, int out_size, void* d_ws, size_t ws_size,
                              hipStream_t stream) {
  const u16* x = (const u16*)d_in[0];
  char* ws = (char*)d_ws;
  k_ssm<<<256, 512, 0, stream>>>(x, (const u16*)d_in[1], (const u16*)d_in[2], (const u16*)d_in[3], (const u16*)d_in[4],
                                 (const u16*)d_in[5], (const u16*)d_in[6], (const u16*)d_in[7], (const u16*)d_in[8],
                                 (const u16*)d_in[10], (const u16*)d_in[11], (const u16*)d_in[12], ws);
  k_ffn<<<256, 512, 0, stream>>>(ws, (u16*)d_out, (const char*)d_in[12]);
}
```

```cpp
#include <hip/hip_runtime.h>
#include <hip/hip_bf16.h>
#include <stdint.h>

typedef unsigned short u16;
typedef unsigned int u32;
typedef unsigned long long u64;
typedef short s16x4 __attribute__((ext_vector_type(4)));
typedef short s16x8 __attribute__((ext_vector_type(8)));
typedef _Float16 h16x8 __attribute__((ext_vector_type(8)));
typedef _Float16 h16x2 __attribute__((ext_vector_type(2)));
typedef __bf16 bf16x2 __attribute__((ext_vector_type(2)));
typedef float f32x2 __attribute__((ext_vector_type(2)));
typedef float f32x4 __attribute__((ext_vector_type(4)));
typedef float f32x16 __attribute__((ext_vector_type(16)));
typedef u32 u32x2 __attribute__((ext_vector_type(2)));
typedef u32 u32x4 __attribute__((ext_vector_type(4)));
#define LDS3 __attribute__((address_space(3)))
#define GAS __attribute__((address_space(1)))
#define DI __device__ __forceinline__
#define SCHED0() __builtin_amdgcn_sched_barrier(0)

#define SEAM_SLEEP 4
constexpr int L = 2048, D = 448, NM = 256, F = 1792;

constexpr u32 WS_SG = 0;
constexpr u32 WS_EPOCH = 16384;
constexpr u32 WS_CNT = 32768;
constexpr u32 WS_CNTA = 65536;
constexpr u32 WS_HT = 81920;
constexpr u32 WS_HF = WS_HT + D * L * 2;
constexpr u32 WS_XG = WS_HF + L * F * 2;
constexpr u32 WS_WT = WS_XG + (D / 2) * L * 8;
constexpr u32 WS_CS = WS_WT + 2 * D * F * 2;
constexpr u32 WS_PS = WS_CS + 16 * 14 * 896;
constexpr u32 WS_END = WS_PS + 16 * 14 * 1024;

#define STAMP(kid, i) do {} while (0)
#define STAMP_DECL do {} while (0)

DI float bf_lo(u32 w) { return __builtin_bit_cast(float, w << 16); }
DI float bf_hi(u32 w) { return __builtin_bit_cast(float, w & 0xffff0000u); }
DI float rbf(float a) { return (float)(__bf16)a; }
DI u32 pack_bf(float a, float b) { bf16x2 v; v.x = (__bf16)a; v.y = (__bf16)b; return __builtin_bit_cast(u32, v); }
DI u32 pack_h(f32x2 a) { h16x2 v; v.x = (_Float16)a.x; v.y = (_Float16)a.y; return __builtin_bit_cast(u32, v); }
DI u32 lds_addr(const void* p) { return (u32)(uintptr_t)(const LDS3 void*)p; }
DI float relu(float v) { return __builtin_bit_cast(float, max(__builtin_bit_cast(int, v), 0)); }
DI u32 next_tag(u32 e) { return e * 2654435761u + 0x9E3779B9u; }

DI int wave_imax(int c) {
#define RED1(ctl) "s_nop 1\n\tv_max_i32_dpp %0, %0, %0 " ctl "\n\t"
  asm(RED1("row_shr:1 row_mask:0xf bank_mask:0xf bound_ctrl:1") RED1("row_shr:2 row_mask:0xf bank_mask:0xf bound_ctrl:1")
      RED1("row_shr:4 row_mask:0xf bank_mask:0xf bound_ctrl:1") RED1("row_shr:8 row_mask:0xf bank_mask:0xf bound_ctrl:1")
      RED1("row_bcast:15 row_mask:0xa bank_mask:0xf") RED1("row_bcast:31 row_mask:0xc bank_mask:0xf")
      "s_nop 0"
      : "+v"(c));
#undef RED1
  return __builtin_amdgcn_readlane(c, 63);
}
DI void oct_sum2(float& a, float& b) {
#define RED2(ctl) "s_nop 0\n\tv_add_f32_dpp %0, %0, %0 " ctl " row_mask:0xf bank_mask:0xf\n\tv_add_f32_dpp %1, %1, %1 " ctl " row_mask:0xf bank_mask:0xf\n\t"
  asm("s_nop 0\n\t" RED2("quad_perm:[1,0,3,2]") RED2("quad_perm:[2,3,0,1]") RED2("row_half_mirror") "s_nop 0" : "+v"(a), "+v"(b));
#undef RED2
}
DI void st128_dev(const char* base, u32 off, u32x4 v) {
  asm volatile("global_store_dwordx4 %0, %1, %2 sc1\n\ts_nop 1" :: "v"(off), "v"(v), "s"(base) : "memory");
}
DI f32x2 cmul(f32x2 a, f32x2 b) {
  f32x2 t, o;
  asm("v_pk_mul_f32 %0, %1, %2 op_sel_hi:[0,1]" : "=v"(t) : "v"(a), "v"(b));
  asm("v_pk_fma_f32 %0, %1, %2, %3 op_sel:[1,1,0] op_sel_hi:[1,0,1] neg_lo:[0,1,0]" : "=v"(o) : "v"(a), "v"(b), "v"(t));
  return o;
}
DI u32 finish_stats(float mean, float var) { return pack_bf(mean, __builtin_amdgcn_sqrtf(rbf(rbf(fmaxf(var, 0.f)) + 1e-5f))); }
DI float dot_bf(u32 a, u32 b, float c) {
  return __builtin_amdgcn_fdot2_f32_bf16(__builtin_bit_cast(bf16x2, a), __builtin_bit_cast(bf16x2, b), c, false);
}

constexpr int KG_ROW = 1040;
constexpr int KG_CH = 53 * KG_ROW;
constexpr int KC_STRIDE = 4160;
constexpr int KC_CH = KG_CH;
constexpr int SL_RED = 4 * KC_STRIDE;
constexpr int SL_U = 2 * KG_CH;
constexpr int U_CH = (256 + L) * 2;
constexpr int SL_X = SL_U + 2 * U_CH;
constexpr int XR_ROW = 144;
constexpr int SL_XX = SL_X + 64 * XR_ROW;
constexpr int SL_T = SL_XX + 2 * L * 2;
constexpr int SL_FLAG = SL_T + 769 * 4;
constexpr int SL_CNT = SL_T + 770 * 4;
constexpr int SL_W = SL_T + 772 * 4;
constexpr int SL_DUMP = SL_W + 32 * 448 + 64;
constexpr int SL_TOTAL = SL_DUMP + 256;
static_assert(SL_RED + 4 * 2048 <= KG_CH && KG_CH % 16 == 0 && SL_W % 16 == 0 && SL_TOTAL <= 163840 && KG_CH + 768 < 65536, "lds map");

struct GatherTab { u32 a[256 * 12]; };
constexpr GatherTab make_gather_tab() {
  GatherTab t = {};
  for (int tc = 0; tc < 256; ++tc)
    for (int j = 0; j < 12; ++j) {
      const int i = 2047 - 8 * tc - j;
      int slot = 768;
      if (i >= 0) {
        const int s = i < 256 ? 0 : i < 512 ? 1 : i < 1024 ? 2 : 3;
        const int lag = s ? ((i + (1 << (s - 1)) - 1 + ((i >> s) & 1)) >> s) << s : i;
        const int lo = (lag & 255) >> 3, b = lag >> 8, r = lag & 7;
        const int row = b == 0 ? r : b == 1 ? 8 + (r >> 1) : b < 4 ? 12 + 2 * (b - 2) + (r >> 2) : 12 + b;
        slot = row * 32 + lo;
      }
      t.a[(j >> 2) * 1024 + tc * 4 + (j & 3)] = SL_T + 4 * slot;
    }
  return t;
}
static __device__ const GatherTab gather_tab = make_gather_tab();

DI s16x8 tr2(u32 a, int o0, int o1) {
  const s16x4 lo = __builtin_amdgcn_ds_read_tr16_b64_v4i16((LDS3 s16x4*)(uintptr_t)(a + o0));
  const s16x4 hi = __builtin_amdgcn_ds_read_tr16_b64_v4i16((LDS3 s16x4*)(uintptr_t)(a + o1));
  return __builtin_shufflevector(lo, hi, 0, 1, 2, 3, 4, 5, 6, 7);
}
template <int N> DI void wait_vm() { asm volatile("s_waitcnt vmcnt(%c0)" :: "i"(N) : "memory"); }
DI float dot_ones(s16x8 f, float s) {
  const u32x4 w4 = __builtin_bit_cast(u32x4, f);
#pragma unroll
  for (int i = 0; i < 4; ++i) s = dot_bf(w4[i], 0x3f803f80u, s);
  return s;
}
DI float half_sum(float v) {
  const u32 w = __builtin_bit_cast(u32, v);
  const auto r = __builtin_amdgcn_permlane32_swap(w, w, false, false);
  const u32 r0 = r[0], r1 = r[1];
  return __builtin_bit_cast(float, r0) + __builtin_bit_cast(float, r1);
}
DI void set_m0(u32 v) { asm volatile("s_mov_b32 m0, %0\n\ts_nop 0" :: "s"(v) : "memory"); }
template <int R0, int R1, int R2, int R3, int R4, int R5>
DI void put6(u32 v0, u32 v1, u32 v2, u32 v3, u32 v4, u32 v5) {
  asm volatile("ds_write_addtid_b32 %0 offset:%c6\n\tds_write_addtid_b32 %1 offset:%c7\n\tds_write_addtid_b32 %2 offset:%c8\n\t"
               "ds_write_addtid_b32 %3 offset:%c9\n\tds_write_addtid_b32 %4 offset:%c10\n\tds_write_addtid_b32 %5 offset:%c11"
               :: "v"(v0), "v"(v1), "v"(v2), "v"(v3), "v"(v4), "v"(v5),
                  "i"(R0 * KG_ROW), "i"(R1 * KG_ROW), "i"(R2 * KG_ROW), "i"(R3 * KG_ROW), "i"(R4 * KG_ROW), "i"(R5 * KG_ROW)
               : "memory");
}
template <int K, int RB>
DI void kg_step(f32x2& fa, f32x2& fb, f32x2& e0, f32x2& e1, f32x2& e2, f32x2& e3, f32x2 z, f32x2 w, f32x2 c) {
  f32x2 t0, t1, t2, t3, t4, t5;
  u32 h0, h1, h2, h3, h4, h5;
#define KG_M(t, a, b) "v_pk_mul_f32 " t ", " a ", " b " op_sel_hi:[0,1]\n\t"
#define KG_F(a, b, t) "v_pk_fma_f32 " a ", " a ", " b ", " t " op_sel:[1,1,0] op_sel_hi:[1,0,1] neg_lo:[0,1,0]\n\t"
  asm volatile(KG_M("%6", "%0", "%12") KG_M("%7", "%1", "%13") KG_M("%8", "%2", "%14") KG_M("%9", "%3", "%14") KG_M("%10", "%4", "%14") KG_M("%11", "%5", "%14")
               KG_F("%0", "%12", "%6") KG_F("%1", "%13", "%7") KG_F("%2", "%14", "%8") KG_F("%3", "%14", "%9") KG_F("%4", "%14", "%10") KG_F("%5", "%14", "%11")
               : "+v"(fa), "+v"(fb), "+v"(e0), "+v"(e1), "+v"(e2), "+v"(e3),
                 "=&v"(t0), "=&v"(t1), "=&v"(t2), "=&v"(t3), "=&v"(t4), "=&v"(t5)
               : "v"(z), "v"(w), "v"(c));
#undef KG_M
#undef KG_F
  h0 = pack_h(fa); h1 = pack_h(fb); h2 = pack_h(e0); h3 = pack_h(e1); h4 = pack_h(e2); h5 = pack_h(e3);
  put6<K, RB, 21 + 4 * K, 22 + 4 * K, 23 + 4 * K, 24 + 4 * K>(h0, h1, h2, h3, h4, h5);
}
DI void group_barrier(u32 cnt_addr, u32 target) {
  u32 one = 1, v, sv;
  asm volatile("s_waitcnt lgkmcnt(0)\n\t"
               "s_mov_b64 exec, 1\n\t"
               "ds_add_u32 %2, %3\n\t"
               "s_mov_b64 exec, -1\n"
               "1:\n\t"
               "ds_read_b32 %0, %2\n\t"
               "s_waitcnt lgkmcnt(0)\n\t"
               "v_readfirstlane_b32 %1, %0\n\t"
               "s_cmp_lt_u32 %1, %4\n\t"
               "s_cbranch_scc1 1b"
               : "=&v"(v), "=&s"(sv) : "v"(cnt_addr), "v"(one), "s"(target) : "memory", "scc");
}

#define MFMA16(a, b, c) __builtin_amdgcn_mfma_f32_16x16x32_bf16((a), (b), (c), 0, 0, 0)
#define MFMA32(a, b, c) __builtin_amdgcn_mfma_f32_32x32x16_bf16((a), (b), (c), 0, 0, 0)

template <int TG> DI constexpr int conv_tile(int i) { return TG == 0 ? (i == 0 ? 7 : i == 1 ? 5 : i == 2 ? 2 : 0) : (i == 0 ? 6 : i == 1 ? 4 : i == 2 ? 3 : 1); }

struct ConvSet { u32x2 al[8], ah[8]; s16x8 b[8]; };
template <int RR, int P> DI void conv_rd(ConvSet& s, u32 ka, u32 ub) {
  asm volatile("ds_read_b64 %0, %3 offset:%c5\n\tds_read_b64 %1, %3 offset:%c6\n\tds_read_b128 %2, %4 offset:%c7"
               : "=&v"(s.al[P]), "=&v"(s.ah[P]), "=&v"(s.b[P])
               : "v"(ka), "v"(ub), "i"(512 * (7 - P) + 64 * (3 - RR)), "i"(512 * (7 - P) + 64 * (3 - RR) + 8), "i"(512 * P + 64 * (3 - RR)));
}
DI s16x8 conv_a(const ConvSet& s, int p) {
  return __builtin_bit_cast(s16x8, __builtin_shufflevector(s.al[p], s.ah[p], 0, 1, 2, 3));
}
template <int TG, int RR, bool PRE> DI void conv_step(const ConvSet& c, ConvSet& n, u32 ka, u32 ub, f32x4 (&acc)[4]) {
  constexpr int NP = TG == 0 ? 8 : 7;
  asm volatile("s_waitcnt lgkmcnt(0)" ::: "memory");
  SCHED0();
#define CV_MM(i, p) acc[i] = MFMA16(conv_a(c, p), c.b[conv_tile<TG>(i) - (p)], acc[i])
#define CV_RD(p) do { if (PRE && (p) < NP) conv_rd<PRE ? RR + 1 : RR, (p)>(n, ka, ub); SCHED0(); } while (0)
  CV_MM(0, 0); CV_RD(0); CV_MM(1, 0); CV_RD(1);
  CV_MM(0, 1); CV_RD(2); CV_MM(1, 1); CV_RD(3);
  CV_MM(0, 2); CV_RD(4); CV_MM(1, 2); CV_RD(5);
  CV_MM(0, 3); CV_RD(6); CV_MM(1, 3); CV_RD(7);
  CV_MM(0, 4); CV_MM(1, 4); CV_MM(0, 5); CV_MM(2, 0); CV_MM(0, 6); CV_MM(2, 1); CV_MM(3, 0); CV_MM(2, 2);
  if (TG == 0) { CV_MM(0, 7); CV_MM(1, 5); } else { CV_MM(2, 3); CV_MM(3, 1); }
  SCHED0();
#undef CV_MM
#undef CV_RD
}
template <int TG> DI void conv_body(u32 ka, u32 ub, f32x4 (&acc)[4]) {
  ConvSet s0, s1;
  conv_rd<0, 0>(s0, ka, ub); conv_rd<0, 1>(s0, ka, ub); conv_rd<0, 2>(s0, ka, ub); conv_rd<0, 3>(s0, ka, ub);
  conv_rd<0, 4>(s0, ka, ub); conv_rd<0, 5>(s0, ka, ub); conv_rd<0, 6>(s0, ka, ub);
  if (TG == 0) conv_rd<0, 7>(s0, ka, ub);
  conv_step<TG, 0, true>(s0, s1, ka, ub, acc);
  conv_step<TG, 1, true>(s1, s0, ka, ub, acc);
  conv_step<TG, 2, true>(s0, s1, ka, ub, acc);
  conv_step<TG, 3, false>(s1, s0, ka, ub, acc);
}

DI void ssm_phase(
    const u16* x, const u16* A_re, const u16* A_im, const u16* B_re, const u16* B_im, const u16* C_re,
    const u16* C_im, const u16* log_dt, const u16* Dskip, const u16* Wg, const u16* Wu, const u16* Wd, char* ws, char* lds) {
  STAMP_DECL;
  STAMP(0, 0);
  const u32 tid = threadIdx.x;
  const u32 xcd = blockIdx.x & 7, idx = blockIdx.x >> 3;
  u32x4 cpf;
  if (idx < 4) {
    const GAS char* pc; u32 n1;
    asm volatile("s_getpc_b64 %0\n.Lssm_code_pc:\n\ts_mov_b32 %1, .Lffn2_code_end-.Lssm_code_pc-16" : "=s"(pc), "=s"(n1));
    asm volatile("global_load_dwordx4 %0, %1, %2" : "=&v"(cpf) : "v"(min(idx * 8192u + tid * 16u, n1)), "s"(pc) : "memory");
  }
  SCHED0();
  const bool chore = idx >= 28;
  const u32 cp = xcd * 28 + min(idx, 27u);
  const u32 xrow = (tid >> 3) * (D * 2), xb_off = xrow + min(tid & 7, 6u) * 16 + idx * (64 * D * 2) + xcd * 112;
  const u32 lane = tid & 63, tc = tid & 255;
  const u32 wave = __builtin_amdgcn_readfirstlane(tid >> 6);
  const u32 ch = wave >> 2, wq = wave & 3;
  u32 xb_lds = lds_addr(lds) + SL_X + (tid >> 3) * XR_ROW + (tid & 7) * 16;
  u32 xg_lds = lds_addr(lds) + SL_X + (tid & 31) * 2 * XR_ROW + (tid >> 5) * 4;
  u32 xg_off = WS_XG + xcd * 28 * (L * 8) + idx * 512 + (tid >> 5) * (L * 8) + (tid & 31) * 16;
  auto publish = [&](u32 tag, u32 a0, u32 a1, u32 a2, u32 a3) {
    st128_dev(ws, xg_off, u32x4{a0, tag, a1, tag});
    st128_dev(ws, xg_off + 14 * (L * 8), u32x4{a2, tag, a3, tag});
  };
  u32 xb_o = xb_off, cp4 = cp * 4;
  asm volatile("" : "+v"(xb_o), "+v"(xb_lds), "+v"(xg_lds), "+v"(xg_off));
  asm volatile("" : "+s"(cp4));
  SCHED0();
  asm volatile("" :: "s"(x), "s"(A_re), "s"(A_im), "s"(B_re), "s"(B_im), "s"(C_re), "s"(C_im), "s"(log_dt), "s"(Dskip),
               "s"(Wg), "s"(Wu), "s"(Wd), "s"(ws));
  u32x4 xr[7];
  if (wave == 7) {
    asm volatile("s_setprio 2\n\t"
                 "global_load_dwordx4 %0, %7, %8\n\tglobal_load_dwordx4 %1, %7, %8 offset:128\n\tglobal_load_dwordx4 %2, %7, %8 offset:256\n\t"
                 "global_load_dwordx4 %3, %7, %8 offset:384\n\tglobal_load_dwordx4 %4, %7, %8 offset:512\n\tglobal_load_dwordx4 %5, %7, %8 offset:640\n\t"
                 "global_load_dwordx4 %6, %7, %8 offset:768"
                 : "=&v"(xr[0]), "=&v"(xr[1]), "=&v"(xr[2]), "=&v"(xr[3]), "=&v"(xr[4]), "=&v"(xr[5]), "=&v"(xr[6])
                 : "v"(xrow + (tid & 7) * 16 + (blockIdx.x * 8 - 56) * (D * 2)), "s"(x) : "memory");
  }
  if (blockIdx.x == 255 && wave == 7) {
#pragma unroll
    for (int i = 0; i < 3; ++i)
      asm volatile("global_store_dword %0, %1, %2 sc1" :: "v"(WS_CNT + (i * 64 + lane) * 256), "v"(0), "s"(ws) : "memory");
  }
  auto stats = [&](u32 tag) {
    asm volatile("" : "+v"(xr[0]), "+v"(xr[1]), "+v"(xr[2]), "+v"(xr[3]), "+v"(xr[4]), "+v"(xr[5]), "+v"(xr[6]));
    float s[4] = {0.f, 0.f, 0.f, 0.f}, q[4] = {0.f, 0.f, 0.f, 0.f};
#pragma unroll
    for (int j = 0; j < 7; ++j)
#pragma unroll
      for (int i = 0; i < 4; ++i) { s[i] = dot_bf(xr[j][i], 0x3f803f80u, s[i]); q[i] = dot_bf(xr[j][i], xr[j][i], q[i]); }
    float st = (s[0] + s[1]) + (s[2] + s[3]), qt = (q[0] + q[1]) + (q[2] + q[3]);
    oct_sum2(st, qt);
    const float mean = st * (1.0f / D);
    const u32x2 gr = u32x2{finish_stats(mean, qt * (1.0f / D) - mean * mean), tag};
    asm volatile("s_mov_b64 exec, %3\n\tglobal_store_dwordx2 %0, %1, %2 sc1\n\ts_mov_b64 exec, -1\n\ts_setprio 0"
                 :: "v"(WS_SG + blockIdx.x * 64 + lane), "v"(gr), "s"(ws), "s"(0x0101010101010101ull) : "memory");
  };
  u32x4 xb, ga0, ga1, ga2;
  u32 epoch, pa, pi_, pbr, pbi, pcr, pci, sl, sd;
  asm volatile("global_load_dwordx4 %0, %4, %5\n\t"
               "s_load_dword %1, %6, %c7\n\ts_load_dword %2, %8, %10\n\ts_load_dword %3, %9, %10"
               : "=&v"(xb), "=&s"(epoch), "=&s"(sl), "=&s"(sd)
               : "v"(xb_o), "s"(x), "s"(ws), "i"(WS_EPOCH), "s"(log_dt), "s"(Dskip), "s"(cp4) : "memory");
  SCHED0();
  const bool tiler = wave < 4 && !chore;
  const u32 wslice = 4 * (xcd >> 1) + (idx & 3), wchunk = (xcd & 1) * 7 + (idx >> 2);
  u32 gu0 = 0, gu1 = 0, ga_off = tc * 16;
  if (tiler) {
    gu0 = lane * 16 + ((lane * 37) >> 9) * (F * 2 - 224) + (wchunk * 32 + wq * 8) * (F * 2) + wslice * 224;
    gu1 = gu0 + 4 * (F * 2) - 1792;
    asm volatile("" : "+v"(gu0), "+v"(gu1));
    __builtin_amdgcn_s_setprio(1);
  }
  const char* gtab = (const char*)gather_tab.a + 4096;
  const char* gtab2 = gtab + 4096;
  asm volatile("" : "+s"(gtab), "+s"(gtab2));
  asm volatile("" : "+v"(ga_off));
  if (tid == 0) *(u32x4*)(lds + SL_T + 768 * 4) = u32x4{0u, 0u, 0u, 0u};
  if (wq == 3) *(u32x2*)(lds + SL_U + ch * U_CH + lane * 8) = u32x2{0u, 0u};
  SCHED0();
  asm volatile("global_load_ushort %0, %6, %7\n\tglobal_load_ushort %1, %6, %8\n\tglobal_load_ushort %2, %6, %9\n\t"
               "global_load_ushort %3, %6, %10\n\tglobal_load_ushort %4, %6, %11\n\tglobal_load_ushort %5, %6, %12"
               : "=&v"(pa), "=&v"(pi_), "=&v"(pbr), "=&v"(pbi), "=&v"(pcr), "=&v"(pci)
               : "v"(cp * 1024 + tid * 2), "s"(A_re), "s"(A_im), "s"(B_re), "s"(B_im), "s"(C_re), "s"(C_im) : "memory");
  asm volatile("global_load_dwordx4 %0, %3, %4 offset:-4096\n\tglobal_load_dwordx4 %1, %3, %4\n\tglobal_load_dwordx4 %2, %3, %5"
               : "=&v"(ga0), "=&v"(ga1), "=&v"(ga2) : "v"(ga_off), "s"(gtab), "s"(gtab2) : "memory");

  asm volatile("s_waitcnt vmcnt(9) lgkmcnt(0)" : "+v"(xb), "+v"(cpf) :: "memory");
  asm volatile("" : "+s"(epoch), "+s"(sl), "+s"(sd));
  const u32 pl = ch ? sl >> 16 : sl, pd = ch ? sd >> 16 : sd;
  *(LDS3 u32x4*)(uintptr_t)xb_lds = xb;
  const u32 tag = next_tag(epoch);
  asm volatile("s_waitcnt lgkmcnt(0)" ::: "memory");
  __builtin_amdgcn_s_barrier();
  asm volatile("" ::: "memory");
  {
    const LDS3 u32* xr_l = (const LDS3 u32*)(uintptr_t)xg_lds;
    const u32 a0 = xr_l[0], a1 = xr_l[XR_ROW / 4], a2 = xr_l[14], a3 = xr_l[XR_ROW / 4 + 14];
    asm volatile("s_waitcnt vmcnt(0)" : "+v"(pa), "+v"(pi_), "+v"(pbr), "+v"(pbi), "+v"(pcr), "+v"(pci), "+v"(ga0), "+v"(ga1), "+v"(ga2) :: "memory");
    if (tiler) {
      const u32 lw = lds_addr(lds) + SL_W + wq * 3584;
      asm volatile("s_mov_b32 m0, %0\n\ts_mov_b64 exec, %6\n\t"
                   "global_load_lds_dwordx4 %2, %4\n\tglobal_load_lds_dwordx4 %3, %4 offset:1792\n\t"
                   "s_mov_b32 m0, %1\n\ts_nop 0\n\t"
                   "global_load_lds_dwordx4 %2, %5\n\tglobal_load_lds_dwordx4 %3, %5 offset:1792\n\t"
                   "s_mov_b64 exec, -1"
                   :: "s"(lw), "s"(lw + 896), "v"(gu0), "v"(gu1), "s"(Wg), "s"(Wu), "s"(0x00ffffffffffffffull)
                   : "memory");
    }
    if (wave == 7) stats(tag); else publish(tag, a0, a1, a2, a3);
    if (chore) {
      if (wave == 4) {
        const GAS u32* g = (const GAS u32*)(ws + WS_SG + 255 * 64 + 4);
        for (u32 spins = 0; spins < (1u << 16); ++spins) {
          if (__builtin_amdgcn_readfirstlane(__hip_atomic_load(g, __ATOMIC_RELAXED, __HIP_MEMORY_SCOPE_AGENT)) == tag) break;
          __builtin_amdgcn_s_sleep(16);
        }
#pragma unroll 1
        for (int i = 0; i < 10; ++i) __builtin_amdgcn_s_sleep(8);
      }
      STAMP(0, 9);
      return;
    }
  }
  STAMP(0, 1);
  s16x8 tf[2][2];
  float tcs[2];
  auto tile_read = [&]() {
    const u32 hh = lane >> 5, blk = (lane >> 4) & 1, q = (lane & 15) >> 2, p = lane & 3;
    const u32 a = lds_addr(lds) + SL_W + wq * 64 + hh * 3584 + q * 224 + blk * 896 + 8 * p;
#pragma unroll
    for (int i = 0; i < 2; ++i) {
      tf[i][0] = tr2(a + 32 * i, 0, 1792); tf[i][1] = tr2(a + 32 * i, 4 * 1792, 5 * 1792);
    }
#pragma unroll
    for (int i = 0; i < 2; ++i) tcs[i] = half_sum(dot_ones(tf[i][1], dot_ones(tf[i][0], 0.f)));
  };
  auto tile_store = [&]() {
    const u32 un = wslice * 14 + wchunk;
    const u32 wt = WS_WT + wslice * (196 * 1024) + (wq < 2 ? wchunk * 8 + 2 * wq : 112 + wchunk * 6 + 2 * wq - 4) * 1024 + lane * 16;
    const u32 ks = wq < 2 ? 4096 : 3072;
    const u32 cs = WS_CS + un * 896 + wq * 256 + lane * 4;
#pragma unroll
    for (int i = 0; i < 2; ++i) {
      if (i == 0 || wq < 3) {
        st128_dev(ws, wt + 1024 * i, __builtin_bit_cast(u32x4, tf[i][0]));
        st128_dev(ws, wt + 1024 * i + ks, __builtin_bit_cast(u32x4, tf[i][1]));
        asm volatile("s_mov_b64 exec, %3\n\tglobal_store_dword %0, %1, %2 sc1\n\ts_mov_b64 exec, -1"
                     :: "v"(cs + 128 * i), "v"(tcs[i]), "s"(ws), "s"(0xffffffffull) : "memory");
      }
    }
  };
  const __amdgpu_buffer_rsrc_t rs = __builtin_amdgcn_make_buffer_rsrc((void*)ws, 0, (int)WS_END, 0x00020000);
  const u32 cnt = lds_addr(lds) + SL_CNT + ch * 4;
  f32x2 cb;
  float inv_sc, p2, rv;
  {
    const float dt = rbf(__builtin_amdgcn_exp2f(bf_lo(pl) * 1.4426950408889634f));
    p2 = (dt * bf_lo(pa)) * 1.4426950408889634f;
    rv = (dt * bf_lo(pi_)) * 0.15915494309189535f;
    cb = cmul(f32x2{bf_lo(pcr), bf_lo(pci)}, f32x2{bf_lo(pbr), bf_lo(pbi)} * dt);
    const float cbx = cb.x, cby = cb.y;
    u32 eb = (u32)wave_imax(max(__builtin_bit_cast(int, cbx) & 0x7fffffff, __builtin_bit_cast(int, cby) & 0x7fffffff)) >> 23;
    asm("s_max_u32 %0, %0, 27\n\ts_min_u32 %0, %0, 227" : "+s"(eb) :: "scc");
    inv_sc = __builtin_bit_cast(float, (eb - 9) << 23);
    cb *= __builtin_bit_cast(float, (263 - eb) << 23);
  }
  STAMP(0, 2);

  {
    set_m0(lds_addr(lds) + ch * KG_CH + wq * 256);
    auto cpow = [&](float mp, float mr) {
      const float mag = __builtin_amdgcn_exp2f(p2 * mp);
      const float t = __builtin_amdgcn_fractf(rv * mr);
      return f32x2{__builtin_amdgcn_cosf(t), __builtin_amdgcn_sinf(t)} * mag;
    };
    const f32x2 z1 = cpow(1.f, 1.f), c8 = cpow(8.f, -8.f), w256 = cpow(256.f, 256.f);
    const f32x2 z2 = cmul(z1, z1), c16 = cmul(c8, c8);
    const f32x2 z4 = cmul(z2, z2), c32 = cmul(c16, c16);
    f32x2 e0 = f32x2{1.f, 0.f}, e1 = c8, e2 = c16, e3 = cmul(c16, c8);
    f32x2 fa = cb, fb = cmul(cb, w256);
    put6<0, 8, 21, 22, 23, 24>(pack_h(fa), pack_h(fb), 0x3c00u, pack_h(e1), pack_h(e2), pack_h(e3));
    const f32x2 g1 = cmul(fb, z2), g2 = cmul(fb, z4);
    kg_step<1, 12>(fa, fb, e0, e1, e2, e3, z1, w256, c32);
    const f32x2 g3 = cmul(g1, z4), g4 = cmul(fb, z4);
    kg_step<2, 14>(fa, fb, e0, e1, e2, e3, z1, w256, c32);
    const u32 h772 = pack_h(cmul(fb, z4));
    put6<9, 10, 11, 13, 15, 15>(pack_h(g1), pack_h(g2), pack_h(g3), pack_h(g4), h772, h772);
    kg_step<3, 16>(fa, fb, e0, e1, e2, e3, z1, w256, c32);
    kg_step<4, 17>(fa, fb, e0, e1, e2, e3, z1, w256, c32);
    kg_step<5, 18>(fa, fb, e0, e1, e2, e3, z1, w256, c32);
    kg_step<6, 19>(fa, fb, e0, e1, e2, e3, z1, w256, c32);
    kg_step<7, 20>(fa, fb, e0, e1, e2, e3, z1, w256, c32);
  }
  STAMP(0, 3);
  {
    char* kg = lds + ch * KG_CH + wq * 256;
    const char* fa = kg + (lane & 31) * KG_ROW + (lane >> 5) * 16;
    h16x8 a[8], b[8];
    asm volatile("s_waitcnt lgkmcnt(0)" ::: "memory");
#pragma unroll
    for (int s = 0; s < 8; ++s) { a[s] = *(const h16x8*)(fa + 32 * s); b[s] = *(const h16x8*)(fa + 21 * KG_ROW + 32 * s); }
    SCHED0();
    f32x16 acc;
#pragma unroll
    for (int i = 0; i < 16; ++i) acc[i] = 0.f;
#pragma unroll
    for (int s = 0; s < 8; ++s) acc = __builtin_amdgcn_mfma_f32_32x32x16_f16(a[s], b[s], acc, 0, 0, 0);
    char* pt = kg + (lane >> 4) * KG_ROW + (lane & 15) * 16;
#pragma unroll
    for (int q = 0; q < 3; ++q)
      *(f32x4*)(pt + 4 * q * KG_ROW) = f32x4{acc[4 * q], acc[4 * q + 1], acc[4 * q + 2], acc[4 * q + 3]} * inv_sc;
  }
  const u32 xo = WS_XG + cp * (L * 8);
  u32x4 gx[4], gs[4];
  auto poll = [&]() {
#pragma unroll
    for (int i = 0; i < 4; ++i) {
      gx[i] = __builtin_amdgcn_raw_buffer_load_b128(rs, tc * 16, xo + 4096 * i, 16);
      gs[i] = __builtin_amdgcn_raw_buffer_load_b128(rs, tc * 16, WS_SG + 4096 * i, 16);
    }
  };
  STAMP(0, 4);
  group_barrier(cnt, 4);
  if (wq < 3) {
    const f32x4* pt = (const f32x4*)(lds + ch * KG_CH + 4 * wq * KG_ROW + (lane >> 4) * KG_ROW + (lane & 15) * 16);
    const f32x4 s = (pt[0] + pt[16]) + (pt[32] + pt[48]);
    const u32 p01 = pack_bf(s[0], s[1]), p23 = pack_bf(s[2], s[3]);
    u16* T = (u16*)(lds + SL_T + wq * 1024 + ch * 2 + (lane >> 5) * 512 + (lane & 31) * 4);
    T[0] = (u16)p01; T[64] = (u16)(p01 >> 16); T[128] = (u16)p23; T[192] = (u16)(p23 >> 16);
  }
  group_barrier(cnt, 8);
  poll();
  const u32 sel = ch ? 0x07060302u : 0x05040100u;
  {
    u32 e[11];
#pragma unroll
    for (int j = 0; j < 11; ++j) {
      const u32 a = j < 4 ? ga0[j & 3] : j < 8 ? ga1[j & 3] : ga2[j & 3];
      e[j] = *(const LDS3 u32*)(uintptr_t)(a + lds_addr(lds));
    }
    char* kc = lds + ch * KC_CH + tc * 16;
#pragma unroll
    for (int s = 0; s < 4; ++s)
      *(u32x4*)(kc + s * KC_STRIDE) = u32x4{__builtin_amdgcn_perm(e[s + 1], e[s], sel), __builtin_amdgcn_perm(e[s + 3], e[s + 2], sel),
                                            __builtin_amdgcn_perm(e[s + 5], e[s + 4], sel), __builtin_amdgcn_perm(e[s + 7], e[s + 6], sel)};
    if (wq == 0 && lane < 8) *(u32x4*)(lds + ch * KC_CH + (lane >> 1) * KC_STRIDE + (256 + (lane & 1)) * 16) = u32x4{0u, 0u, 0u, 0u};
  }
  STAMP(0, 5);
  u32 cv_ka, cv_ub, ep_lo;
  {
    const u32 b = lane & 15, g = lane >> 4, r3 = 8 * (wq >> 1) + 6;
    const u32 gg = 16 * (g & 1) - 32 * (g >> 1);
    cv_ka = lds_addr(lds) + ch * KC_CH + 3 * KC_STRIDE + (2044 - 16 * r3 - 256 * 7) * 2 - (b & 3) * KC_STRIDE - 8 * (b >> 2) + gg;
    cv_ub = lds_addr(lds) + SL_U + ch * U_CH + 512 - 32 * r3 + 32 * b + gg;
    ep_lo = 32 * b + 8 * g;
    asm volatile("" : "+v"(cv_ka), "+v"(cv_ub), "+v"(ep_lo));
  }
  {
    bool clean = true;
    for (u32 spins = 0;; ++spins) {
      u32 bad = 0;
#pragma unroll
      for (int i = 0; i < 4; ++i) bad |= ((gx[i][1] ^ tag) | (gx[i][3] ^ tag)) | ((gs[i][1] ^ tag) | (gs[i][3] ^ tag));
      if (__all(bad == 0u)) break;
      if (spins >= 4096u) { clean = false; break; }
      __builtin_amdgcn_s_sleep(4);
      asm volatile("" ::: "memory");
      poll();
    }
    u32 xs[8], st[8];
#pragma unroll
    for (int i = 0; i < 8; ++i) { xs[i] = gx[i >> 1][2 * (i & 1)]; st[i] = gs[i >> 1][2 * (i & 1)]; }
    if (!clean) {
      *(u32*)(lds + SL_FLAG) = 1u;
#pragma unroll 1
      for (int i = 0; i < 8; ++i) {
        const u32* xrow = (const u32*)(x + (512 * (i >> 1) + 2 * tc + (i & 1)) * D);
        float s = 0.f, q = 0.f;
#pragma unroll 1
        for (int j = 0; j < D / 2; ++j) { const u32 w = xrow[j]; s = dot_bf(w, 0x3f803f80u, s); q = dot_bf(w, w, q); }
        const float mean = s * (1.0f / D);
#pragma unroll
        for (int k = 0; k < 7; ++k) { st[k] = st[k + 1]; xs[k] = xs[k + 1]; }
        st[7] = finish_stats(mean, q * (1.0f / D) - mean * mean);
        xs[7] = xrow[cp];
      }
    }
    u32x4 xw, uw;
#pragma unroll
    for (int i = 0; i < 4; ++i) {
      xw[i] = __builtin_amdgcn_perm(xs[2 * i + 1], xs[2 * i], sel);
      const f32x2 u2 = (f32x2{bf_lo(xw[i]), bf_hi(xw[i])} - f32x2{bf_lo(st[2 * i]), bf_lo(st[2 * i + 1])}) *
                       f32x2{__builtin_amdgcn_rcpf(bf_hi(st[2 * i])), __builtin_amdgcn_rcpf(bf_hi(st[2 * i + 1]))};
      uw[i] = pack_bf(u2.x, u2.y);
    }
    u32* up = (u32*)(lds + SL_U + ch * U_CH + 512) + tc;
    u32* xp = (u32*)(lds + SL_XX + ch * L * 2) + tc;
#pragma unroll
    for (int i = 0; i < 4; ++i) { up[256 * i] = uw[i]; xp[256 * i] = xw[i]; }
  }
  STAMP(0, 6);
  group_barrier(cnt, 12);
  if (tiler) { tile_read(); tile_store(); }
  if (blockIdx.x == 0 && tid == 0 && *(const u32*)(lds + SL_FLAG) == 0u)
    __hip_atomic_store((GAS u32*)(ws + WS_EPOCH), tag, __ATOMIC_RELAXED, __HIP_MEMORY_SCOPE_AGENT);
  if (wave < 7)
    asm volatile("s_mov_b32 m0, %0\n\ts_nop 0\n\tglobal_load_lds_dword %1, %2"
                 :: "s"(lds_addr(lds) + SL_DUMP), "v"(idx * 57344 + tid * 128), "s"(Wd) : "memory");
  STAMP(0, 7);
  const u32 tg = wq & 1, rh = wq >> 1;
  f32x4 acc[4];
#pragma unroll
  for (int i = 0; i < 4; ++i) acc[i] = f32x4{0.f, 0.f, 0.f, 0.f};
  if (tg == 0) conv_body<0>(cv_ka, cv_ub, acc);
  else conv_body<1>(cv_ka, cv_ub, acc);
  STAMP(0, 8);
  {
    f32x4* red = (f32x4*)(lds + ch * KG_CH + SL_RED) + lane;
    f32x4 y[2];
    if (rh) { red[wq * 128] = acc[0]; red[wq * 128 + 64] = acc[3]; y[0] = acc[1]; y[1] = acc[2]; }
    else { red[wq * 128] = acc[1]; red[wq * 128 + 64] = acc[2]; y[0] = acc[0]; y[1] = acc[3]; }
    const int t0 = rh ? 5 - tg : 7 - tg, t1 = rh ? 2 + tg : tg;
    const char* up = lds + SL_U + ch * U_CH + 512 + ep_lo;
    const char* xp = lds + SL_XX + ch * L * 2 + ep_lo;
    const u32x2 uu0 = *(const u32x2*)(up + 512 * t0), uu1 = *(const u32x2*)(up + 512 * t1);
    const u32x2 xx0 = *(const u32x2*)(xp + 512 * t0), xx1 = *(const u32x2*)(xp + 512 * t1);
    const float dsk = bf_lo(pd);
    f32x2 b0[2], b1[2];
#pragma unroll
    for (int j = 0; j < 2; ++j) {
      b0[j] = __builtin_elementwise_fma(f32x2{bf_lo(uu0[j]), bf_hi(uu0[j])}, f32x2{dsk, dsk}, f32x2{bf_lo(xx0[j]), bf_hi(xx0[j])}) + f32x2{y[0][2 * j], y[0][2 * j + 1]};
      b1[j] = __builtin_elementwise_fma(f32x2{bf_lo(uu1[j]), bf_hi(uu1[j])}, f32x2{dsk, dsk}, f32x2{bf_lo(xx1[j]), bf_hi(xx1[j])}) + f32x2{y[1][2 * j], y[1][2 * j + 1]};
    }
    asm volatile("" : "+v"(b0[0]), "+v"(b0[1]), "+v"(b1[0]), "+v"(b1[1]));
    group_barrier(cnt, 16);
    const f32x4 o0 = red[(wq ^ 2) * 128], o1 = red[(wq ^ 2) * 128 + 64];
    const auto e0 = __builtin_amdgcn_permlane16_swap(pack_bf(b0[0].x + o0[0], b0[0].y + o0[1]), pack_bf(b1[0].x + o1[0], b1[0].y + o1[1]), false, false);
    const auto e1 = __builtin_amdgcn_permlane16_swap(pack_bf(b0[1].x + o0[2], b0[1].y + o0[3]), pack_bf(b1[1].x + o1[2], b1[1].y + o1[3]), false, false);
    const u32 lo16 = ep_lo & ~8u;
    st128_dev(ws, WS_HT + (2 * cp + ch) * 256 + (lo16 & 255) + (lo16 >> 8) * (D * 256) + 2 * D * 256 * ((lane & 16) ? t1 : t0), u32x4{e0[0], e1[0], e0[1], e1[1]});
  }
  STAMP(0, 9);
}

constexpr int F1_STAGE = 16384;
constexpr int F1_P2 = 4 * F1_STAGE;
constexpr int F1_CS = F1_P2 + 84 * 1024;
constexpr int F1_ARR = F1_CS + 7 * 256;
constexpr int F2_PS = F1_ARR + 32;
constexpr int F2_RES = F2_PS + 2048;
constexpr int F1_TOTAL = F2_RES + 4 * 2048;
static_assert(F1_TOTAL <= 163840, "lds map");
constexpr int F2_WIN = 8;
constexpr int PUB1 = 16;
constexpr int LOOK2 = 3;

DI void f1_publish(char* ws, u32 arr_addr, u32 last, int cnt) {
  u32 old, one = 1;
  asm volatile("s_mov_b64 exec, 1\n\tds_add_rtn_u32 %0, %1, %2\n\ts_mov_b64 exec, -1\n\ts_waitcnt lgkmcnt(0)" : "=&v"(old) : "v"(arr_addr), "v"(one) : "memory");
  if (__builtin_amdgcn_readfirstlane(old) == last && (threadIdx.x & 63) == 0)
    __hip_atomic_fetch_add((GAS u32*)(ws + WS_CNT + cnt * 256), 1u, __ATOMIC_RELAXED, __HIP_MEMORY_SCOPE_AGENT);
}
DI constexpr int f1_slot(int c) { return c <= 8 ? c : c < 13 ? c - 9 : 7; }
DI constexpr int f2_ks(int i) { return i < 16 ? (i >> 2) * 7 + (i & 3) : ((i - 16) / 3) * 7 + 4 + (i - 16) % 3; }
#define BARRIER() do { __builtin_amdgcn_s_barrier(); asm volatile("" ::: "memory"); } while (0)

__global__ __launch_bounds__(512, 2) void k_all(
    const u16* x, const u16* A_re, const u16* A_im, const u16* B_re, const u16* B_im, const u16* C_re,
    const u16* C_im, const u16* log_dt, const u16* Dskip, const u16* Wg, const u16* Wu, const u16* Wd_, char* ws, u16* out) {
  __shared__ __attribute__((aligned(16))) char lds[F1_TOTAL > SL_TOTAL ? F1_TOTAL : SL_TOTAL];
  ssm_phase(x, A_re, A_im, B_re, B_im, C_re, C_im, log_dt, Dskip, Wg, Wu, Wd_, ws, lds);
  const char* Wd = (const char*)Wd_;
  wait_vm<0>();
  __syncthreads();
  if (threadIdx.x < 8 && blockIdx.x < 224)
    __hip_atomic_fetch_add((GAS u32*)(ws + WS_CNTA + (threadIdx.x * 8 + (blockIdx.x & 7)) * 256), 1u, __ATOMIC_RELAXED, __HIP_MEMORY_SCOPE_AGENT);
  STAMP_DECL;
  STAMP(1, 0);
  const int tid = threadIdx.x, lane = tid & 63;
  const int wave = __builtin_amdgcn_readfirstlane(tid >> 6);
  const int xcd = blockIdx.x & 7, bi = blockIdx.x >> 3;
  const int panel = (xcd & 1) * 8 + (bi & 7), slice = (xcd >> 1) * 4 + (bi >> 3);
  const u32 vo = lane * 16;

  if (wave >= 4) {
    const int lw = wave - 4;
    const char* hb = ws + WS_HT + panel * (D * 256) + lw * 2048;
    const char* wb = ws + WS_WT + slice * (196 * 1024);
    const u32 ho = (lane >> 4) * 256 + (((lane & 15) ^ ((lane >> 4) << 2)) * 16);
    const u32 lbase = lds_addr(lds) + lw * 2048;
    auto stage = [&](int c) {
      asm volatile("s_mov_b32 m0, %0\n\ts_nop 0\n\t"
                   "global_load_lds_dwordx4 %2, %4 sc1\n\tglobal_load_lds_dwordx4 %2, %4 offset:1024 sc1\n\t"
                   "s_mov_b32 m0, %1\n\ts_nop 0\n\t"
                   "global_load_lds_dwordx4 %3, %5 sc1\n\tglobal_load_lds_dwordx4 %3, %5 offset:1024 sc1"
                   :: "s"(lbase + f1_slot(c) * F1_STAGE), "s"(lbase + f1_slot(c) * F1_STAGE + 8192),
                      "v"(ho), "v"(vo), "s"(hb + c * 8192), "s"(wb + lw * 2048 + c * 8192)
                   : "memory");
    };
    if (lw == 0) {
      const GAS u32* ca = (const GAS u32*)(ws + WS_CNTA + (xcd * 8 + (lane & 7)) * 256);
      for (u32 spins = 0; spins < (1u << 16); ++spins) {
        if (__all(__hip_atomic_load(ca, __ATOMIC_RELAXED, __HIP_MEMORY_SCOPE_AGENT) >= 28u)) break;
        __builtin_amdgcn_s_sleep(SEAM_SLEEP);
      }
    }
    BARRIER();
    STAMP(1, 1);
    stage(0);
    SCHED0();
    auto p2 = [&](int i) {
      asm volatile("s_mov_b32 m0, %0\n\ts_nop 0\n\tglobal_load_lds_dwordx4 %1, %2 sc1"
                   :: "s"(lds_addr(lds) + F1_P2 + (i * 4 + lw) * 1024), "v"(vo), "s"(wb + (112 + i * 4 + lw) * 1024) : "memory");
    };
    f32x2 psum = {0.f, 0.f};
    stage(1); stage(2);
    f32x4 v[4];
    asm volatile("global_load_dwordx4 %0, %4, %6 sc1\n\tglobal_load_dwordx4 %1, %4, %6 offset:3584 sc1\n\t"
                 "global_load_dwordx4 %2, %4, %7 sc1\n\tglobal_load_dwordx4 %3, %5, %6 sc1"
                 : "=&v"(v[0]), "=&v"(v[1]), "=&v"(v[2]), "=&v"(v[3])
                 : "v"(lw * 896 + min(lane, 55) * 16), "v"((lw < 2 ? lw + 12 : lw) * 896 + min(lane, 55) * 16),
                   "s"(ws + WS_CS + slice * (14 * 896)), "s"(ws + WS_CS + slice * (14 * 896) + 7168) : "memory");
    wait_vm<2 * 4 + 4>();
    BARRIER();
    auto csum_a = [&]() {
      asm volatile("" : "+v"(v[0]), "+v"(v[1]), "+v"(v[2]), "+v"(v[3]));
      f32x4 t = (v[0] + v[1]) + v[2];
      if (lw < 2) t += v[3];
      *(f32x4*)(lds + F2_RES + lw * 1024 + lane * 16) = t;
    };
    auto csum_b = [&]() {
      const int i = tid - 256;
      const float* q = (const float*)(lds + F2_RES) + i;
      const float csum = (q[0] + q[256]) + (q[512] + q[768]);
      u32* ca = (u32*)(lds + F1_CS) + (i >> 5) * 64 + (i & 31);
      if (i < 224) { ca[0] = pack_bf(csum, csum - rbf(csum)); ca[32] = 0u; }
    };
    auto part = [&](int c0, int c1) {
      if (slice >= c0 && slice <= c1) {
        const int q = (lane & 15) >> 2, sl = c0 == 8 ? (slice == 13 ? 7 : slice == 8 ? 8 : slice - 9) : slice;
        const u32 a = lds_addr(lds) + sl * F1_STAGE + (8 * (lane >> 5) + q) * 256 + ((lw ^ q) * 64) + ((lane >> 4) & 1) * 32 + 8 * (lane & 3);
        const u32x4 n0 = __builtin_bit_cast(u32x4, tr2(a, 0, 1024)), n1 = __builtin_bit_cast(u32x4, tr2(a, 4096, 5120));
        float s0 = 0.f, s1 = 0.f, q0 = 0.f, q1 = 0.f;
#pragma unroll
        for (int i = 0; i < 8; ++i) {
          const u32 w = i < 4 ? n0[i & 3] : n1[i & 3];
          const float lo = bf_lo(w), hi = bf_hi(w);
          s0 += lo; q0 = __builtin_fmaf(lo, lo, q0);
          s1 += hi; q1 = __builtin_fmaf(hi, hi, q1);
          asm("" : "+v"(s0)); asm("" : "+v"(q0)); asm("" : "+v"(s1)); asm("" : "+v"(q1));
        }
        psum = f32x2{s0 + s1, q0 + q1};
        asm volatile("" : "+v"(psum));
      }
    };
#define CLOSE(n) do { asm volatile("s_waitcnt vmcnt(" #n ") lgkmcnt(0)" ::: "memory"); BARRIER(); } while (0)
    stage(3); stage(4); CLOSE(16);
    stage(5); stage(6); CLOSE(12);
    part(0, 3); stage(7); stage(8); csum_a(); CLOSE(12);
    stage(9); stage(10); csum_b(); CLOSE(12);
    part(4, 7); stage(11); stage(12); CLOSE(12);
    stage(13); p2(0); p2(1); p2(2); p2(3); CLOSE(12);
    p2(4); p2(5); p2(6); p2(7); p2(8); p2(9); p2(10); p2(11); CLOSE(12);
    part(8, 13); CLOSE(0);
#undef CLOSE
    const __amdgpu_buffer_rsrc_t rs = __builtin_amdgcn_make_buffer_rsrc((void*)ws, 0, (int)WS_END, 0x00020000);
    if (slice < 14) {
      const float a = half_sum(psum.x), b = half_sum(psum.y);
      if (lane < 32) __builtin_amdgcn_raw_buffer_store_b64(__builtin_bit_cast(u32x2, f32x2{a, b}), rs, lane * 8, WS_PS + ((panel * 14 + slice) * 128 + lw * 32) * 8, 16);
    }
    p2(12); p2(13); p2(14); p2(15); p2(16); p2(17); p2(18); p2(19); p2(20);
    const bool down = bi < 28;
    const int tgp = (xcd & 1) * 16 + (xcd >> 1) * 4 + (bi & 3), dp = down ? bi >> 2 : 0;
    const u32 a1 = lds_addr(lds) + lw * 16384, a2 = lds_addr(lds) + F1_P2 + lw * 21504;
    const u32 wsrc = (lane >> 3) * (D * 2) + (((lane & 7) ^ ((lane >> 2) & 4)) * 16);
    const char* wbase = Wd + lw * 448 * (D * 2) + dp * 128;
    auto wd_load = [&](u32 dst, int i0, int n) {
#pragma unroll
      for (int i = 0; i < n; ++i) {
        const char* b0 = wbase + f2_ks(i0 + i) * 16 * (D * 2);
        asm volatile("s_mov_b32 m0, %0\n\ts_nop 0\n\tglobal_load_lds_dwordx4 %1, %2\n\tglobal_load_lds_dwordx4 %1, %3 offset:1024"
                     :: "s"(dst + i * 2048), "v"(wsrc), "s"(b0), "s"(b0 + 8 * (D * 2) - 1024) : "memory");
      }
    };
    const u32 q4 = (lane & 15) >> 2;
    const u32 tq = (4 * (lane >> 5) + q4) * 128 + (q4 >> 1) * 64 + ((lane >> 4) & 1) * 32 + (lane & 3) * 8;
    const u32 t10 = a1 + tq, t11 = a1 + (tq ^ 64), t20 = a2 + tq, t21 = a2 + (tq ^ 64);
    wd_load(a1, 0, 8);
    wait_vm<16>();
    BARRIER();
    f1_publish(ws, lds_addr(lds) + F1_ARR, 7u, 64 + panel * 4 + (slice >> 2));
    if (!down) { wait_vm<0>(); STAMP(1, 9); return; }
    const u32 cnt_off = WS_CNT + ((tgp >> 1) * 4 + lw) * 256;
    s16x8 ra[8][2];
    u32 look;
#pragma unroll
    for (int k = 0; k < 8; ++k) {
      wait_vm<14>();
      ra[k][0] = tr2(t10, k * 2048, k * 2048 + 1024); ra[k][1] = tr2(t11, k * 2048, k * 2048 + 1024);
      asm volatile("" : "+v"(ra[k][0]), "+v"(ra[k][1]));
      wd_load(a1 + k * 2048, 8 + k, 1);
      if (k == 4) asm volatile("global_load_dword %0, %1, %2 sc1" : "=&v"(look) : "v"(cnt_off), "s"(ws) : "memory");
    }
    const int tok0 = tgp * 64 + (lw & 1) * 32, d0 = dp * 64 + (lw >> 1) * 32;
    const u32 aux = lds_addr(lds) + F2_RES + lw * 2048;
    {
      const char* hr = ws + WS_HT + (tok0 >> 7) * (D * 256) + d0 * 256 + (tok0 & 127) * 2;
      asm volatile("s_mov_b32 m0, %0\n\ts_nop 0\n\tglobal_load_lds_dwordx4 %1, %2 sc1\n\tglobal_load_lds_dwordx4 %1, %3 offset:1024 sc1"
                   :: "s"(aux), "v"((lane >> 2) * 256 + (lane & 3) * 16), "s"(hr), "s"(hr + 4096 - 1024) : "memory");
    }
    auto wait_cnt = [&](int set) {
      for (u32 spins = 0; spins < (1u << 16); ++spins) {
        const u32 c = __builtin_amdgcn_raw_buffer_load_b32(rs, 0, cnt_off + set * 64 * 256, 16);
        if (__builtin_amdgcn_readfirstlane(c) >= 4u) break;
        __builtin_amdgcn_s_sleep(2);
        asm volatile("" ::: "memory");
      }
      asm volatile("" ::: "memory");
    };
    STAMP(1, 4);
    asm volatile("s_waitcnt vmcnt(8)" : "+v"(look) :: "memory");
    if (__builtin_amdgcn_readfirstlane(look) < 4u) wait_cnt(0);
    STAMP(1, 5);
    const u32 sb = WS_HF + (tgp * 224 + lw * 28) * 1024;
    s16x8 rb[F2_WIN][2];
    auto ldb = [&](int i) {
      const int k = f2_ks(i);
      rb[i % F2_WIN][0] = __builtin_bit_cast(s16x8, __builtin_amdgcn_raw_buffer_load_b128(rs, vo + (k & 3) * 1024, sb + (k >> 2) * 4096, 16));
      rb[i % F2_WIN][1] = __builtin_bit_cast(s16x8, __builtin_amdgcn_raw_buffer_load_b128(rs, vo + (k & 3) * 1024, sb + (k >> 2) * 4096 + 112 * 1024, 16));
    };
#pragma unroll
    for (int k = 0; k < F2_WIN; ++k) { ldb(k); SCHED0(); }
    {
      u32 c, sv;
      asm volatile("1:\n\tds_read_b32 %0, %2\n\ts_waitcnt lgkmcnt(0)\n\tv_readfirstlane_b32 %1, %0\n\ts_cmp_lt_u32 %1, 4\n\ts_cbranch_scc0 2f\n\ts_sleep 1\n\ts_branch 1b\n2:"
                   : "=&v"(c), "=&s"(sv) : "v"(lds_addr(lds) + F1_ARR + 4) : "memory", "scc");
    }
    wd_load(a2, 16, 10);
    SCHED0();
    f32x2 ps[4];
    f32x16 acc[4];
#pragma unroll
    for (int a = 0; a < 4; ++a)
#pragma unroll
      for (int i = 0; i < 16; ++i) acc[a][i] = 0.f;
    u32x2 hres[4];
    s16x8 la[2][2];
    static_assert(F2_WIN <= 8 && F2_WIN >= 2, "LDS-DMA must be older than fragments consumed before it is read");
#pragma unroll
    for (int k = 0; k < 28; ++k) {
      const int n = k + 1;
      if (n >= 8 && n < 28) {
        const int o = (n < 16 ? n - 8 : n < 26 ? n - 16 : n - 26) * 2048;
        la[n & 1][0] = tr2(n >= 16 && n < 26 ? t20 : t10, o, o + 1024);
        la[n & 1][1] = tr2(n >= 16 && n < 26 ? t21 : t11, o, o + 1024);
      }
#pragma unroll
      for (int a = 0; a < 4; ++a) acc[a] = MFMA32(k < 8 ? ra[k < 8 ? k : 0][a >> 1] : la[k & 1][a >> 1], rb[k % F2_WIN][a & 1], acc[a]);
      SCHED0();
      if (k == 9) wd_load(a1, 26, 2);
      if (k + F2_WIN == 16) {
        STAMP(1, 7);
        asm volatile("s_waitcnt vmcnt(%c1)" : "+v"(look) : "i"(2 * LOOK2) : "memory");
        if (__builtin_amdgcn_readfirstlane(look) < 4u) wait_cnt(1);
        STAMP(1, 8);
#pragma unroll
        for (int i = 0; i < 4; ++i)
          ps[i] = __builtin_bit_cast(f32x2, __builtin_amdgcn_raw_buffer_load_b64(rs, lane * 8 + (i < 2 || lw < 3 ? i : i - 2) * 1024,
                                                                                 WS_PS + (tgp >> 1) * (14 * 1024) + (tgp & 1) * 512 + lw * 4096, 16));
        SCHED0();
      }
      if (k + F2_WIN + LOOK2 == 16) asm volatile("global_load_dword %0, %1, %2 sc1" : "=&v"(look) : "v"(cnt_off + 64 * 256), "s"(ws) : "memory");
      if (k + F2_WIN < 28) ldb(k + F2_WIN);
      if (k == 18) {
        const f32x2 t = lw < 3 ? (ps[0] + ps[1]) + (ps[2] + ps[3]) : ps[0] + ps[1];
        *(LDS3 f32x2*)(uintptr_t)(lds_addr(lds) + F2_PS + lw * 512 + lane * 8) = t;
      }
      if (k == 20) {
        const u32 a = aux + (4 * (lane >> 5) + ((lane & 15) >> 2)) * 64 + ((lane >> 4) & 1) * 32 + (lane & 3) * 8;
#pragma unroll
        for (int g = 0; g < 4; ++g) hres[g] = __builtin_bit_cast(u32x2, __builtin_amdgcn_ds_read_tr16_b64_v4i16((LDS3 s16x4*)(uintptr_t)(a + g * 512)));
      }
      SCHED0();
    }
    STAMP(1, 6);
    f32x16 mine;
#pragma unroll
    for (int a = 0; a < 4; ++a) {
      if (a == lw) { mine = acc[a]; }
      else {
        LDS3 f32x4* pp = (LDS3 f32x4*)(uintptr_t)(a1 + a * 4096 + vo);
#pragma unroll
        for (int g = 0; g < 4; ++g) pp[g * 64] = f32x4{acc[a][4 * g], acc[a][4 * g + 1], acc[a][4 * g + 2], acc[a][4 * g + 3]};
      }
    }
    __syncthreads();
    float rs2;
    {
      const LDS3 f32x2* pq = (const LDS3 f32x2*)(uintptr_t)(lds_addr(lds) + F2_PS + ((lw & 1) * 32 + (lane & 31)) * 8);
      const f32x2 t = (pq[0] + pq[64]) + (pq[128] + pq[192]);
      const float mean = t.x * (1.0f / D);
      const float var = fmaxf(t.y * (1.0f / D) - mean * mean, 0.f);
      const float r = __builtin_amdgcn_rcpf(rbf(__builtin_amdgcn_sqrtf(rbf(rbf(var) + 1e-5f))));
      rs2 = r * r;
    }
    u32 pk[8];
    const u32 rdb = lds_addr(lds) + lw * 4096 + vo;
#pragma unroll
    for (int g = 0; g < 4; ++g) {
      const f32x4 p0 = *(const LDS3 f32x4*)(uintptr_t)(rdb + ((lw + 1) & 3) * 16384 + g * 1024);
      const f32x4 p1 = *(const LDS3 f32x4*)(uintptr_t)(rdb + ((lw + 2) & 3) * 16384 + g * 1024);
      const f32x4 p2v = *(const LDS3 f32x4*)(uintptr_t)(rdb + ((lw + 3) & 3) * 16384 + g * 1024);
      const f32x4 sv = (p0 + p1) + (p2v + f32x4{mine[4 * g], mine[4 * g + 1], mine[4 * g + 2], mine[4 * g + 3]});
      const f32x2 o0 = __builtin_elementwise_fma(f32x2{sv[0], sv[1]}, f32x2{rs2, rs2}, f32x2{bf_lo(hres[g][0]), bf_hi(hres[g][0])});
      const f32x2 o1 = __builtin_elementwise_fma(f32x2{sv[2], sv[3]}, f32x2{rs2, rs2}, f32x2{bf_lo(hres[g][1]), bf_hi(hres[g][1])});
      pk[2 * g] = pack_bf(o0.x, o0.y);
      pk[2 * g + 1] = pack_bf(o1.x, o1.y);
    }
    char* ob = (char*)out + tok0 * (D * 2) + d0 * 2;
#pragma unroll
    for (int k = 0; k < 2; ++k) {
      const auto r0 = __builtin_amdgcn_permlane32_swap(pk[4 * k], pk[4 * k + 2], false, false);
      const auto r1 = __builtin_amdgcn_permlane32_swap(pk[4 * k + 1], pk[4 * k + 3], false, false);
      *(u32x4*)(ob + (u32)((lane & 31) * (D * 2) + (lane >> 5) * 16 + 32 * k)) = u32x4{r0[0], r1[0], r0[1], r1[1]};
    }
    STAMP(1, 9);
    asm volatile(".Lffn2_code_end:");
    return;
  }

  const int q = (lane & 15) >> 2;
  u32 bB[3], bA[3];
#pragma unroll
  for (int i = 0; i < 3; ++i) {
    bB[i] = lds_addr(lds) + i * 65536 + (8 * (lane >> 5) + q) * 256 + ((wave ^ q) * 64) + ((lane >> 4) & 1) * 32 + 8 * (lane & 3);
    bA[i] = lds_addr(lds) + i * 65536 + 8192 + vo;
    asm volatile("" : "+v"(bB[i]), "+v"(bA[i]));
  }
  char* dst = ws + WS_HF + ((panel * 4 + wave) * 112 + slice * 7) * 1024;
  s16x8 bk[28], fa[2][4];
  f32x16 acc[7];
  auto rd1 = [&](int k) {
    const int sl = f1_slot(k >> 1), o = (sl & 3) * F1_STAGE + (k & 1) * 4096;
    bk[k] = tr2(bB[sl >> 2], o, o + 1024);
#pragma unroll
    for (int t = 0; t < 4; ++t) fa[k & 1][t] = *(const LDS3 s16x8*)(uintptr_t)(bA[sl >> 2] + o + t * 1024);
  };
  auto rd2 = [&](int k) {
#pragma unroll
    for (int t = 0; t < 3; ++t) {
      const int kb = 56 + k * 3 + t;
      fa[k & 1][t] = *(const LDS3 s16x8*)(uintptr_t)(bA[kb >> 6] + (kb & 63) * 1024);
    }
  };
  const u32 sw = (lane & 15) == ((lane >> 4) & 1) ? 0x3f803f80u : 0u;
  const s16x8 sel = __builtin_bit_cast(s16x8, u32x4{sw, sw, sw, sw});
  f32x4 msum = {0.f, 0.f, 0.f, 0.f};
  auto put = [&](int t) {
    u32x4 o;
#pragma unroll
    for (int j = 0; j < 4; ++j) {
      float a0 = relu(acc[t][2 * j]) * acc[t][8 + 2 * j], a1 = relu(acc[t][2 * j + 1]) * acc[t][9 + 2 * j];
      asm("" : "+v"(a0)); asm("" : "+v"(a1));
      o[j] = pack_bf(a0, a1);
    }
    st128_dev(dst + (t >> 2) * 4096, vo + (t & 3) * 1024, o);
  };
  const f32x16 zero = {0.f, 0.f, 0.f, 0.f, 0.f, 0.f, 0.f, 0.f, 0.f, 0.f, 0.f, 0.f, 0.f, 0.f, 0.f, 0.f};
  if (tid == 0) *(u32x4*)(lds + F1_ARR) = u32x4{0u, 0u, 0u, 0u};
  asm volatile("s_waitcnt lgkmcnt(0)" ::: "memory");
  BARRIER();
  BARRIER();
  STAMP(1, 1);
  rd1(0);
  SCHED0();
#pragma unroll
  for (int k = 0; k < 28; ++k) {
    if (k == 1 || (k & 3) == 3) {
      asm volatile("s_waitcnt lgkmcnt(0)" ::: "memory");
      BARRIER();
      SCHED0();
    }
    if (k < 27) rd1(k + 1); else rd2(0);
#pragma unroll
    for (int t = 0; t < 4; ++t) acc[t] = MFMA32(fa[k & 1][t], bk[k], k == 0 ? zero : acc[t]);
    msum = MFMA16(sel, bk[k], msum);
    __builtin_amdgcn_sched_group_barrier(0x8, 1, 0);
    __builtin_amdgcn_sched_group_barrier(0x100, 3, 0);
    __builtin_amdgcn_sched_group_barrier(0x8, 1, 0);
    __builtin_amdgcn_sched_group_barrier(0x100, 3, 0);
    __builtin_amdgcn_sched_group_barrier(0x8, 3, 0);
    SCHED0();
  }
  STAMP(1, 2);
  {
    const u32* ca = (const u32*)(lds + F1_CS) + lane;
    u32 cw[7];
#pragma unroll
    for (int t = 0; t < 7; ++t) cw[t] = ca[t * 64];
    const float m0 = msum[0], m1 = msum[1];
    const auto mr = __builtin_amdgcn_permlane16_swap(__builtin_bit_cast(u32, m0), __builtin_bit_cast(u32, m1), false, false);
    const u32 mt = mr[0];
    const float nmean = __builtin_bit_cast(float, mt) * (-1.0f / D);
    const s16x8 mo = __builtin_bit_cast(s16x8, u32x4{pack_bf(nmean, nmean), 0u, 0u, 0u});
#pragma unroll
    for (int t = 0; t < 4; ++t) acc[t] = MFMA32(__builtin_bit_cast(s16x8, u32x4{cw[t], 0u, 0u, 0u}), mo, acc[t]);
    put(0);
    SCHED0();
#pragma unroll
    for (int t = 4; t < 7; ++t) acc[t] = MFMA32(__builtin_bit_cast(s16x8, u32x4{cw[t], 0u, 0u, 0u}), mo, zero);
    SCHED0();
  }
#pragma unroll
  for (int k = 0; k < 26; ++k) {
    if (k == 15) BARRIER();
    rd2(k + 1);
#pragma unroll
    for (int t = 0; t < 3; ++t) acc[4 + t] = MFMA32(fa[k & 1][t], bk[k], acc[4 + t]);
    if (k == 0 || k == 2 || k == 4) put(1 + k / 2);
    if (k == PUB1) {
      wait_vm<0>();
      f1_publish(ws, lds_addr(lds) + F1_ARR + 8, 3u, panel * 4 + (slice >> 2));
    }
#pragma unroll
    for (int i = 0; i < 3; ++i) {
      __builtin_amdgcn_sched_group_barrier(0x8, 1, 0);
      __builtin_amdgcn_sched_group_barrier(0x100, 1, 0);
      __builtin_amdgcn_sched_group_barrier(0x2, 7, 0);
    }
    SCHED0();
  }
  STAMP(1, 3);
  rd2(27);
  {
    u32 one = 1;
    asm volatile("s_mov_b64 exec, 1\n\tds_add_u32 %0, %1\n\ts_mov_b64 exec, -1" :: "v"(lds_addr(lds) + F1_ARR + 4), "v"(one) : "memory");
  }
  SCHED0();
  acc[4] = MFMA32(fa[0][0], bk[26], acc[4]); acc[4] = MFMA32(fa[1][0], bk[27], acc[4]);
  acc[5] = MFMA32(fa[0][1], bk[26], acc[5]); acc[5] = MFMA32(fa[1][1], bk[27], acc[5]);
  SCHED0();
  acc[6] = MFMA32(fa[0][2], bk[26], acc[6]); acc[6] = MFMA32(fa[1][2], bk[27], acc[6]);
  put(4);
  SCHED0();
  put(5); put(6);
  STAMP(1, 4);
  wait_vm<0>();
  f1_publish(ws, lds_addr(lds) + F1_ARR, 7u, 64 + panel * 4 + (slice >> 2));
  STAMP(1, 9);
}

extern "C" void kernel_launch(void* const* d_in, const int* in_sizes, int n_in,
                              void* d_out, int out_size, void* d_ws, size_t ws_size,
                              hipStream_t stream) {
  const u16* x = (const u16*)d_in[0];
  char* ws = (char*)d_ws;
  k_all<<<256, 512, 0, stream>>>(x, (const u16*)d_in[1], (const u16*)d_in[2], (const u16*)d_in[3], (const u16*)d_in[4],
                                 (const u16*)d_in[5], (const u16*)d_in[6], (const u16*)d_in[7], (const u16*)d_in[8],
                                 (const u16*)d_in[10], (const u16*)d_in[11], (const u16*)d_in[12], ws, (u16*)d_out);
}
```

```cpp
#include <hip/hip_runtime.h>
#include <hip/hip_bf16.h>
#include <stdint.h>

typedef unsigned short u16;
typedef unsigned int u32;
typedef unsigned long long u64;
typedef short s16x4 __attribute__((ext_vector_type(4)));
typedef short s16x8 __attribute__((ext_vector_type(8)));
typedef _Float16 h16x8 __attribute__((ext_vector_type(8)));
typedef _Float16 h16x2 __attribute__((ext_vector_type(2)));
typedef __bf16 bf16x2 __attribute__((ext_vector_type(2)));
typedef float f32x2 __attribute__((ext_vector_type(2)));
typedef float f32x4 __attribute__((ext_vector_type(4)));
typedef float f32x16 __attribute__((ext_vector_type(16)));
typedef u32 u32x2 __attribute__((ext_vector_type(2)));
typedef u32 u32x4 __attribute__((ext_vector_type(4)));
#define LDS3 __attribute__((address_space(3)))
#define GAS __attribute__((address_space(1)))
#define DI __device__ __forceinline__
#define SCHED0() __builtin_amdgcn_sched_barrier(0)

constexpr int L = 2048, D = 448, NM = 256, F = 1792;

constexpr u32 WS_SG = 0;
constexpr u32 WS_EPOCH = 16384;
constexpr u32 WS_CNT = 32768;
constexpr u32 WS_HT = 65536;
constexpr u32 WS_HF = WS_HT + D * L * 2;
constexpr u32 WS_XG = WS_HF + L * F * 2;
constexpr u32 WS_WT = WS_XG + (D / 2) * L * 8;
constexpr u32 WS_CS = WS_WT + 2 * D * F * 2;
constexpr u32 WS_PS = WS_CS + 16 * 14 * 896;
constexpr u32 WS_END = WS_PS + 16 * 14 * 1024;

DI float bf_lo(u32 w) { return __builtin_bit_cast(float, w << 16); }
DI float bf_hi(u32 w) { return __builtin_bit_cast(float, w & 0xffff0000u); }
DI float rbf(float a) { return (float)(__bf16)a; }
DI u32 pack_bf(float a, float b) { bf16x2 v; v.x = (__bf16)a; v.y = (__bf16)b; return __builtin_bit_cast(u32, v); }
DI u32 pack_h(f32x2 a) { h16x2 v; v.x = (_Float16)a.x; v.y = (_Float16)a.y; return __builtin_bit_cast(u32, v); }
DI u32 lds_addr(const void* p) { return (u32)(uintptr_t)(const LDS3 void*)p; }
DI float relu(float v) { return __builtin_bit_cast(float, max(__builtin_bit_cast(int, v), 0)); }
DI u32 next_tag(u32 e) { return e * 2654435761u + 0x9E3779B9u; }

DI int wave_imax(int c) {
#define RED1(ctl) "s_nop 1\n\tv_max_i32_dpp %0, %0, %0 " ctl "\n\t"
  asm(RED1("row_shr:1 row_mask:0xf bank_mask:0xf bound_ctrl:1") RED1("row_shr:2 row_mask:0xf bank_mask:0xf bound_ctrl:1")
      RED1("row_shr:4 row_mask:0xf bank_mask:0xf bound_ctrl:1") RED1("row_shr:8 row_mask:0xf bank_mask:0xf bound_ctrl:1")
      RED1("row_bcast:15 row_mask:0xa bank_mask:0xf") RED1("row_bcast:31 row_mask:0xc bank_mask:0xf")
      "s_nop 0"
      : "+v"(c));
#undef RED1
  return __builtin_amdgcn_readlane(c, 63);
}
DI void oct_sum2(float& a, float& b) {
#define RED2(ctl) "s_nop 0\n\tv_add_f32_dpp %0, %0, %0 " ctl " row_mask:0xf bank_mask:0xf\n\tv_add_f32_dpp %1, %1, %1 " ctl " row_mask:0xf bank_mask:0xf\n\t"
  asm("s_nop 0\n\t" RED2("quad_perm:[1,0,3,2]") RED2("quad_perm:[2,3,0,1]") RED2("row_half_mirror") "s_nop 0" : "+v"(a), "+v"(b));
#undef RED2
}
DI void st128_dev(const char* base, u32 off, u32x4 v) {
  asm volatile("global_store_dwordx4 %0, %1, %2 sc1\n\ts_nop 1" :: "v"(off), "v"(v), "s"(base) : "memory");
}
DI f32x2 cmul(f32x2 a, f32x2 b) {
  f32x2 t, o;
  asm("v_pk_mul_f32 %0, %1, %2 op_sel_hi:[0,1]" : "=v"(t) : "v"(a), "v"(b));
  asm("v_pk_fma_f32 %0, %1, %2, %3 op_sel:[1,1,0] op_sel_hi:[1,0,1] neg_lo:[0,1,0]" : "=v"(o) : "v"(a), "v"(b), "v"(t));
  return o;
}
DI u32 finish_stats(float mean, float var) { return pack_bf(mean, __builtin_amdgcn_sqrtf(rbf(rbf(fmaxf(var, 0.f)) + 1e-5f))); }
DI float dot_bf(u32 a, u32 b, float c) {
  return __builtin_amdgcn_fdot2_f32_bf16(__builtin_bit_cast(bf16x2, a), __builtin_bit_cast(bf16x2, b), c, false);
}

constexpr int KG_ROW = 1040;
constexpr int KG_CH = 53 * KG_ROW;
constexpr int KC_STRIDE = 4160;
constexpr int KC_CH = KG_CH;
constexpr int SL_RED = 4 * KC_STRIDE;
constexpr int SL_U = 2 * KG_CH;
constexpr int U_CH = (256 + L) * 2;
constexpr int SL_X = SL_U + 2 * U_CH;
constexpr int XR_ROW = 144;
constexpr int SL_XX = SL_X + 64 * XR_ROW;
constexpr int SL_T = SL_XX + 2 * L * 2;
constexpr int SL_FLAG = SL_T + 769 * 4;
constexpr int SL_CNT = SL_T + 770 * 4;
constexpr int SL_W = SL_T + 772 * 4;
constexpr int SL_DUMP = SL_W + 32 * 448 + 64;
constexpr int SL_TOTAL = SL_DUMP + 256;
static_assert(SL_RED + 4 * 2048 <= KG_CH && KG_CH % 16 == 0 && SL_W % 16 == 0 && SL_TOTAL <= 163840 && KG_CH + 768 < 65536, "lds map");

struct GatherTab { u32 a[256 * 12]; };
constexpr GatherTab make_gather_tab() {
  GatherTab t = {};
  for (int tc = 0; tc < 256; ++tc)
    for (int j = 0; j < 12; ++j) {
      const int i = 2047 - 8 * tc - j;
      int slot = 768;
      if (i >= 0) {
        const int s = i < 256 ? 0 : i < 512 ? 1 : i < 1024 ? 2 : 3;
        const int lag = s ? ((i + (1 << (s - 1)) - 1 + ((i >> s) & 1)) >> s) << s : i;
        const int lo = (lag & 255) >> 3, b = lag >> 8, r = lag & 7;
        const int row = b == 0 ? r : b == 1 ? 8 + (r >> 1) : b < 4 ? 12 + 2 * (b - 2) + (r >> 2) : 12 + b;
        slot = row * 32 + lo;
      }
      t.a[(j >> 2) * 1024 + tc * 4 + (j & 3)] = SL_T + 4 * slot;
    }
  return t;
}
static __device__ const GatherTab gather_tab = make_gather_tab();

DI s16x8 tr2(u32 a, int o0, int o1) {
  const s16x4 lo = __builtin_amdgcn_ds_read_tr16_b64_v4i16((LDS3 s16x4*)(uintptr_t)(a + o0));
  const s16x4 hi = __builtin_amdgcn_ds_read_tr16_b64_v4i16((LDS3 s16x4*)(uintptr_t)(a + o1));
  return __builtin_shufflevector(lo, hi, 0, 1, 2, 3, 4, 5, 6, 7);
}
template <int N> DI void wait_vm() { asm volatile("s_waitcnt vmcnt(%c0)" :: "i"(N) : "memory"); }
DI float dot_ones(s16x8 f, float s) {
  const u32x4 w4 = __builtin_bit_cast(u32x4, f);
#pragma unroll
  for (int i = 0; i < 4; ++i) s = dot_bf(w4[i], 0x3f803f80u, s);
  return s;
}
DI float half_sum(float v) {
  const u32 w = __builtin_bit_cast(u32, v);
  const auto r = __builtin_amdgcn_permlane32_swap(w, w, false, false);
  const u32 r0 = r[0], r1 = r[1];
  return __builtin_bit_cast(float, r0) + __builtin_bit_cast(float, r1);
}
DI void set_m0(u32 v) { asm volatile("s_mov_b32 m0, %0\n\ts_nop 0" :: "s"(v) : "memory"); }
template <int R0, int R1, int R2, int R3, int R4, int R5>
DI void put6(u32 v0, u32 v1, u32 v2, u32 v3, u32 v4, u32 v5) {
  asm volatile("ds_write_addtid_b32 %0 offset:%c6\n\tds_write_addtid_b32 %1 offset:%c7\n\tds_write_addtid_b32 %2 offset:%c8\n\t"
               "ds_write_addtid_b32 %3 offset:%c9\n\tds_write_addtid_b32 %4 offset:%c10\n\tds_write_addtid_b32 %5 offset:%c11"
               :: "v"(v0), "v"(v1), "v"(v2), "v"(v3), "v"(v4), "v"(v5),
                  "i"(R0 * KG_ROW), "i"(R1 * KG_ROW), "i"(R2 * KG_ROW), "i"(R3 * KG_ROW), "i"(R4 * KG_ROW), "i"(R5 * KG_ROW)
               : "memory");
}
template <int K, int RB>
DI void kg_step(f32x2& fa, f32x2& fb, f32x2& e0, f32x2& e1, f32x2& e2, f32x2& e3, f32x2 z, f32x2 w, f32x2 c) {
  f32x2 t0, t1, t2, t3, t4, t5;
  u32 h0, h1, h2, h3, h4, h5;
#define KG_M(t, a, b) "v_pk_mul_f32 " t ", " a ", " b " op_sel_hi:[0,1]\n\t"
#define KG_F(a, b, t) "v_pk_fma_f32 " a ", " a ", " b ", " t " op_sel:[1,1,0] op_sel_hi:[1,0,1] neg_lo:[0,1,0]\n\t"
  asm volatile(KG_M("%6", "%0", "%12") KG_M("%7", "%1", "%13") KG_M("%8", "%2", "%14") KG_M("%9", "%3", "%14") KG_M("%10", "%4", "%14") KG_M("%11", "%5", "%14")
               KG_F("%0", "%12", "%6") KG_F("%1", "%13", "%7") KG_F("%2", "%14", "%8") KG_F("%3", "%14", "%9") KG_F("%4", "%14", "%10") KG_F("%5", "%14", "%11")
               : "+v"(fa), "+v"(fb), "+v"(e0), "+v"(e1), "+v"(e2), "+v"(e3),
                 "=&v"(t0), "=&v"(t1), "=&v"(t2), "=&v"(t3), "=&v"(t4), "=&v"(t5)
               : "v"(z), "v"(w), "v"(c));
#undef KG_M
#undef KG_F
  h0 = pack_h(fa); h1 = pack_h(fb); h2 = pack_h(e0); h3 = pack_h(e1); h4 = pack_h(e2); h5 = pack_h(e3);
  put6<K, RB, 21 + 4 * K, 22 + 4 * K, 23 + 4 * K, 24 + 4 * K>(h0, h1, h2, h3, h4, h5);
}
DI void group_barrier(u32 cnt_addr, u32 target) {
  u32 one = 1, v, sv;
  asm volatile("s_waitcnt lgkmcnt(0)\n\t"
               "s_mov_b64 exec, 1\n\t"
               "ds_add_u32 %2, %3\n\t"
               "s_mov_b64 exec, -1\n"
               "1:\n\t"
               "ds_read_b32 %0, %2\n\t"
               "s_waitcnt lgkmcnt(0)\n\t"
               "v_readfirstlane_b32 %1, %0\n\t"
               "s_cmp_lt_u32 %1, %4\n\t"
               "s_cbranch_scc1 1b"
               : "=&v"(v), "=&s"(sv) : "v"(cnt_addr), "v"(one), "s"(target) : "memory", "scc");
}

#define MFMA16(a, b, c) __builtin_amdgcn_mfma_f32_16x16x32_bf16((a), (b), (c), 0, 0, 0)
#define MFMA32(a, b, c) __builtin_amdgcn_mfma_f32_32x32x16_bf16((a), (b), (c), 0, 0, 0)

template <int TG> DI constexpr int conv_tile(int i) { return TG == 0 ? (i == 0 ? 7 : i == 1 ? 5 : i == 2 ? 2 : 0) : (i == 0 ? 6 : i == 1 ? 4 : i == 2 ? 3 : 1); }

struct ConvSet { u32x2 al[8], ah[8]; s16x8 b[8]; };
template <int RR, int P> DI void conv_rd(ConvSet& s, u32 ka, u32 ub) {
  asm volatile("ds_read_b64 %0, %3 offset:%c5\n\tds_read_b64 %1, %3 offset:%c6\n\tds_read_b128 %2, %4 offset:%c7"
               : "=&v"(s.al[P]), "=&v"(s.ah[P]), "=&v"(s.b[P])
               : "v"(ka), "v"(ub), "i"(512 * (7 - P) + 64 * (3 - RR)), "i"(512 * (7 - P) + 64 * (3 - RR) + 8), "i"(512 * P + 64 * (3 - RR)));
}
DI s16x8 conv_a(const ConvSet& s, int p) {
  return __builtin_bit_cast(s16x8, __builtin_shufflevector(s.al[p], s.ah[p], 0, 1, 2, 3));
}
template <int TG, int RR, bool PRE> DI void conv_step(const ConvSet& c, ConvSet& n, u32 ka, u32 ub, f32x4 (&acc)[4]) {
  constexpr int NP = TG == 0 ? 8 : 7;
  asm volatile("s_waitcnt lgkmcnt(0)" ::: "memory");
  SCHED0();
#define CV_MM(i, p) acc[i] = MFMA16(conv_a(c, p), c.b[conv_tile<TG>(i) - (p)], acc[i])
#define CV_RD(p) do { if (PRE && (p) < NP) conv_rd<PRE ? RR + 1 : RR, (p)>(n, ka, ub); SCHED0(); } while (0)
  CV_MM(0, 0); CV_RD(0); CV_MM(1, 0); CV_RD(1);
  CV_MM(0, 1); CV_RD(2); CV_MM(1, 1); CV_RD(3);
  CV_MM(0, 2); CV_RD(4); CV_MM(1, 2); CV_RD(5);
  CV_MM(0, 3); CV_RD(6); CV_MM(1, 3); CV_RD(7);
  CV_MM(0, 4); CV_MM(1, 4); CV_MM(0, 5); CV_MM(2, 0); CV_MM(0, 6); CV_MM(2, 1); CV_MM(3, 0); CV_MM(2, 2);
  if (TG == 0) { CV_MM(0, 7); CV_MM(1, 5); } else { CV_MM(2, 3); CV_MM(3, 1); }
  SCHED0();
#undef CV_MM
#undef CV_RD
}
template <int TG> DI void conv_body(u32 ka, u32 ub, f32x4 (&acc)[4]) {
  ConvSet s0, s1;
  conv_rd<0, 0>(s0, ka, ub); conv_rd<0, 1>(s0, ka, ub); conv_rd<0, 2>(s0, ka, ub); conv_rd<0, 3>(s0, ka, ub);
  conv_rd<0, 4>(s0, ka, ub); conv_rd<0, 5>(s0, ka, ub); conv_rd<0, 6>(s0, ka, ub);
  if (TG == 0) conv_rd<0, 7>(s0, ka, ub);
  conv_step<TG, 0, true>(s0, s1, ka, ub, acc);
  conv_step<TG, 1, true>(s1, s0, ka, ub, acc);
  conv_step<TG, 2, true>(s0, s1, ka, ub, acc);
  conv_step<TG, 3, false>(s1, s0, ka, ub, acc);
}

__global__ __launch_bounds__(512, 2) void k_ssm(
    const u16* x, const u16* A_re, const u16* A_im, const u16* B_re, const u16* B_im, const u16* C_re,
    const u16* C_im, const u16* log_dt, const u16* Dskip, const u16* Wg, const u16* Wu, const u16* Wd, char* ws) {
  __shared__ __attribute__((aligned(16))) char lds[SL_TOTAL];
  const u32 tid = threadIdx.x;
  const u32 xcd = blockIdx.x & 7, idx = blockIdx.x >> 3;
  u32x4 cpf;
  if (idx < 4) {
    const GAS char* pc; u32 n1;
    asm volatile("s_getpc_b64 %0\n.Lssm_code_pc:\n\ts_mov_b32 %1, .Lffn2_code_end-.Lssm_code_pc-16" : "=s"(pc), "=s"(n1));
    asm volatile("global_load_dwordx4 %0, %1, %2" : "=&v"(cpf) : "v"(min(idx * 8192u + tid * 16u, (u32)max((int)n1, 0))), "s"(pc) : "memory");
  }
  SCHED0();
  const bool chore = idx >= 28;
  const u32 cp = xcd * 28 + min(idx, 27u);
  const u32 xrow = (tid >> 3) * (D * 2), xb_off = xrow + min(tid & 7, 6u) * 16 + idx * (64 * D * 2) + xcd * 112;
  const u32 lane = tid & 63, tc = tid & 255;
  const u32 wave = __builtin_amdgcn_readfirstlane(tid >> 6);
  const u32 ch = wave >> 2, wq = wave & 3;
  u32 xb_lds = lds_addr(lds) + SL_X + (tid >> 3) * XR_ROW + (tid & 7) * 16;
  u32 xg_lds = lds_addr(lds) + SL_X + (tid & 31) * 2 * XR_ROW + (tid >> 5) * 4;
  u32 xg_off = WS_XG + xcd * 28 * (L * 8) + idx * 512 + (tid >> 5) * (L * 8) + (tid & 31) * 16;
  auto publish = [&](u32 tag, u32 a0, u32 a1, u32 a2, u32 a3) {
    st128_dev(ws, xg_off, u32x4{a0, tag, a1, tag});
    st128_dev(ws, xg_off + 14 * (L * 8), u32x4{a2, tag, a3, tag});
  };
  u32 xb_o = xb_off, cp4 = cp * 4;
  asm volatile("" : "+v"(xb_o), "+v"(xb_lds), "+v"(xg_lds), "+v"(xg_off));
  asm volatile("" : "+s"(cp4));
  SCHED0();
  asm volatile("" :: "s"(x), "s"(A_re), "s"(A_im), "s"(B_re), "s"(B_im), "s"(C_re), "s"(C_im), "s"(log_dt), "s"(Dskip),
               "s"(Wg), "s"(Wu), "s"(Wd), "s"(ws));
  u32x4 xr[7];
  if (wave == 7) {
    asm volatile("s_setprio 2\n\t"
                 "global_load_dwordx4 %0, %7, %8\n\tglobal_load_dwordx4 %1, %7, %8 offset:128\n\tglobal_load_dwordx4 %2, %7, %8 offset:256\n\t"
                 "global_load_dwordx4 %3, %7, %8 offset:384\n\tglobal_load_dwordx4 %4, %7, %8 offset:512\n\tglobal_load_dwordx4 %5, %7, %8 offset:640\n\t"
                 "global_load_dwordx4 %6, %7, %8 offset:768"
                 : "=&v"(xr[0]), "=&v"(xr[1]), "=&v"(xr[2]), "=&v"(xr[3]), "=&v"(xr[4]), "=&v"(xr[5]), "=&v"(xr[6])
                 : "v"(xrow + (tid & 7) * 16 + (blockIdx.x * 8 - 56) * (D * 2)), "s"(x) : "memory");
  }
  auto stats = [&](u32 tag) {
    asm volatile("" : "+v"(xr[0]), "+v"(xr[1]), "+v"(xr[2]), "+v"(xr[3]), "+v"(xr[4]), "+v"(xr[5]), "+v"(xr[6]));
    float s[4] = {0.f, 0.f, 0.f, 0.f}, q[4] = {0.f, 0.f, 0.f, 0.f};
#pragma unroll
    for (int j = 0; j < 7; ++j)
#pragma unroll
      for (int i = 0; i < 4; ++i) { s[i] = dot_bf(xr[j][i], 0x3f803f80u, s[i]); q[i] = dot_bf(xr[j][i], xr[j][i], q[i]); }
    float st = (s[0] + s[1]) + (s[2] + s[3]), qt = (q[0] + q[1]) + (q[2] + q[3]);
    oct_sum2(st, qt);
    const float mean = st * (1.0f / D);
    const u32x2 gr = u32x2{finish_stats(mean, qt * (1.0f / D) - mean * mean), tag};
    asm volatile("s_mov_b64 exec, %3\n\tglobal_store_dwordx2 %0, %1, %2 sc1\n\ts_mov_b64 exec, -1\n\ts_setprio 0"
                 :: "v"(WS_SG + blockIdx.x * 64 + lane), "v"(gr), "s"(ws), "s"(0x0101010101010101ull) : "memory");
  };
  u32x4 xb, ga0, ga1, ga2;
  u32 epoch, pa, pi_, pbr, pbi, pcr, pci, sl, sd;
  asm volatile("global_load_dwordx4 %0, %4, %5\n\t"
               "s_load_dword %1, %6, %c7\n\ts_load_dword %2, %8, %10\n\ts_load_dword %3, %9, %10"
               : "=&v"(xb), "=&s"(epoch), "=&s"(sl), "=&s"(sd)
               : "v"(xb_o), "s"(x), "s"(ws), "i"(WS_EPOCH), "s"(log_dt), "s"(Dskip), "s"(cp4) : "memory");
  SCHED0();
  const bool tiler = wave < 4 && !chore;
  const u32 wslice = 4 * (xcd >> 1) + (idx & 3), wchunk = (xcd & 1) * 7 + (idx >> 2);
  u32 gu0 = 0, gu1 = 0, ga_off = tc * 16;
  if (tiler) {
    gu0 = lane * 16 + ((lane * 37) >> 9) * (F * 2 - 224) + (wchunk * 32 + wq * 8) * (F * 2) + wslice * 224;
    gu1 = gu0 + 4 * (F * 2) - 1792;
    asm volatile("" : "+v"(gu0), "+v"(gu1));
    __builtin_amdgcn_s_setprio(1);
  }
  const char* gtab = (const char*)gather_tab.a + 4096;
  const char* gtab2 = gtab + 4096;
  asm volatile("" : "+s"(gtab), "+s"(gtab2));
  asm volatile("" : "+v"(ga_off));
  if (tid == 0) *(u32x4*)(lds + SL_T + 768 * 4) = u32x4{0u, 0u, 0u, 0u};
  if (wq == 3) *(u32x2*)(lds + SL_U + ch * U_CH + lane * 8) = u32x2{0u, 0u};
  SCHED0();
  asm volatile("global_load_ushort %0, %6, %7\n\tglobal_load_ushort %1, %6, %8\n\tglobal_load_ushort %2, %6, %9\n\t"
               "global_load_ushort %3, %6, %10\n\tglobal_load_ushort %4, %6, %11\n\tglobal_load_ushort %5, %6, %12"
               : "=&v"(pa), "=&v"(pi_), "=&v"(pbr), "=&v"(pbi), "=&v"(pcr), "=&v"(pci)
               : "v"(cp * 1024 + tid * 2), "s"(A_re), "s"(A_im), "s"(B_re), "s"(B_im), "s"(C_re), "s"(C_im) : "memory");
  asm volatile("global_load_dwordx4 %0, %3, %4 offset:-4096\n\tglobal_load_dwordx4 %1, %3, %4\n\tglobal_load_dwordx4 %2, %3, %5"
               : "=&v"(ga0), "=&v"(ga1), "=&v"(ga2) : "v"(ga_off), "s"(gtab), "s"(gtab2) : "memory");

  asm volatile("s_waitcnt vmcnt(9) lgkmcnt(0)" : "+v"(xb), "+v"(cpf) :: "memory");
  asm volatile("" : "+s"(epoch), "+s"(sl), "+s"(sd));
  const u32 pl = ch ? sl >> 16 : sl, pd = ch ? sd >> 16 : sd;
  *(LDS3 u32x4*)(uintptr_t)xb_lds = xb;
  const u32 tag = next_tag(epoch);
  asm volatile("s_waitcnt lgkmcnt(0)" ::: "memory");
  __builtin_amdgcn_s_barrier();
  asm volatile("" ::: "memory");
  {
    const LDS3 u32* xr_l = (const LDS3 u32*)(uintptr_t)xg_lds;
    const u32 a0 = xr_l[0], a1 = xr_l[XR_ROW / 4], a2 = xr_l[14], a3 = xr_l[XR_ROW / 4 + 14];
    asm volatile("s_waitcnt vmcnt(0)" : "+v"(pa), "+v"(pi_), "+v"(pbr), "+v"(pbi), "+v"(pcr), "+v"(pci), "+v"(ga0), "+v"(ga1), "+v"(ga2) :: "memory");
    if (tiler) {
      const u32 lw = lds_addr(lds) + SL_W + wq * 3584;
      asm volatile("s_mov_b32 m0, %0\n\ts_mov_b64 exec, %6\n\t"
                   "global_load_lds_dwordx4 %2, %4\n\tglobal_load_lds_dwordx4 %3, %4 offset:1792\n\t"
                   "s_mov_b32 m0, %1\n\ts_nop 0\n\t"
                   "global_load_lds_dwordx4 %2, %5\n\tglobal_load_lds_dwordx4 %3, %5 offset:1792\n\t"
                   "s_mov_b64 exec, -1"
                   :: "s"(lw), "s"(lw + 896), "v"(gu0), "v"(gu1), "s"(Wg), "s"(Wu), "s"(0x00ffffffffffffffull)
                   : "memory");
    }
    if (wave == 7) stats(tag); else publish(tag, a0, a1, a2, a3);
    if (chore) {
      if (blockIdx.x == 255 && tid < 128) *(u32*)(ws + WS_CNT + tid * 256) = 0u;
      return;
    }
  }
  s16x8 tf[2][2];
  float tcs[2];
  auto tile_read = [&]() {
    const u32 hh = lane >> 5, blk = (lane >> 4) & 1, q = (lane & 15) >> 2, p = lane & 3;
    const u32 a = lds_addr(lds) + SL_W + wq * 64 + hh * 3584 + q * 224 + blk * 896 + 8 * p;
#pragma unroll
    for (int i = 0; i < 2; ++i) {
      tf[i][0] = tr2(a + 32 * i, 0, 1792); tf[i][1] = tr2(a + 32 * i, 4 * 1792, 5 * 1792);
    }
#pragma unroll
    for (int i = 0; i < 2; ++i) tcs[i] = half_sum(dot_ones(tf[i][1], dot_ones(tf[i][0], 0.f)));
  };
  auto tile_store = [&]() {
    const u32 un = wslice * 14 + wchunk;
    const u32 wt = WS_WT + wslice * (196 * 1024) + (wq < 2 ? wchunk * 8 + 2 * wq : 112 + wchunk * 6 + 2 * wq - 4) * 1024 + lane * 16;
    const u32 ks = wq < 2 ? 4096 : 3072;
    const u32 cs = WS_CS + un * 896 + wq * 256 + lane * 4;
#pragma unroll
    for (int i = 0; i < 2; ++i) {
      if (i == 0 || wq < 3) {
        st128_dev(ws, wt + 1024 * i, __builtin_bit_cast(u32x4, tf[i][0]));
        st128_dev(ws, wt + 1024 * i + ks, __builtin_bit_cast(u32x4, tf[i][1]));
        asm volatile("s_mov_b64 exec, %3\n\tglobal_store_dword %0, %1, %2 sc1\n\ts_mov_b64 exec, -1"
                     :: "v"(cs + 128 * i), "v"(tcs[i]), "s"(ws), "s"(0xffffffffull) : "memory");
      }
    }
  };
  const __amdgpu_buffer_rsrc_t rs = __builtin_amdgcn_make_buffer_rsrc((void*)ws, 0, (int)WS_END, 0x00020000);
  const u32 cnt = lds_addr(lds) + SL_CNT + ch * 4;
  f32x2 cb;
  float inv_sc, p2, rv;
  {
    const float dt = rbf(__builtin_amdgcn_exp2f(bf_lo(pl) * 1.4426950408889634f));
    p2 = (dt * bf_lo(pa)) * 1.4426950408889634f;
    rv = (dt * bf_lo(pi_)) * 0.15915494309189535f;
    cb = cmul(f32x2{bf_lo(pcr), bf_lo(pci)}, f32x2{bf_lo(pbr), bf_lo(pbi)} * dt);
    const float cbx = cb.x, cby = cb.y;
    u32 eb = (u32)wave_imax(max(__builtin_bit_cast(int, cbx) & 0x7fffffff, __builtin_bit_cast(int, cby) & 0x7fffffff)) >> 23;
    asm("s_max_u32 %0, %0, 27\n\ts_min_u32 %0, %0, 227" : "+s"(eb) :: "scc");
    inv_sc = __builtin_bit_cast(float, (eb - 9) << 23);
    cb *= __builtin_bit_cast(float, (263 - eb) << 23);
  }

  {
    set_m0(lds_addr(lds) + ch * KG_CH + wq * 256);
    auto cpow = [&](float mp, float mr) {
      const float mag = __builtin_amdgcn_exp2f(p2 * mp);
      const float t = __builtin_amdgcn_fractf(rv * mr);
      return f32x2{__builtin_amdgcn_cosf(t), __builtin_amdgcn_sinf(t)} * mag;
    };
    const f32x2 z1 = cpow(1.f, 1.f), c8 = cpow(8.f, -8.f), w256 = cpow(256.f, 256.f);
    const f32x2 z2 = cmul(z1, z1), c16 = cmul(c8, c8);
    const f32x2 z4 = cmul(z2, z2), c32 = cmul(c16, c16);
    f32x2 e0 = f32x2{1.f, 0.f}, e1 = c8, e2 = c16, e3 = cmul(c16, c8);
    f32x2 fa = cb, fb = cmul(cb, w256);
    put6<0, 8, 21, 22, 23, 24>(pack_h(fa), pack_h(fb), 0x3c00u, pack_h(e1), pack_h(e2), pack_h(e3));
    const f32x2 g1 = cmul(fb, z2), g2 = cmul(fb, z4);
    kg_step<1, 12>(fa, fb, e0, e1, e2, e3, z1, w256, c32);
    const f32x2 g3 = cmul(g1, z4), g4 = cmul(fb, z4);
    kg_step<2, 14>(fa, fb, e0, e1, e2, e3, z1, w256, c32);
    const u32 h772 = pack_h(cmul(fb, z4));
    put6<9, 10, 11, 13, 15, 15>(pack_h(g1), pack_h(g2), pack_h(g3), pack_h(g4), h772, h772);
    kg_step<3, 16>(fa, fb, e0, e1, e2, e3, z1, w256, c32);
    kg_step<4, 17>(fa, fb, e0, e1, e2, e3, z1, w256, c32);
    kg_step<5, 18>(fa, fb, e0, e1, e2, e3, z1, w256, c32);
    kg_step<6, 19>(fa, fb, e0, e1, e2, e3, z1, w256, c32);
    kg_step<7, 20>(fa, fb, e0, e1, e2, e3, z1, w256, c32);
  }
  {
    char* kg = lds + ch * KG_CH + wq * 256;
    const char* fa = kg + (lane & 31) * KG_ROW + (lane >> 5) * 16;
    h16x8 a[8], b[8];
    asm volatile("s_waitcnt lgkmcnt(0)" ::: "memory");
#pragma unroll
    for (int s = 0; s < 8; ++s) { a[s] = *(const h16x8*)(fa + 32 * s); b[s] = *(const h16x8*)(fa + 21 * KG_ROW + 32 * s); }
    SCHED0();
    f32x16 acc;
#pragma unroll
    for (int i = 0; i < 16; ++i) acc[i] = 0.f;
#pragma unroll
    for (int s = 0; s < 8; ++s) acc = __builtin_amdgcn_mfma_f32_32x32x16_f16(a[s], b[s], acc, 0, 0, 0);
    char* pt = kg + (lane >> 4) * KG_ROW + (lane & 15) * 16;
#pragma unroll
    for (int q = 0; q < 3; ++q)
      *(f32x4*)(pt + 4 * q * KG_ROW) = f32x4{acc[4 * q], acc[4 * q + 1], acc[4 * q + 2], acc[4 * q + 3]} * inv_sc;
  }
  const u32 xo = WS_XG + cp * (L * 8);
  u32x4 gx[4], gs[4];
  auto poll = [&]() {
#pragma unroll
    for (int i = 0; i < 4; ++i) {
      gx[i] = __builtin_amdgcn_raw_buffer_load_b128(rs, tc * 16, xo + 4096 * i, 16);
      gs[i] = __builtin_amdgcn_raw_buffer_load_b128(rs, tc * 16, WS_SG + 4096 * i, 16);
    }
  };
  group_barrier(cnt, 4);
  if (wq < 3) {
    const f32x4* pt = (const f32x4*)(lds + ch * KG_CH + 4 * wq * KG_ROW + (lane >> 4) * KG_ROW + (lane & 15) * 16);
    const f32x4 s = (pt[0] + pt[16]) + (pt[32] + pt[48]);
    const u32 p01 = pack_bf(s[0], s[1]), p23 = pack_bf(s[2], s[3]);
    u16* T = (u16*)(lds + SL_T + wq * 1024 + ch * 2 + (lane >> 5) * 512 + (lane & 31) * 4);
    T[0] = (u16)p01; T[64] = (u16)(p01 >> 16); T[128] = (u16)p23; T[192] = (u16)(p23 >> 16);
  }
  group_barrier(cnt, 8);
  poll();
  const u32 sel = ch ? 0x07060302u : 0x05040100u;
  {
    u32 e[11];
#pragma unroll
    for (int j = 0; j < 11; ++j) {
      const u32 a = j < 4 ? ga0[j & 3] : j < 8 ? ga1[j & 3] : ga2[j & 3];
      e[j] = *(const LDS3 u32*)(uintptr_t)(a + lds_addr(lds));
    }
    char* kc = lds + ch * KC_CH + tc * 16;
#pragma unroll
    for (int s = 0; s < 4; ++s)
      *(u32x4*)(kc + s * KC_STRIDE) = u32x4{__builtin_amdgcn_perm(e[s + 1], e[s], sel), __builtin_amdgcn_perm(e[s + 3], e[s + 2], sel),
                                            __builtin_amdgcn_perm(e[s + 5], e[s + 4], sel), __builtin_amdgcn_perm(e[s + 7], e[s + 6], sel)};
    if (wq == 0 && lane < 8) *(u32x4*)(lds + ch * KC_CH + (lane >> 1) * KC_STRIDE + (256 + (lane & 1)) * 16) = u32x4{0u, 0u, 0u, 0u};
  }
  u32 cv_ka, cv_ub, ep_lo;
  {
    const u32 b = lane & 15, g = lane >> 4, r3 = 8 * (wq >> 1) + 6;
    const u32 gg = 16 * (g & 1) - 32 * (g >> 1);
    cv_ka = lds_addr(lds) + ch * KC_CH + 3 * KC_STRIDE + (2044 - 16 * r3 - 256 * 7) * 2 - (b & 3) * KC_STRIDE - 8 * (b >> 2) + gg;
    cv_ub = lds_addr(lds) + SL_U + ch * U_CH + 512 - 32 * r3 + 32 * b + gg;
    ep_lo = 32 * b + 8 * g;
    asm volatile("" : "+v"(cv_ka), "+v"(cv_ub), "+v"(ep_lo));
  }
  {
    bool clean = true;
    for (u32 spins = 0;; ++spins) {
      u32 bad = 0;
#pragma unroll
      for (int i = 0; i < 4; ++i) bad |= ((gx[i][1] ^ tag) | (gx[i][3] ^ tag)) | ((gs[i][1] ^ tag) | (gs[i][3] ^ tag));
      if (__all(bad == 0u)) break;
      if (spins >= 4096u) { clean = false; break; }
      __builtin_amdgcn_s_sleep(4);
      asm volatile("" ::: "memory");
      poll();
    }
    u32 xs[8], st[8];
#pragma unroll
    for (int i = 0; i < 8; ++i) { xs[i] = gx[i >> 1][2 * (i & 1)]; st[i] = gs[i >> 1][2 * (i & 1)]; }
    if (!clean) {
      *(u32*)(lds + SL_FLAG) = 1u;
#pragma unroll 1
      for (int i = 0; i < 8; ++i) {
        const u32* xrow = (const u32*)(x + (512 * (i >> 1) + 2 * tc + (i & 1)) * D);
        float s = 0.f, q = 0.f;
#pragma unroll 1
        for (int j = 0; j < D / 2; ++j) { const u32 w = xrow[j]; s = dot_bf(w, 0x3f803f80u, s); q = dot_bf(w, w, q); }
        const float mean = s * (1.0f / D);
#pragma unroll
        for (int k = 0; k < 7; ++k) { st[k] = st[k + 1]; xs[k] = xs[k + 1]; }
        st[7] = finish_stats(mean, q * (1.0f / D) - mean * mean);
        xs[7] = xrow[cp];
      }
    }
    u32x4 xw, uw;
#pragma unroll
    for (int i = 0; i < 4; ++i) {
      xw[i] = __builtin_amdgcn_perm(xs[2 * i + 1], xs[2 * i], sel);
      const f32x2 u2 = (f32x2{bf_lo(xw[i]), bf_hi(xw[i])} - f32x2{bf_lo(st[2 * i]), bf_lo(st[2 * i + 1])}) *
                       f32x2{__builtin_amdgcn_rcpf(bf_hi(st[2 * i])), __builtin_amdgcn_rcpf(bf_hi(st[2 * i + 1]))};
      uw[i] = pack_bf(u2.x, u2.y);
    }
    u32* up = (u32*)(lds + SL_U + ch * U_CH + 512) + tc;
    u32* xp = (u32*)(lds + SL_XX + ch * L * 2) + tc;
#pragma unroll
    for (int i = 0; i < 4; ++i) { up[256 * i] = uw[i]; xp[256 * i] = xw[i]; }
  }
  group_barrier(cnt, 12);
  if (tiler) { tile_read(); tile_store(); }
  if (blockIdx.x == 0 && tid == 0 && *(const u32*)(lds + SL_FLAG) == 0u)
    __hip_atomic_store((GAS u32*)(ws + WS_EPOCH), tag, __ATOMIC_RELAXED, __HIP_MEMORY_SCOPE_AGENT);
  if (wave < 7)
    asm volatile("s_mov_b32 m0, %0\n\ts_nop 0\n\tglobal_load_lds_dword %1, %2"
                 :: "s"(lds_addr(lds) + SL_DUMP), "v"(idx * 57344 + tid * 128), "s"(Wd) : "memory");
  const u32 tg = wq & 1, rh = wq >> 1;
  f32x4 acc[4];
#pragma unroll
  for (int i = 0; i < 4; ++i) acc[i] = f32x4{0.f, 0.f, 0.f, 0.f};
  if (tg == 0) conv_body<0>(cv_ka, cv_ub, acc);
  else conv_body<1>(cv_ka, cv_ub, acc);
  {
    f32x4* red = (f32x4*)(lds + ch * KG_CH + SL_RED) + lane;
    f32x4 y[2];
    if (rh) { red[wq * 128] = acc[0]; red[wq * 128 + 64] = acc[3]; y[0] = acc[1]; y[1] = acc[2]; }
    else { red[wq * 128] = acc[1]; red[wq * 128 + 64] = acc[2]; y[0] = acc[0]; y[1] = acc[3]; }
    const int t0 = rh ? 5 - tg : 7 - tg, t1 = rh ? 2 + tg : tg;
    const char* up = lds + SL_U + ch * U_CH + 512 + ep_lo;
    const char* xp = lds + SL_XX + ch * L * 2 + ep_lo;
    const u32x2 uu0 = *(const u32x2*)(up + 512 * t0), uu1 = *(const u32x2*)(up + 512 * t1);
    const u32x2 xx0 = *(const u32x2*)(xp + 512 * t0), xx1 = *(const u32x2*)(xp + 512 * t1);
    const float dsk = bf_lo(pd);
    f32x2 b0[2], b1[2];
#pragma unroll
    for (int j = 0; j < 2; ++j) {
      b0[j] = __builtin_elementwise_fma(f32x2{bf_lo(uu0[j]), bf_hi(uu0[j])}, f32x2{dsk, dsk}, f32x2{bf_lo(xx0[j]), bf_hi(xx0[j])}) + f32x2{y[0][2 * j], y[0][2 * j + 1]};
      b1[j] = __builtin_elementwise_fma(f32x2{bf_lo(uu1[j]), bf_hi(uu1[j])}, f32x2{dsk, dsk}, f32x2{bf_lo(xx1[j]), bf_hi(xx1[j])}) + f32x2{y[1][2 * j], y[1][2 * j + 1]};
    }
    u32 h_off = WS_HT + (2 * cp + ch) * 256 + (ep_lo & 0xf7) + (ep_lo >> 8) * (D * 256) + 2 * D * 256 * ((lane & 16) ? t1 : t0);
    asm volatile("" : "+v"(b0[0]), "+v"(b0[1]), "+v"(b1[0]), "+v"(b1[1]), "+v"(h_off));
    group_barrier(cnt, 16);
    const f32x4 o0 = red[(wq ^ 2) * 128], o1 = red[(wq ^ 2) * 128 + 64];
    const auto e0 = __builtin_amdgcn_permlane16_swap(pack_bf(b0[0].x + o0[0], b0[0].y + o0[1]), pack_bf(b1[0].x + o1[0], b1[0].y + o1[1]), false, false);
    const auto e1 = __builtin_amdgcn_permlane16_swap(pack_bf(b0[1].x + o0[2], b0[1].y + o0[3]), pack_bf(b1[1].x + o1[2], b1[1].y + o1[3]), false, false);
    st128_dev(ws, h_off, u32x4{e0[0], e1[0], e0[1], e1[1]});
    wait_vm<1>();
  }
}

constexpr int F1_STAGE = 16384;
constexpr int F1_P2 = 4 * F1_STAGE;
constexpr int F1_CS = F1_P2 + 84 * 1024;
constexpr int F1_ARR = F1_CS + 7 * 256;
constexpr int F2_PS = F1_ARR + 32;
constexpr int F2_RES = F2_PS + 2048;
constexpr int F1_TOTAL = F2_RES + 4 * 2048;
static_assert(F1_TOTAL <= 163840, "lds map");
constexpr int F2_WIN = 8;
constexpr int PUB1 = 16;
constexpr int LOOK2 = 3;

DI void f1_publish(char* ws, u32 arr_addr, u32 last, int cnt) {
  u32 old, one = 1;
  asm volatile("s_mov_b64 exec, 1\n\tds_add_rtn_u32 %0, %1, %2\n\ts_mov_b64 exec, -1\n\ts_waitcnt lgkmcnt(0)" : "=&v"(old) : "v"(arr_addr), "v"(one) : "memory");
  if (__builtin_amdgcn_readfirstlane(old) == last && (threadIdx.x & 63) == 0)
    __hip_atomic_fetch_add((GAS u32*)(ws + WS_CNT + cnt * 256), 1u, __ATOMIC_RELAXED, __HIP_MEMORY_SCOPE_AGENT);
}
DI constexpr int f1_slot(int c) { return c <= 8 ? c : c < 13 ? c - 9 : 7; }
DI constexpr int f2_ks(int i) { return i < 16 ? (i >> 2) * 7 + (i & 3) : ((i - 16) / 3) * 7 + 4 + (i - 16) % 3; }
#define BARRIER() do { __builtin_amdgcn_s_barrier(); asm volatile("" ::: "memory"); } while (0)

__global__ __launch_bounds__(512, 2) void k_ffn(char* __restrict__ ws, u16* __restrict__ out, const char* __restrict__ Wd) {
  __shared__ __attribute__((aligned(16))) char lds[F1_TOTAL];
  const int tid = threadIdx.x, lane = tid & 63;
  const int wave = __builtin_amdgcn_readfirstlane(tid >> 6);
  const int xcd = blockIdx.x & 7, bi = blockIdx.x >> 3;
  const int panel = (xcd & 1) * 8 + (bi & 7), slice = (xcd >> 1) * 4 + (bi >> 3);
  const u32 vo = lane * 16;

  if (wave >= 4) {
    const int lw = wave - 4;
    const char* hb = ws + WS_HT + panel * (D * 256) + lw * 2048;
    const char* wb = ws + WS_WT + slice * (196 * 1024);
    const u32 ho = (lane >> 4) * 256 + (((lane & 15) ^ ((lane >> 4) << 2)) * 16);
    const u32 lbase = lds_addr(lds) + lw * 2048;
    auto stage = [&](int c) {
      asm volatile("s_mov_b32 m0, %0\n\ts_nop 0\n\t"
                   "global_load_lds_dwordx4 %2, %4\n\tglobal_load_lds_dwordx4 %2, %4 offset:1024\n\t"
                   "s_mov_b32 m0, %1\n\ts_nop 0\n\t"
                   "global_load_lds_dwordx4 %3, %5\n\tglobal_load_lds_dwordx4 %3, %5 offset:1024"
                   :: "s"(lbase + f1_slot(c) * F1_STAGE), "s"(lbase + f1_slot(c) * F1_STAGE + 8192),
                      "v"(ho), "v"(vo), "s"(hb + c * 8192), "s"(wb + lw * 2048 + c * 8192)
                   : "memory");
    };
    stage(0);
    SCHED0();
    auto p2 = [&](int i) {
      asm volatile("s_mov_b32 m0, %0\n\ts_nop 0\n\tglobal_load_lds_dwordx4 %1, %2"
                   :: "s"(lds_addr(lds) + F1_P2 + (i * 4 + lw) * 1024), "v"(vo), "s"(wb + (112 + i * 4 + lw) * 1024) : "memory");
    };
    f32x2 psum = {0.f, 0.f};
    stage(1); stage(2);
    f32x4 v[4];
    asm volatile("global_load_dwordx4 %0, %4, %6\n\tglobal_load_dwordx4 %1, %4, %6 offset:3584\n\t"
                 "global_load_dwordx4 %2, %4, %7\n\tglobal_load_dwordx4 %3, %5, %6"
                 : "=&v"(v[0]), "=&v"(v[1]), "=&v"(v[2]), "=&v"(v[3])
                 : "v"(lw * 896 + min(lane, 55) * 16), "v"((lw < 2 ? lw + 12 : lw) * 896 + min(lane, 55) * 16),
                   "s"(ws + WS_CS + slice * (14 * 896)), "s"(ws + WS_CS + slice * (14 * 896) + 7168) : "memory");
    wait_vm<2 * 4 + 4>();
    BARRIER();
    auto csum_a = [&]() {
      asm volatile("" : "+v"(v[0]), "+v"(v[1]), "+v"(v[2]), "+v"(v[3]));
      f32x4 t = (v[0] + v[1]) + v[2];
      if (lw < 2) t += v[3];
      *(f32x4*)(lds + F2_RES + lw * 1024 + lane * 16) = t;
    };
    auto csum_b = [&]() {
      const int i = tid - 256;
      const float* q = (const float*)(lds + F2_RES) + i;
      const float csum = (q[0] + q[256]) + (q[512] + q[768]);
      u32* ca = (u32*)(lds + F1_CS) + (i >> 5) * 64 + (i & 31);
      if (i < 224) { ca[0] = pack_bf(csum, csum - rbf(csum)); ca[32] = 0u; }
    };
    auto part = [&](int c0, int c1) {
      if (slice >= c0 && slice <= c1) {
        const int q = (lane & 15) >> 2, sl = c0 == 8 ? (slice == 13 ? 7 : slice == 8 ? 8 : slice - 9) : slice;
        const u32 a = lds_addr(lds) + sl * F1_STAGE + (8 * (lane >> 5) + q) * 256 + ((lw ^ q) * 64) + ((lane >> 4) & 1) * 32 + 8 * (lane & 3);
        const u32x4 n0 = __builtin_bit_cast(u32x4, tr2(a, 0, 1024)), n1 = __builtin_bit_cast(u32x4, tr2(a, 4096, 5120));
        float s0 = 0.f, s1 = 0.f, q0 = 0.f, q1 = 0.f;
#pragma unroll
        for (int i = 0; i < 8; ++i) {
          const u32 w = i < 4 ? n0[i & 3] : n1[i & 3];
          const float lo = bf_lo(w), hi = bf_hi(w);
          s0 += lo; q0 = __builtin_fmaf(lo, lo, q0);
          s1 += hi; q1 = __builtin_fmaf(hi, hi, q1);
          asm("" : "+v"(s0)); asm("" : "+v"(q0)); asm("" : "+v"(s1)); asm("" : "+v"(q1));
        }
        psum = f32x2{s0 + s1, q0 + q1};
        asm volatile("" : "+v"(psum));
      }
    };
#define CLOSE(n) do { asm volatile("s_waitcnt vmcnt(" #n ") lgkmcnt(0)" ::: "memory"); BARRIER(); } while (0)
    stage(3); stage(4); CLOSE(16);
    stage(5); stage(6); CLOSE(12);
    part(0, 3); stage(7); stage(8); csum_a(); CLOSE(12);
    stage(9); stage(10); csum_b(); CLOSE(12);
    part(4, 7); stage(11); stage(12); CLOSE(12);
    stage(13); p2(0); p2(1); p2(2); p2(3); CLOSE(12);
    p2(4); p2(5); p2(6); p2(7); p2(8); p2(9); p2(10); p2(11); CLOSE(12);
    part(8, 13); CLOSE(0);
#undef CLOSE
    const __amdgpu_buffer_rsrc_t rs = __builtin_amdgcn_make_buffer_rsrc((void*)ws, 0, (int)WS_END, 0x00020000);
    if (slice < 14) {
      const float a = half_sum(psum.x), b = half_sum(psum.y);
      if (lane < 32) __builtin_amdgcn_raw_buffer_store_b64(__builtin_bit_cast(u32x2, f32x2{a, b}), rs, lane * 8, WS_PS + ((panel * 14 + slice) * 128 + lw * 32) * 8, 16);
    }
    p2(12); p2(13); p2(14); p2(15); p2(16); p2(17); p2(18); p2(19); p2(20);
    const bool down = bi < 28;
    const int tgp = (xcd & 1) * 16 + (xcd >> 1) * 4 + (bi & 3), dp = down ? bi >> 2 : 0;
    const u32 a1 = lds_addr(lds) + lw * 16384, a2 = lds_addr(lds) + F1_P2 + lw * 21504;
    const u32 wsrc = (lane >> 3) * (D * 2) + (((lane & 7) ^ ((lane >> 2) & 4)) * 16);
    const char* wbase = Wd + lw * 448 * (D * 2) + dp * 128;
    auto wd_load = [&](u32 dst, int i0, int n) {
#pragma unroll
      for (int i = 0; i < n; ++i) {
        const char* b0 = wbase + f2_ks(i0 + i) * 16 * (D * 2);
        asm volatile("s_mov_b32 m0, %0\n\ts_nop 0\n\tglobal_load_lds_dwordx4 %1, %2\n\tglobal_load_lds_dwordx4 %1, %3 offset:1024"
                     :: "s"(dst + i * 2048), "v"(wsrc), "s"(b0), "s"(b0 + 8 * (D * 2) - 1024) : "memory");
      }
    };
    const u32 q4 = (lane & 15) >> 2;
    const u32 tq = (4 * (lane >> 5) + q4) * 128 + (q4 >> 1) * 64 + ((lane >> 4) & 1) * 32 + (lane & 3) * 8;
    const u32 t10 = a1 + tq, t11 = a1 + (tq ^ 64), t20 = a2 + tq, t21 = a2 + (tq ^ 64);
    wd_load(a1, 0, 8);
    wait_vm<16>();
    BARRIER();
    f1_publish(ws, lds_addr(lds) + F1_ARR, 7u, 64 + panel * 4 + (slice >> 2));
    if (!down) { wait_vm<0>(); return; }
    const u32 cnt_off = WS_CNT + ((tgp >> 1) * 4 + lw) * 256;
    s16x8 ra[8][2];
    u32 look;
#pragma unroll
    for (int k = 0; k < 8; ++k) {
      wait_vm<14>();
      ra[k][0] = tr2(t10, k * 2048, k * 2048 + 1024); ra[k][1] = tr2(t11, k * 2048, k * 2048 + 1024);
      asm volatile("" : "+v"(ra[k][0]), "+v"(ra[k][1]));
      wd_load(a1 + k * 2048, 8 + k, 1);
      if (k == 4) asm volatile("global_load_dword %0, %1, %2 sc1" : "=&v"(look) : "v"(cnt_off), "s"(ws) : "memory");
    }
    const int tok0 = tgp * 64 + (lw & 1) * 32, d0 = dp * 64 + (lw >> 1) * 32;
    const u32 aux = lds_addr(lds) + F2_RES + lw * 2048;
    {
      const char* hr = ws + WS_HT + (tok0 >> 7) * (D * 256) + d0 * 256 + (tok0 & 127) * 2;
      asm volatile("s_mov_b32 m0, %0\n\ts_nop 0\n\tglobal_load_lds_dwordx4 %1, %2\n\tglobal_load_lds_dwordx4 %1, %3 offset:1024"
                   :: "s"(aux), "v"((lane >> 2) * 256 + (lane & 3) * 16), "s"(hr), "s"(hr + 4096 - 1024) : "memory");
    }
    auto wait_cnt = [&](int set) {
      for (u32 spins = 0; spins < (1u << 16); ++spins) {
        const u32 c = __builtin_amdgcn_raw_buffer_load_b32(rs, 0, cnt_off + set * 64 * 256, 16);
        if (__builtin_amdgcn_readfirstlane(c) >= 4u) break;
        __builtin_amdgcn_s_sleep(2);
        asm volatile("" ::: "memory");
      }
      asm volatile("" ::: "memory");
    };
    asm volatile("s_waitcnt vmcnt(8)" : "+v"(look) :: "memory");
    if (__builtin_amdgcn_readfirstlane(look) < 4u) wait_cnt(0);
    const u32 sb = WS_HF + (tgp * 224 + lw * 28) * 1024;
    s16x8 rb[F2_WIN][2];
    auto ldb = [&](int i) {
      const int k = f2_ks(i);
      rb[i % F2_WIN][0] = __builtin_bit_cast(s16x8, __builtin_amdgcn_raw_buffer_load_b128(rs, vo + (k & 3) * 1024, sb + (k >> 2) * 4096, 16));
      rb[i % F2_WIN][1] = __builtin_bit_cast(s16x8, __builtin_amdgcn_raw_buffer_load_b128(rs, vo + (k & 3) * 1024, sb + (k >> 2) * 4096 + 112 * 1024, 16));
    };
#pragma unroll
    for (int k = 0; k < F2_WIN; ++k) { ldb(k); SCHED0(); }
    {
      u32 c, sv;
      asm volatile("1:\n\tds_read_b32 %0, %2\n\ts_waitcnt lgkmcnt(0)\n\tv_readfirstlane_b32 %1, %0\n\ts_cmp_lt_u32 %1, 4\n\ts_cbranch_scc0 2f\n\ts_sleep 1\n\ts_branch 1b\n2:"
                   : "=&v"(c), "=&s"(sv) : "v"(lds_addr(lds) + F1_ARR + 4) : "memory", "scc");
    }
    wd_load(a2, 16, 10);
    SCHED0();
    f32x2 ps[4];
    f32x16 acc[4];
#pragma unroll
    for (int a = 0; a < 4; ++a)
#pragma unroll
      for (int i = 0; i < 16; ++i) acc[a][i] = 0.f;
    u32x2 hres[4];
    s16x8 la[2][2];
    static_assert(F2_WIN <= 8 && F2_WIN >= 2, "LDS-DMA must be older than fragments consumed before it is read");
#pragma unroll
    for (int k = 0; k < 28; ++k) {
      const int n = k + 1;
      if (n >= 8 && n < 28) {
        const int o = (n < 16 ? n - 8 : n < 26 ? n - 16 : n - 26) * 2048;
        la[n & 1][0] = tr2(n >= 16 && n < 26 ? t20 : t10, o, o + 1024);
        la[n & 1][1] = tr2(n >= 16 && n < 26 ? t21 : t11, o, o + 1024);
      }
#pragma unroll
      for (int a = 0; a < 4; ++a) acc[a] = MFMA32(k < 8 ? ra[k < 8 ? k : 0][a >> 1] : la[k & 1][a >> 1], rb[k % F2_WIN][a & 1], acc[a]);
      SCHED0();
      if (k == 9) wd_load(a1, 26, 2);
      if (k + F2_WIN == 16) {
        asm volatile("s_waitcnt vmcnt(%c1)" : "+v"(look) : "i"(2 * LOOK2) : "memory");
        if (__builtin_amdgcn_readfirstlane(look) < 4u) wait_cnt(1);
#pragma unroll
        for (int i = 0; i < 4; ++i)
          ps[i] = __builtin_bit_cast(f32x2, __builtin_amdgcn_raw_buffer_load_b64(rs, lane * 8 + (i < 2 || lw < 3 ? i : i - 2) * 1024,
                                                                                 WS_PS + (tgp >> 1) * (14 * 1024) + (tgp & 1) * 512 + lw * 4096, 16));
        SCHED0();
      }
      if (k + F2_WIN + LOOK2 == 16) asm volatile("global_load_dword %0, %1, %2 sc1" : "=&v"(look) : "v"(cnt_off + 64 * 256), "s"(ws) : "memory");
      if (k + F2_WIN < 28) ldb(k + F2_WIN);
      if (k == 18) {
        const f32x2 t = lw < 3 ? (ps[0] + ps[1]) + (ps[2] + ps[3]) : ps[0] + ps[1];
        *(LDS3 f32x2*)(uintptr_t)(lds_addr(lds) + F2_PS + lw * 512 + lane * 8) = t;
      }
      if (k == 20) {
        const u32 a = aux + (4 * (lane >> 5) + ((lane & 15) >> 2)) * 64 + ((lane >> 4) & 1) * 32 + (lane & 3) * 8;
#pragma unroll
        for (int g = 0; g < 4; ++g) hres[g] = __builtin_bit_cast(u32x2, __builtin_amdgcn_ds_read_tr16_b64_v4i16((LDS3 s16x4*)(uintptr_t)(a + g * 512)));
      }
      SCHED0();
    }
    f32x16 mine;
#pragma unroll
    for (int a = 0; a < 4; ++a) {
      if (a == lw) { mine = acc[a]; }
      else {
        LDS3 f32x4* pp = (LDS3 f32x4*)(uintptr_t)(a1 + a * 4096 + vo);
#pragma unroll
        for (int g = 0; g < 4; ++g) pp[g * 64] = f32x4{acc[a][4 * g], acc[a][4 * g + 1], acc[a][4 * g + 2], acc[a][4 * g + 3]};
      }
    }
    __syncthreads();
    float rs2;
    {
      const LDS3 f32x2* pq = (const LDS3 f32x2*)(uintptr_t)(lds_addr(lds) + F2_PS + ((lw & 1) * 32 + (lane & 31)) * 8);
      const f32x2 t = (pq[0] + pq[64]) + (pq[128] + pq[192]);
      const float mean = t.x * (1.0f / D);
      const float var = fmaxf(t.y * (1.0f / D) - mean * mean, 0.f);
      const float r = __builtin_amdgcn_rcpf(rbf(__builtin_amdgcn_sqrtf(rbf(rbf(var) + 1e-5f))));
      rs2 = r * r;
    }
    u32 pk[8];
    const u32 rdb = lds_addr(lds) + lw * 4096 + vo;
#pragma unroll
    for (int g = 0; g < 4; ++g) {
      const f32x4 p0 = *(const LDS3 f32x4*)(uintptr_t)(rdb + ((lw + 1) & 3) * 16384 + g * 1024);
      const f32x4 p1 = *(const LDS3 f32x4*)(uintptr_t)(rdb + ((lw + 2) & 3) * 16384 + g * 1024);
      const f32x4 p2v = *(const LDS3 f32x4*)(uintptr_t)(rdb + ((lw + 3) & 3) * 16384 + g * 1024);
      const f32x4 sv = (p0 + p1) + (p2v + f32x4{mine[4 * g], mine[4 * g + 1], mine[4 * g + 2], mine[4 * g + 3]});
      const f32x2 o0 = __builtin_elementwise_fma(f32x2{sv[0], sv[1]}, f32x2{rs2, rs2}, f32x2{bf_lo(hres[g][0]), bf_hi(hres[g][0])});
      const f32x2 o1 = __builtin_elementwise_fma(f32x2{sv[2], sv[3]}, f32x2{rs2, rs2}, f32x2{bf_lo(hres[g][1]), bf_hi(hres[g][1])});
      pk[2 * g] = pack_bf(o0.x, o0.y);
      pk[2 * g + 1] = pack_bf(o1.x, o1.y);
    }
    char* ob = (char*)out + tok0 * (D * 2) + d0 * 2;
#pragma unroll
    for (int k = 0; k < 2; ++k) {
      const auto r0 = __builtin_amdgcn_permlane32_swap(pk[4 * k], pk[4 * k + 2], false, false);
      const auto r1 = __builtin_amdgcn_permlane32_swap(pk[4 * k + 1], pk[4 * k + 3], false, false);
      *(u32x4*)(ob + (u32)((lane & 31) * (D * 2) + (lane >> 5) * 16 + 32 * k)) = u32x4{r0[0], r1[0], r0[1], r1[1]};
    }
    if (blockIdx.x == 0 && tid == 256) { u32* e = (u32*)(ws + WS_EPOCH); *e = next_tag(*e) ^ 0x5bd1e995u; }
    asm volatile(".Lffn2_code_end:");
    return;
  }

  const int q = (lane & 15) >> 2;
  u32 bB[3], bA[3];
#pragma unroll
  for (int i = 0; i < 3; ++i) {
    bB[i] = lds_addr(lds) + i * 65536 + (8 * (lane >> 5) + q) * 256 + ((wave ^ q) * 64) + ((lane >> 4) & 1) * 32 + 8 * (lane & 3);
    bA[i] = lds_addr(lds) + i * 65536 + 8192 + vo;
    asm volatile("" : "+v"(bB[i]), "+v"(bA[i]));
  }
  char* dst = ws + WS_HF + ((panel * 4 + wave) * 112 + slice * 7) * 1024;
  s16x8 bk[28], fa[2][4];
  f32x16 acc[7];
  auto rd1 = [&](int k) {
    const int sl = f1_slot(k >> 1), o = (sl & 3) * F1_STAGE + (k & 1) * 4096;
    bk[k] = tr2(bB[sl >> 2], o, o + 1024);
#pragma unroll
    for (int t = 0; t < 4; ++t) fa[k & 1][t] = *(const LDS3 s16x8*)(uintptr_t)(bA[sl >> 2] + o + t * 1024);
  };
  auto rd2 = [&](int k) {
#pragma unroll
    for (int t = 0; t < 3; ++t) {
      const int kb = 56 + k * 3 + t;
      fa[k & 1][t] = *(const LDS3 s16x8*)(uintptr_t)(bA[kb >> 6] + (kb & 63) * 1024);
    }
  };
  const u32 sw = (lane & 15) == ((lane >> 4) & 1) ? 0x3f803f80u : 0u;
  const s16x8 sel = __builtin_bit_cast(s16x8, u32x4{sw, sw, sw, sw});
  f32x4 msum = {0.f, 0.f, 0.f, 0.f};
  auto put = [&](int t) {
    u32x4 o;
#pragma unroll
    for (int j = 0; j < 4; ++j) {
      float a0 = relu(acc[t][2 * j]) * acc[t][8 + 2 * j], a1 = relu(acc[t][2 * j + 1]) * acc[t][9 + 2 * j];
      asm("" : "+v"(a0)); asm("" : "+v"(a1));
      o[j] = pack_bf(a0, a1);
    }
    st128_dev(dst + (t >> 2) * 4096, vo + (t & 3) * 1024, o);
  };
  const f32x16 zero = {0.f, 0.f, 0.f, 0.f, 0.f, 0.f, 0.f, 0.f, 0.f, 0.f, 0.f, 0.f, 0.f, 0.f, 0.f, 0.f};
  if (tid == 0) *(u32x4*)(lds + F1_ARR) = u32x4{0u, 0u, 0u, 0u};
  asm volatile("s_waitcnt lgkmcnt(0)" ::: "memory");
  BARRIER();
  rd1(0);
  SCHED0();
#pragma unroll
  for (int k = 0; k < 28; ++k) {
    if (k == 1 || (k & 3) == 3) {
      asm volatile("s_waitcnt lgkmcnt(0)" ::: "memory");
      BARRIER();
      SCHED0();
    }
    if (k < 27) rd1(k + 1); else rd2(0);
#pragma unroll
    for (int t = 0; t < 4; ++t) acc[t] = MFMA32(fa[k & 1][t], bk[k], k == 0 ? zero : acc[t]);
    msum = MFMA16(sel, bk[k], msum);
    __builtin_amdgcn_sched_group_barrier(0x8, 1, 0);
    __builtin_amdgcn_sched_group_barrier(0x100, 3, 0);
    __builtin_amdgcn_sched_group_barrier(0x8, 1, 0);
    __builtin_amdgcn_sched_group_barrier(0x100, 3, 0);
    __builtin_amdgcn_sched_group_barrier(0x8, 3, 0);
    SCHED0();
  }
  {
    const u32* ca = (const u32*)(lds + F1_CS) + lane;
    u32 cw[7];
#pragma unroll
    for (int t = 0; t < 7; ++t) cw[t] = ca[t * 64];
    const float m0 = msum[0], m1 = msum[1];
    const auto mr = __builtin_amdgcn_permlane16_swap(__builtin_bit_cast(u32, m0), __builtin_bit_cast(u32, m1), false, false);
    const u32 mt = mr[0];
    const float nmean = __builtin_bit_cast(float, mt) * (-1.0f / D);
    const s16x8 mo = __builtin_bit_cast(s16x8, u32x4{pack_bf(nmean, nmean), 0u, 0u, 0u});
#pragma unroll
    for (int t = 0; t < 4; ++t) acc[t] = MFMA32(__builtin_bit_cast(s16x8, u32x4{cw[t], 0u, 0u, 0u}), mo, acc[t]);
    put(0);
    SCHED0();
#pragma unroll
    for (int t = 4; t < 7; ++t) acc[t] = MFMA32(__builtin_bit_cast(s16x8, u32x4{cw[t], 0u, 0u, 0u}), mo, zero);
    SCHED0();
  }
#pragma unroll
  for (int k = 0; k < 26; ++k) {
    if (k == 15) BARRIER();
    rd2(k + 1);
#pragma unroll
    for (int t = 0; t < 3; ++t) acc[4 + t] = MFMA32(fa[k & 1][t], bk[k], acc[4 + t]);
    if (k == 0 || k == 2 || k == 4) put(1 + k / 2);
    if (k == PUB1) {
      wait_vm<0>();
      f1_publish(ws, lds_addr(lds) + F1_ARR + 8, 3u, panel * 4 + (slice >> 2));
    }
#pragma unroll
    for (int i = 0; i < 3; ++i) {
      __builtin_amdgcn_sched_group_barrier(0x8, 1, 0);
      __builtin_amdgcn_sched_group_barrier(0x100, 1, 0);
      __builtin_amdgcn_sched_group_barrier(0x2, 7, 0);
    }
    SCHED0();
  }
  rd2(27);
  {
    u32 one = 1;
    asm volatile("s_mov_b64 exec, 1\n\tds_add_u32 %0, %1\n\ts_mov_b64 exec, -1" :: "v"(lds_addr(lds) + F1_ARR + 4), "v"(one) : "memory");
  }
  SCHED0();
  acc[4] = MFMA32(fa[0][0], bk[26], acc[4]); acc[4] = MFMA32(fa[1][0], bk[27], acc[4]);
  acc[5] = MFMA32(fa[0][1], bk[26], acc[5]); acc[5] = MFMA32(fa[1][1], bk[27], acc[5]);
  SCHED0();
  acc[6] = MFMA32(fa[0][2], bk[26], acc[6]); acc[6] = MFMA32(fa[1][2], bk[27], acc[6]);
  put(4);
  SCHED0();
  put(5); put(6);
  wait_vm<0>();
  f1_publish(ws, lds_addr(lds) + F1_ARR, 7u, 64 + panel * 4 + (slice >> 2));
}

extern "C" void kernel_launch(void* const* d_in, const int* in_sizes, int n_in,
                              void* d_out, int out_size, void* d_ws, size_t ws_size,
                              hipStream_t stream) {
  const u16* x = (const u16*)d_in[0];
  char* ws = (char*)d_ws;
  k_ssm<<<256, 512, 0, stream>>>(x, (const u16*)d_in[1], (const u16*)d_in[2], (const u16*)d_in[3], (const u16*)d_in[4],
                                 (const u16*)d_in[5], (const u16*)d_in[6], (const u16*)d_in[7], (const u16*)d_in[8],
                                 (const u16*)d_in[10], (const u16*)d_in[11], (const u16*)d_in[12], ws);
  k_ffn<<<256, 512, 0, stream>>>(ws, (u16*)d_out, (const char*)d_in[12]);
}
```
